```python
import torch

def _softmax(x, axis=-1):
    return torch.softmax(x, dim=axis)
def _gelu(x):
    return torch.nn.functional.gelu(x, approximate="tanh")
def _silu(x):
    return torch.nn.functional.silu(x)
def _sigmoid(x):
    return torch.sigmoid(x)
def _layernorm(x, axis=-1, eps=1e-5):
    m = x.mean(dim=axis, keepdim=True)
    v = x.var(dim=axis, keepdim=True, unbiased=False)
    return (x - m) / torch.sqrt(v + eps)
def _scatter_add(tgt, idx, src, axis=0):
    return tgt.clone().index_add_(axis, idx, src)
def _segment_sum(src, seg, n):
    out = torch.zeros((n,) + src.shape[1:], dtype=src.dtype)
    return out.index_add_(0, seg, src)
def _pad(x, pw):
    flat = tuple(v for pair in reversed(pw) for v in pair)
    return torch.nn.functional.pad(x, flat)

kernel_name = 'standalone__mha_block_cached__near_ridge'

def setup_inputs():

    _w = (lambda a: (lambda t: t.to(torch.bfloat16) if t.is_floating_point() else t)(torch.as_tensor(a)))
    B, Sq, D = 8, 128, 512
    H, Sk, Dh = 32, 2048, 128
    return {
        "x": _w((lambda *s: torch.randn(*s, dtype=torch.bfloat16) * 0.8)(B, Sq, D)),
        "k_cache": _w((lambda *s: torch.randn(*s, dtype=torch.bfloat16) * 0.8)(B, H, Sk, Dh)),
        "v_cache": _w((lambda *s: torch.randn(*s, dtype=torch.bfloat16) * 0.05)(B, H, Sk, Dh)),
        "wq": _w((lambda *s: torch.randn(*s, dtype=torch.bfloat16) * 0.8)(D, H * Dh) * (D ** -0.5)),
        "wo": _w((lambda *s: torch.randn(*s, dtype=torch.bfloat16) * 0.05)(H * Dh, D) * ((H * Dh) ** -0.5)),
    }

def reference(x, k_cache, v_cache, wq, wo):
    B, Sq, D = x.shape
    H, Dh = 32, 128
    q = (lambda x, *d: x.permute(*d))((x @ wq).reshape(B, Sq, H, Dh), 0, 2, 1, 3)
    scores = torch.einsum("bhqd,bhkd->bhqk", q, k_cache) * (Dh ** -0.5)
    p = _softmax(scores, axis=-1)
    o = torch.einsum("bhqk,bhkd->bhqd", p, v_cache)
    return (lambda x, *d: x.permute(*d))(o, 0, 2, 1, 3).reshape(B, Sq, H * Dh) @ wo
```

```cpp
#include <hip/hip_runtime.h>
#include <hip/hip_bf16.h>
#include <stdint.h>

typedef __bf16 bf16x8 __attribute__((ext_vector_type(8)));
typedef __bf16 bf16x2 __attribute__((ext_vector_type(2)));
typedef short s16x4 __attribute__((ext_vector_type(4)));
typedef short s16x8 __attribute__((ext_vector_type(8)));
typedef float f32x16 __attribute__((ext_vector_type(16)));
typedef float f32x4 __attribute__((ext_vector_type(4)));
typedef float f32x2 __attribute__((ext_vector_type(2)));
typedef unsigned u32x4 __attribute__((ext_vector_type(4)));
typedef unsigned u32x2 __attribute__((ext_vector_type(2)));
#define LAS __attribute__((address_space(3)))
#define GAS __attribute__((address_space(1)))
#define DI __device__ __forceinline__
#define MFMA(a, b, c) __builtin_amdgcn_mfma_f32_32x32x16_bf16((a), (b), (c), 0, 0, 0)
#define RLX_AGENT __ATOMIC_RELAXED, __HIP_MEMORY_SCOPE_AGENT

constexpr int NB = 8, SQ = 128, DM = 512, NH = 32, SK = 2048, DH = 128;
constexpr int NSLOT = 5;
constexpr unsigned SLOTB = 32768u;
constexpr unsigned LDS_BYTES = NSLOT * SLOTB;
constexpr float CSC = 0.088388347648318447f * 1.4426950408889634f;
constexpr size_t WS_FLAG = 0;
constexpr size_t FLAG_BYTES = 2 * 256 * 128;
constexpr size_t WS_O = 1 << 20;
constexpr size_t WS_P = WS_O + (8u << 20);
constexpr size_t WS_END = WS_P + (16u << 20);
#define KV_NT " nt"

DI unsigned rfl(unsigned x) { return __builtin_amdgcn_readfirstlane(x); }
DI unsigned cvtpk(float lo, float hi) { f32x2 v = {lo, hi}; bf16x2 b = __builtin_convertvector(v, bf16x2); return __builtin_bit_cast(unsigned, b); }
DI bf16x8 pack8(const f32x16& a, int s) {
  u32x4 w = {cvtpk(a[8 * s], a[8 * s + 1]), cvtpk(a[8 * s + 2], a[8 * s + 3]), cvtpk(a[8 * s + 4], a[8 * s + 5]), cvtpk(a[8 * s + 6], a[8 * s + 7])};
  return __builtin_bit_cast(bf16x8, w);
}
DI bf16x8 lds_b128(unsigned a) { return *(const LAS bf16x8*)a; }
DI s16x4 lds_tr(unsigned a) { return __builtin_amdgcn_ds_read_tr16_b64_v4i16((LAS s16x4*)a); }
DI bf16x8 lds_tr2(unsigned a0, unsigned a1) {
  s16x4 lo = lds_tr(a0), hi = lds_tr(a1);
  return __builtin_bit_cast(bf16x8, __builtin_shufflevector(lo, hi, 0, 1, 2, 3, 4, 5, 6, 7));
}
DI void dma4_kv(unsigned m0v, unsigned voff, __amdgpu_buffer_rsrc_t srd, unsigned soff) {
  unsigned keep;
  asm volatile("s_mov_b32 %0, m0\n\ts_mov_b32 m0, %1\n\ts_nop 3\n\t"
               "buffer_load_dwordx4 %2, %3, %4 offen" KV_NT " lds\n\t"
               "buffer_load_dwordx4 %2, %3, %4 offen offset:1024" KV_NT " lds\n\t"
               "buffer_load_dwordx4 %2, %3, %4 offen offset:2048" KV_NT " lds\n\t"
               "buffer_load_dwordx4 %2, %3, %4 offen offset:3072" KV_NT " lds\n\t"
               "s_mov_b32 m0, %0"
               : "=&s"(keep) : "s"(m0v), "v"(voff), "s"(srd), "s"(soff) : "memory");
}
DI void dma1(unsigned m0v, unsigned voff, __amdgpu_buffer_rsrc_t srd, unsigned soff) {
  unsigned keep;
  asm volatile("s_mov_b32 %0, m0\n\ts_mov_b32 m0, %1\n\ts_nop 3\n\t"
               "buffer_load_dwordx4 %2, %3, %4 offen lds\n\t"
               "s_mov_b32 m0, %0"
               : "=&s"(keep) : "s"(m0v), "v"(voff), "s"(srd), "s"(soff) : "memory");
}
#define WAIT_BAR(N) asm volatile("s_waitcnt vmcnt(" #N ") lgkmcnt(0)\n\ts_barrier" ::: "memory")
#define BAR() asm volatile("s_waitcnt lgkmcnt(0)\n\ts_barrier" ::: "memory")

DI float xhalf_max(float v) { auto r = __builtin_amdgcn_permlane32_swap(__float_as_uint(v), __float_as_uint(v), false, false); return fmaxf(__uint_as_float(r[0]), __uint_as_float(r[1])); }
DI float xhalf_sum(float v) { auto r = __builtin_amdgcn_permlane32_swap(__float_as_uint(v), __float_as_uint(v), false, false); return __uint_as_float(r[0]) + __uint_as_float(r[1]); }

DI void wait_flags(GAS unsigned* p, bool active) {
  for (unsigned spins = 0; spins < (1u << 20); ++spins) {
    unsigned v = 1u;
    if (active) v = __hip_atomic_load(p, RLX_AGENT);
    if (__all(v != 0u)) break;
    __builtin_amdgcn_s_sleep(1);
  }
}

#define STAMP(i) do { } while (0)

__global__ __launch_bounds__(512, 2) void mha_fused(const unsigned short* __restrict__ x, const unsigned short* __restrict__ kc, const unsigned short* __restrict__ vc,
                                                    const unsigned short* __restrict__ wq, const unsigned short* __restrict__ wo, unsigned short* __restrict__ out, char* __restrict__ ws) {
  extern __shared__ __attribute__((aligned(16))) char lds[];
  const unsigned tid = threadIdx.x, lane = tid & 63u, r32 = lane & 31u, hi = lane >> 5;
  const unsigned wid = rfl(tid >> 6), g = wid >> 2, w4 = wid & 3u;
  const unsigned bid = blockIdx.x, hg = bid & 7u, b = bid >> 5, j = (bid >> 3) & 3u, h = hg * 4u + j;
  const unsigned lds0 = (unsigned)(uintptr_t)lds;
  STAMP(0);

  const __amdgpu_buffer_rsrc_t srdK = __builtin_amdgcn_make_buffer_rsrc((void*)(kc + (size_t)(b * NH + h) * SK * DH), (short)0, SK * DH * 2, 0x00020000);
  const __amdgpu_buffer_rsrc_t srdV = __builtin_amdgcn_make_buffer_rsrc((void*)(vc + (size_t)(b * NH + h) * SK * DH), (short)0, SK * DH * 2, 0x00020000);
  const __amdgpu_buffer_rsrc_t srdX = __builtin_amdgcn_make_buffer_rsrc((void*)(x + (size_t)b * SQ * DM), (short)0, SQ * DM * 2, 0x00020000);
  const __amdgpu_buffer_rsrc_t srdWq = __builtin_amdgcn_make_buffer_rsrc((void*)wq, (short)0, DM * NH * DH * 2, 0x00020000);
  const __amdgpu_buffer_rsrc_t srdWo = __builtin_amdgcn_make_buffer_rsrc((void*)(wo + (size_t)hg * 512 * DM), (short)0, 512 * DM * 2, 0x00020000);

  const unsigned l4 = lane >> 4, l15 = lane & 15u;
  const unsigned voffK = w4 * 4096u + l4 * 256u + ((l15 ^ (l4 | (w4 << 2))) << 4);
  const unsigned voffV = w4 * 4096u + l4 * 256u + ((l15 ^ (l4 << 2)) << 4);
  const unsigned voffWq = l4 * 8192u + ((l15 ^ (l4 << 2)) << 4);
  const unsigned voffWo = l4 * 1024u + ((l15 ^ (l4 << 2)) << 4);
  const unsigned voffX = (lane >> 3) * 1024u + (((lane & 7u) ^ (((lane >> 4) & 1u) | (w4 << 1))) << 4);

  auto issue_wo = [&](int cc, unsigned slot) {
    const unsigned c = (j + (unsigned)cc) & 3u, sb = lds0 + slot * SLOTB;
#pragma unroll
    for (int q = 0; q < 8; ++q) dma1(sb + (w4 * 8u + q) * 1024u, voffWo, srdWo, (c * 128u + w4 * 32u + q * 4u) * 1024u + j * 256u);
  };
  auto issue = [&](int i) {
    const unsigned sb = lds0 + (unsigned)(i % NSLOT) * SLOTB;
    if (i < 8) {
#pragma unroll
      for (int q = 0; q < 4; ++q) dma1(sb + (w4 * 4u + q) * 1024u, voffWq, srdWq, (unsigned)(i * 64 + w4 * 16 + q * 4) * 8192u + h * 256u);
#pragma unroll
      for (int q = 0; q < 4; ++q) dma1(sb + 16384u + (q * 4u + w4) * 1024u, voffX, srdX, (unsigned)(q * 4 + w4) * 8192u + (unsigned)i * 128u);
    } else if (i <= 40) {
      const int p = i - 8;
      if (p < 32) dma4_kv(sb + w4 * 4096u, voffK, srdK, (unsigned)p * 16384u);
      if (p > 0) dma4_kv(sb + 16384u + w4 * 4096u, voffV, srdV, (unsigned)(p - 1) * 16384u);
    } else {
      issue_wo(i - 41, (unsigned)(i % NSLOT));
    }
  };

  if (g == 1) { issue(0); issue(2); } else { issue(1); issue(3); }

  const unsigned blk = (lane >> 4) & 1u, q4 = l15 >> 2, p4 = lane & 3u, pp = ((p4 & 1u) << 1) | (p4 >> 1);
  const unsigned trW = (8u * hi + q4) * 256u + (q4 << 6) + blk * 32u + pp * 8u;
  const unsigned trV = (4u * hi + q4) * 256u + (q4 << 6) + blk * 32u + pp * 8u;
  const unsigned rdK = ((r32 & 7u) + 16u * (r32 >> 3)) * 256u + ((hi ^ ((r32 & 3u) | ((r32 >> 3) << 2))) << 4);
  const unsigned rdX = (w4 * 32u + r32) * 128u + ((hi ^ (((r32 >> 1) & 1u) | ((r32 >> 3) << 1))) << 4);

  bf16x8 qf[8];
  {
    f32x16 qa0 = {}, qa1 = {};
    for (int i = 0; i < 8; ++i) {
      if ((unsigned)((i + 1) & 1) == g) { if (i == 6) WAIT_BAR(4); else WAIT_BAR(8); issue(i + 4); } else { BAR(); }
      const unsigned sb = lds0 + (unsigned)(i % NSLOT) * SLOTB;
      const unsigned aw0 = (sb + trW) ^ ((2u * g) << 6), aw1 = (sb + trW) ^ ((2u * g + 1u) << 6), ax = sb + 16384u + rdX;
#pragma unroll
      for (int s = 0; s < 4; ++s) {
        const bf16x8 xb = lds_b128(ax ^ (unsigned)(s << 5));
        const bf16x8 a0 = lds_tr2(aw0 + s * 4096, aw0 + s * 4096 + 1024);
        const bf16x8 a1 = lds_tr2(aw1 + s * 4096, aw1 + s * 4096 + 1024);
        qa0 = MFMA(a0, xb, qa0);
        qa1 = MFMA(a1, xb, qa1);
      }
    }
    const unsigned xb = lds0 + (7 % NSLOT) * SLOTB;
    const bf16x8 m0_ = pack8(qa0, 0), m1_ = pack8(qa0, 1), m2_ = pack8(qa1, 0), m3_ = pack8(qa1, 1);
    BAR();
    const unsigned mine = xb + wid * 4096u + lane * 16u, other = xb + (wid ^ 4u) * 4096u + lane * 16u;
    *(LAS bf16x8*)(mine) = m0_; *(LAS bf16x8*)(mine + 1024) = m1_; *(LAS bf16x8*)(mine + 2048) = m2_; *(LAS bf16x8*)(mine + 3072) = m3_;
    BAR();
    const bf16x8 o0 = lds_b128(other), o1 = lds_b128(other + 1024), o2 = lds_b128(other + 2048), o3 = lds_b128(other + 3072);
    if (g == 0) { qf[0] = m0_; qf[1] = m1_; qf[2] = m2_; qf[3] = m3_; qf[4] = o0; qf[5] = o1; qf[6] = o2; qf[7] = o3; }
    else        { qf[0] = o0; qf[1] = o1; qf[2] = o2; qf[3] = o3; qf[4] = m0_; qf[5] = m1_; qf[6] = m2_; qf[7] = m3_; }
  }
  STAMP(1);

  f32x16 o[4] = {};
  float m_run = -__builtin_inff(), l_run = 0.f;
  if (g == 1) { WAIT_BAR(8); issue(12); }
  for (int it = 0; it < 16; ++it) {
    const int p = 2 * it + (int)g, i = p + 8;
    BAR();
    bf16x8 pf[4]; float alpha;
    {
      const unsigned ak = lds0 + (unsigned)(i % NSLOT) * SLOTB + rdK;
      f32x16 s0 = {}, s1 = {};
#pragma unroll
      for (int s = 0; s < 8; ++s) {
        const bf16x8 k0 = lds_b128(ak ^ (unsigned)(s << 5));
        const bf16x8 k1 = lds_b128((ak ^ (unsigned)(s << 5)) + 2048);
        s0 = MFMA(k0, qf[s], s0);
        s1 = MFMA(k1, qf[s], s1);
      }
      float mx = fmaxf(s0[0], s1[0]);
#pragma unroll
      for (int r = 1; r < 16; ++r) mx = fmaxf(fmaxf(mx, s0[r]), s1[r]);
      mx = xhalf_max(mx);
      const float m_new = fmaxf(m_run, mx);
      alpha = __builtin_amdgcn_exp2f((m_run - m_new) * CSC);
      m_run = m_new;
      const float nm = -m_new * CSC;
      float ps = 0.f;
#pragma unroll
      for (int r = 0; r < 16; ++r) { s0[r] = __builtin_amdgcn_exp2f(fmaf(s0[r], CSC, nm)); ps += s0[r]; }
#pragma unroll
      for (int r = 0; r < 16; ++r) { s1[r] = __builtin_amdgcn_exp2f(fmaf(s1[r], CSC, nm)); ps += s1[r]; }
      l_run = l_run * alpha + ps;
      pf[0] = pack8(s0, 0); pf[1] = pack8(s0, 1); pf[2] = pack8(s1, 0); pf[3] = pack8(s1, 1);
    }
    if (it == 15) {
      l_run = xhalf_sum(l_run);
      if (hi == 0) *(LAS f32x2*)(lds0 + wid * 256u + r32 * 8u) = (f32x2){m_run, l_run};
    }
    if (i + 1 == 38) WAIT_BAR(4); else WAIT_BAR(8);
    if (i + 5 <= 42) issue(i + 5);
    {
      const unsigned av = lds0 + (unsigned)((i + 1) % NSLOT) * SLOTB + 16384u + trV;
      if (__any(alpha != 1.f)) {
#pragma unroll
        for (int d = 0; d < 4; ++d)
#pragma unroll
          for (int r = 0; r < 16; ++r) o[d][r] *= alpha;
      }
#pragma unroll
      for (int kb = 0; kb < 2; ++kb)
#pragma unroll
        for (int s = 0; s < 2; ++s)
#pragma unroll
          for (int d = 0; d < 4; ++d) {
            const unsigned a = (av ^ (unsigned)(d << 6)) + (32 * s + 8 * kb) * 256;
            const bf16x8 vf = lds_tr2(a, a + 4096);
            o[d] = MFMA(vf, pf[2 * kb + s], o[d]);
          }
    }
  }
  STAMP(2);
  if (g == 0) BAR();
  const __amdgpu_buffer_rsrc_t srdO = __builtin_amdgcn_make_buffer_rsrc((void*)(ws + WS_O + (size_t)(b * NH + hg * 4u) * 32768u), (short)0, 4 * 32768, 0x00020000);
  {
    const f32x2 st = *(const LAS f32x2*)(lds0 + (wid ^ 4u) * 256u + r32 * 8u);
    const float ms = fmaxf(m_run, st[0]);
    const float e_me = __builtin_amdgcn_exp2f((m_run - ms) * CSC), e_ot = __builtin_amdgcn_exp2f((st[0] - ms) * CSC);
    const float sc = e_me / (l_run * e_me + st[1] * e_ot);
    const unsigned xa = lds0 + 3u * SLOTB + w4 * 16384u + lane * 16u;
    if (g == 0) {
#pragma unroll
      for (int d = 0; d < 4; ++d)
#pragma unroll
        for (int q = 0; q < 4; ++q)
          *(LAS f32x4*)(xa + (d * 4 + q) * 1024) = (f32x4){o[d][4 * q] * sc, o[d][4 * q + 1] * sc, o[d][4 * q + 2] * sc, o[d][4 * q + 3] * sc};
      WAIT_BAR(0);
      issue_wo(2, 0u);
      BAR();
      issue_wo(3, 3u);
      WAIT_BAR(0);
    } else {
      BAR();
#pragma unroll
      for (int d = 0; d < 4; ++d) {
#pragma unroll
        for (int q = 0; q < 4; ++q) {
          const f32x4 v = *(const LAS f32x4*)(xa + (d * 4 + q) * 1024);
#pragma unroll
          for (int e = 0; e < 4; ++e) o[d][4 * q + e] = fmaf(o[d][4 * q + e], sc, v[e]);
        }
#pragma unroll
        for (int s = 0; s < 2; ++s)
          __builtin_amdgcn_raw_buffer_store_b128(__builtin_bit_cast(u32x4, pack8(o[d], s)), srdO, j * 32768u + w4 * 8192u + (unsigned)(2 * d + s) * 1024u + lane * 16u, 0, 16);
      }
      WAIT_BAR(0);
      STAMP(3);
      if (wid == 4) {
        GAS unsigned* fl = (GAS unsigned*)(ws + WS_FLAG) + (size_t)(b * NH + hg * 4u) * 32u;
        if (lane == 0) __hip_atomic_store(fl + j * 32u, 1u, RLX_AGENT);
        wait_flags(fl + (lane & 3u) * 32u, lane < 4u);
      }
      STAMP(4);
      BAR();
    }
  }
  {
    f32x16 c0 = {}, c1 = {};
    u32x4 of[4][8];
#pragma unroll
    for (int cc = 0; cc < 4; ++cc)
#pragma unroll
      for (int s = 0; s < 8; ++s)
        of[cc][s] = __builtin_amdgcn_raw_buffer_load_b128(srdO, w4 * 8192u + (unsigned)s * 1024u + lane * 16u, ((j + (unsigned)cc) & 3u) * 32768u, 16);
#pragma unroll
    for (int cc = 0; cc < 4; ++cc) {
      const unsigned sb = lds0 + (cc == 0 ? 1u : cc == 1 ? 2u : cc == 2 ? 0u : 3u) * SLOTB;
      const unsigned aw0 = (sb + trW) ^ ((2u * g) << 6), aw1 = (sb + trW) ^ ((2u * g + 1u) << 6);
#pragma unroll
      for (int s = 0; s < 8; ++s) {
        const bf16x8 a0 = lds_tr2(aw0 + s * 4096, aw0 + s * 4096 + 1024);
        const bf16x8 a1 = lds_tr2(aw1 + s * 4096, aw1 + s * 4096 + 1024);
        c0 = MFMA(a0, __builtin_bit_cast(bf16x8, of[cc][s]), c0);
        c1 = MFMA(a1, __builtin_bit_cast(bf16x8, of[cc][s]), c1);
      }
    }
    STAMP(5);
    const __amdgpu_buffer_rsrc_t srdP = __builtin_amdgcn_make_buffer_rsrc((void*)(ws + WS_P + (size_t)(b * 4u + j) * 524288u), (short)0, 524288, 0x00020000);
#pragma unroll
    for (int q = 0; q < 4; ++q) {
      __builtin_amdgcn_raw_buffer_store_b128(__builtin_bit_cast(u32x4, (f32x4){c0[4 * q], c0[4 * q + 1], c0[4 * q + 2], c0[4 * q + 3]}), srdP, hg * 65536u + wid * 8192u + (unsigned)q * 1024u + lane * 16u, 0, 16);
      __builtin_amdgcn_raw_buffer_store_b128(__builtin_bit_cast(u32x4, (f32x4){c1[4 * q], c1[4 * q + 1], c1[4 * q + 2], c1[4 * q + 3]}), srdP, hg * 65536u + wid * 8192u + (unsigned)(4 + q) * 1024u + lane * 16u, 0, 16);
    }
    WAIT_BAR(0);
    STAMP(6);
    if (wid == 4) {
      GAS unsigned* fl = (GAS unsigned*)(ws + WS_FLAG) + (size_t)(256u + (b * 4u + j) * 8u) * 32u;
      if (lane == 0) __hip_atomic_store(fl + hg * 32u, 1u, RLX_AGENT);
      wait_flags(fl + (lane & 7u) * 32u, lane < 8u);
    }
    STAMP(7);
    BAR();
    f32x4 pv[8];
#pragma unroll
    for (int u = 0; u < 8; ++u)
      pv[u] = __builtin_bit_cast(f32x4, __builtin_amdgcn_raw_buffer_load_b128(srdP, hg * 8192u + wid * 1024u + lane * 16u, (unsigned)u * 65536u, 16));
    f32x4 acc = pv[0];
#pragma unroll
    for (int u = 1; u < 8; ++u) acc += pv[u];
    const unsigned qrow = (hg & 3u) * 32u + r32, n = j * 128u + ((hg >> 2) * 2u + g) * 32u + (w4 >> 1) * 16u + hi * 8u + (w4 & 1u) * 4u;
    *(u32x2*)(out + ((size_t)b * SQ + qrow) * DM + n) = (u32x2){cvtpk(acc[0], acc[1]), cvtpk(acc[2], acc[3])};
    STAMP(8);
  }
}

extern "C" void kernel_launch(void* const* d_in, const int* in_sizes, int n_in, void* d_out, int out_size, void* d_ws, size_t ws_size, hipStream_t stream) {
  (void)in_sizes; (void)n_in; (void)out_size; (void)ws_size;
  (void)hipFuncSetAttribute((const void*)mha_fused, hipFuncAttributeMaxDynamicSharedMemorySize, (int)LDS_BYTES);
  (void)hipMemsetAsync((char*)d_ws + WS_FLAG, 0, FLAG_BYTES, stream);
  hipLaunchKernelGGL(mha_fused, dim3(256), dim3(512), LDS_BYTES, stream, (const unsigned short*)d_in[0], (const unsigned short*)d_in[1], (const unsigned short*)d_in[2],
                     (const unsigned short*)d_in[3], (const unsigned short*)d_in[4], (unsigned short*)d_out, (char*)d_ws);
}
```

```cpp
#include <hip/hip_runtime.h>
#include <hip/hip_bf16.h>
#include <stdint.h>
#include <type_traits>

typedef __bf16 bf16x8 __attribute__((ext_vector_type(8)));
typedef __bf16 bf16x2 __attribute__((ext_vector_type(2)));
typedef short s16x4 __attribute__((ext_vector_type(4)));
typedef float f32x16 __attribute__((ext_vector_type(16)));
typedef float f32x4 __attribute__((ext_vector_type(4)));
typedef float f32x2 __attribute__((ext_vector_type(2)));
typedef unsigned u32x4 __attribute__((ext_vector_type(4)));
typedef unsigned u32x2 __attribute__((ext_vector_type(2)));
#define LAS __attribute__((address_space(3)))
#define GAS __attribute__((address_space(1)))
#define DI __device__ __forceinline__
#define MFMA(a, b, c) __builtin_amdgcn_mfma_f32_32x32x16_bf16((a), (b), (c), 0, 0, 0)
#define RLX_AGENT __ATOMIC_RELAXED, __HIP_MEMORY_SCOPE_AGENT
#define PIN(x) asm volatile("" : "+v"(x))
#define SBAR() __builtin_amdgcn_sched_barrier(0)
#define PRIO(n) __builtin_amdgcn_s_setprio(n)

constexpr int SQ = 128, DM = 512, NH = 32, SK = 2048, DH = 128;
constexpr unsigned U = 16384u, SL = 32768u;
constexpr unsigned LDS_BYTES = 5 * SL;
#define PVPRIO 0
#define SWIT0 3
#define PSLIM 8192.0f
constexpr float CSC = 0.088388347648318447f * 1.4426950408889634f;
constexpr size_t WS_FLAG = 0;
constexpr size_t FLAG_BYTES = 256 * 128;
constexpr size_t WS_O = 1 << 20;
constexpr size_t WS_P = WS_O + (8u << 20);
constexpr size_t WS_END = WS_P + (16u << 20);

DI unsigned rfl(unsigned x) { return __builtin_amdgcn_readfirstlane(x); }
DI unsigned cvtpk(float lo, float hi) { f32x2 v = {lo, hi}; bf16x2 b = __builtin_convertvector(v, bf16x2); return __builtin_bit_cast(unsigned, b); }
DI bf16x8 pack8(const f32x16& a, int s) {
  u32x4 w = {cvtpk(a[8 * s], a[8 * s + 1]), cvtpk(a[8 * s + 2], a[8 * s + 3]), cvtpk(a[8 * s + 4], a[8 * s + 5]), cvtpk(a[8 * s + 6], a[8 * s + 7])};
  return __builtin_bit_cast(bf16x8, w);
}
DI bf16x8 lds_b128(unsigned a) { return *(const LAS bf16x8*)a; }
DI f32x4 lds_f4(unsigned a) { return *(const LAS f32x4*)a; }
DI void lds_st4(unsigned a, const f32x16& v, int q) { *(LAS f32x4*)a = (f32x4){v[4 * q], v[4 * q + 1], v[4 * q + 2], v[4 * q + 3]}; }
DI s16x4 lds_tr(unsigned a) { return __builtin_amdgcn_ds_read_tr16_b64_v4i16((LAS s16x4*)a); }
DI bf16x8 lds_tr2(unsigned a0, unsigned a1) {
  s16x4 lo = lds_tr(a0), hi = lds_tr(a1);
  return __builtin_bit_cast(bf16x8, __builtin_shufflevector(lo, hi, 0, 1, 2, 3, 4, 5, 6, 7));
}
DI void dma4_kv(unsigned m0v, unsigned voff, __amdgpu_buffer_rsrc_t srd, unsigned soff) {
  unsigned keep;
  asm volatile("s_mov_b32 %0, m0\n\ts_mov_b32 m0, %1\n\ts_nop 3\n\t"
               "buffer_load_dwordx4 %2, %3, %4 offen nt lds\n\t"
               "buffer_load_dwordx4 %2, %3, %4 offen offset:1024 nt lds\n\t"
               "buffer_load_dwordx4 %2, %3, %4 offen offset:2048 nt lds\n\t"
               "buffer_load_dwordx4 %2, %3, %4 offen offset:3072 nt lds\n\t"
               "s_mov_b32 m0, %0"
               : "=&s"(keep) : "s"(rfl(m0v)), "v"(voff), "s"(srd), "s"(rfl(soff)) : "memory");
}
DI void dma1(unsigned m0v, unsigned voff, __amdgpu_buffer_rsrc_t srd, unsigned soff) {
  unsigned keep;
  asm volatile("s_mov_b32 %0, m0\n\ts_mov_b32 m0, %1\n\ts_nop 3\n\t"
               "buffer_load_dwordx4 %2, %3, %4 offen lds\n\t"
               "s_mov_b32 m0, %0"
               : "=&s"(keep) : "s"(rfl(m0v)), "v"(voff), "s"(srd), "s"(rfl(soff)) : "memory");
}
DI void dma1nt(unsigned m0v, unsigned voff, __amdgpu_buffer_rsrc_t srd, unsigned soff) {
  unsigned keep;
  asm volatile("s_mov_b32 %0, m0\n\ts_mov_b32 m0, %1\n\ts_nop 3\n\t"
               "buffer_load_dwordx4 %2, %3, %4 offen nt lds\n\t"
               "s_mov_b32 m0, %0"
               : "=&s"(keep) : "s"(rfl(m0v)), "v"(voff), "s"(srd), "s"(rfl(soff)) : "memory");
}
#define DMAWQ dma1nt
#define DMAX dma1
#define WAIT_BAR(N) asm volatile("s_waitcnt vmcnt(" #N ") lgkmcnt(0)\n\ts_barrier" ::: "memory")
#define BAR() asm volatile("s_waitcnt lgkmcnt(0)\n\ts_barrier" ::: "memory")
#define WAITVM(N) asm volatile("s_waitcnt vmcnt(" #N ")" ::: "memory")

DI float xhalf_max(float v) { auto r = __builtin_amdgcn_permlane32_swap(__float_as_uint(v), __float_as_uint(v), false, false); return fmaxf(__uint_as_float(r[0]), __uint_as_float(r[1])); }
DI float xhalf_sum(float v) { auto r = __builtin_amdgcn_permlane32_swap(__float_as_uint(v), __float_as_uint(v), false, false); return __uint_as_float(r[0]) + __uint_as_float(r[1]); }

DI void wait_flags(GAS unsigned* p, bool active) {
  for (unsigned spins = 0; spins < (1u << 20); ++spins) {
    unsigned v = 1u;
    if (active) v = __hip_atomic_load(p, RLX_AGENT);
    if (__all(v != 0u)) break;
    __builtin_amdgcn_s_sleep(1);
  }
  asm volatile("" ::: "memory");
}

#define STAMP(i) do { } while (0)

__global__ __launch_bounds__(512, 2) void mha_fused(const unsigned short* __restrict__ x, const unsigned short* __restrict__ kc, const unsigned short* __restrict__ vc,
                                                    const unsigned short* __restrict__ wq, const unsigned short* __restrict__ wo, unsigned short* __restrict__ out, char* __restrict__ ws) {
  extern __shared__ __attribute__((aligned(16))) char lds[];
  const unsigned tid = threadIdx.x, lane = tid & 63u, r32 = lane & 31u, hi = lane >> 5;
  const unsigned wid = rfl(tid >> 6), g = wid >> 2, w4 = wid & 3u;
  const unsigned bid = blockIdx.x, hg = bid & 7u, b = bid >> 5, j = (bid >> 3) & 3u, h = hg * 4u + j;
  const unsigned lds0 = (unsigned)(uintptr_t)lds;
  STAMP(0);
  asm volatile("" :: "s"(out), "s"(ws), "s"(wo));

  const __amdgpu_buffer_rsrc_t srdK = __builtin_amdgcn_make_buffer_rsrc((void*)(kc + (size_t)(b * NH + h) * SK * DH), (short)0, SK * DH * 2, 0x00020000);
  const __amdgpu_buffer_rsrc_t srdV = __builtin_amdgcn_make_buffer_rsrc((void*)(vc + (size_t)(b * NH + h) * SK * DH), (short)0, SK * DH * 2, 0x00020000);
  const __amdgpu_buffer_rsrc_t srdX = __builtin_amdgcn_make_buffer_rsrc((void*)(x + (size_t)b * SQ * DM), (short)0, SQ * DM * 2, 0x00020000);
  const __amdgpu_buffer_rsrc_t srdWq = __builtin_amdgcn_make_buffer_rsrc((void*)wq, (short)0, DM * NH * DH * 2, 0x00020000);
  const __amdgpu_buffer_rsrc_t srdWo = __builtin_amdgcn_make_buffer_rsrc((void*)(wo + (size_t)hg * 512 * DM), (short)0, 512 * DM * 2, 0x00020000);

  const unsigned l4 = lane >> 4, l15 = lane & 15u;
  const unsigned voffK = w4 * 4096u + l4 * 256u + ((l15 ^ (l4 | (w4 << 2))) << 4);
  const unsigned voffV = w4 * 4096u + l4 * 256u + ((l15 ^ (l4 << 2)) << 4);
  const unsigned voffWq = l4 * 8192u + ((l15 ^ (l4 << 2)) << 4);
  const unsigned voffWo = l4 * 1024u + ((l15 ^ (l4 << 2)) << 4);
  const unsigned voffX = (lane >> 3) * 1024u + (((lane & 7u) ^ (((lane >> 4) & 1u) | (w4 << 1))) << 4);

  auto issueK = [&](unsigned t, unsigned off) { dma4_kv(lds0 + off + w4 * 4096u, voffK, srdK, t * U); };
  auto issueV = [&](unsigned t, unsigned off) { dma4_kv(lds0 + off + w4 * 4096u, voffV, srdV, t * U); };
  auto issueWo = [&](unsigned cc, unsigned slot) {
    const unsigned c = (j + cc) & 3u;
#pragma unroll
    for (int q = 0; q < 8; ++q) dma1(lds0 + slot * SL + (w4 * 8u + q) * 1024u, voffWo, srdWo, (c * 128u + w4 * 32u + q * 4u) * 1024u + j * 256u);
  };
#define ROT ((b + 2u * j) & 7u)
  const unsigned rot = ROT;
  auto qslot = [](int c) { return (c % 3 == 0) ? 3u : (c % 3 == 1) ? 4u : 2u; };
  auto issueQP = [&](int c) {
    const unsigned kcn = ((unsigned)c + rot) & 7u, sb = lds0 + qslot(c) * SL;
#pragma unroll
    for (int q = 0; q < 4; ++q) {
      DMAWQ(sb + (w4 * 4u + q) * 1024u, voffWq, srdWq, (kcn * 64u + w4 * 16u + q * 4u) * 8192u + h * 256u);
      DMAX(sb + U + (q * 4u + w4) * 1024u, voffX, srdX, (q * 4u + w4) * 8192u + kcn * 128u);
    }
  };
  const bool typeA = false;

  const unsigned blk = (lane >> 4) & 1u, q4 = l15 >> 2, p4 = lane & 3u, pp = ((p4 & 1u) << 1) | (p4 >> 1);
  const unsigned trV = lds0 + U + (4u * hi + q4) * 256u + ((q4 ^ (2u * g)) << 6) + blk * 32u + pp * 8u;
  const unsigned rdK = lds0 + ((r32 & 7u) + 16u * (r32 >> 3)) * 256u + ((hi ^ ((r32 & 3u) | ((r32 >> 3) << 2))) << 4);
  const unsigned qx = lds0 + 3u * SL + w4 * 4096u + lane * 16u;

  bf16x8 qf[8], kf[16];
  auto rdk = [&](unsigned off, int i) { return lds_b128(((rdK + off) ^ (unsigned)((i & 7) << 5)) + (i >> 3) * 2048); };
  if (g == 0) {
    const unsigned trW = lds0 + (8u * hi + q4) * 256u + (q4 << 6) + blk * 32u + pp * 8u;
    const unsigned rdX = lds0 + U + (w4 * 32u + r32) * 128u + ((hi ^ (((r32 >> 1) & 1u) | ((r32 >> 3) << 1))) << 4);
    f32x16 qa[4] = {};
    bf16x8 xb[2][4], wa_[2][16];
    auto rdq = [&](int c, int s) {
      const unsigned u0 = qslot(c) * SL;
      xb[c & 1][s] = lds_b128((rdX + u0) ^ (unsigned)(s << 5));
#pragma unroll
      for (int d = 0; d < 4; ++d) { const unsigned a = ((trW + u0) ^ (unsigned)(d << 6)) + s * 4096; wa_[c & 1][4 * s + d] = lds_tr2(a, a + 1024); }
    };
    BAR();
#pragma unroll
    for (int s = 0; s < 4; ++s) rdq(0, s);
#pragma unroll
    for (int c = 0; c < 8; ++c) {
      if (c < 7) BAR();
#pragma unroll
      for (int s = 0; s < 4; ++s)
#pragma unroll
        for (int d = 0; d < 4; ++d) {
          SBAR();
          qa[d] = MFMA(wa_[c & 1][4 * s + d], xb[c & 1][s], qa[d]);
          SBAR();
          if (c < 7) {
            const unsigned u0 = qslot(c + 1) * SL, a = ((trW + u0) ^ (unsigned)(d << 6)) + s * 4096;
            if (d == 0) xb[(c + 1) & 1][s] = lds_b128((rdX + u0) ^ (unsigned)(s << 5));
            wa_[(c + 1) & 1][4 * s + d] = lds_tr2(a, a + 1024);
          }
        }
      SBAR();
    }
#pragma unroll
    for (int d = 0; d < 4; ++d) {
#pragma unroll
      for (int r = 0; r < 16; ++r) qa[d][r] *= CSC;
      qf[2 * d] = pack8(qa[d], 0); qf[2 * d + 1] = pack8(qa[d], 1);
    }
#pragma unroll
    for (int f = 0; f < 8; ++f) *(LAS bf16x8*)(qx + (f >> 2) * U + (f & 3) * 1024) = qf[f];
    BAR();
#pragma unroll
    for (int i = 0; i < 16; ++i) kf[i] = rdk(U, i);
  } else {
    issueQP(0); issueQP(1);
    WAIT_BAR(8); issueQP(2);
#pragma unroll
    for (int c = 0; c < 5; ++c) { WAIT_BAR(8); issueQP(c + 3); }
    issueK(0, U); issueK(1, 0);
    WAIT_BAR(16);
    WAIT_BAR(8);
    issueK(2, SL); issueV(0, SL + U);
    WAIT_BAR(8);
#pragma unroll
    for (int f = 0; f < 8; ++f) qf[f] = lds_b128(qx + (f >> 2) * U + (f & 3) * 1024);
#pragma unroll
    for (int i = 0; i < 16; ++i) kf[i] = rdk(0, i);
    issueK(3, 2u * SL); issueV(1, 2u * SL + U);
    WAIT_BAR(8);
  }
  STAMP(1);

  f32x16 o[4] = {};
  float m_run = 0.f, l_run = 0.f;
  f32x16 negm = {};
  bf16x8 pf[4], vf[16];
  auto vfrag = [&](unsigned av, int ks, int d) { const unsigned a = (av ^ (unsigned)(d << 6)) + (32 * (ks & 1) + 8 * (ks >> 1)) * 256; return lds_tr2(a, a + 4096); };
  auto scores_slow = [&](bool first, float& ps) {
    f32x16 s0 = {}, s1 = {};
#pragma unroll
    for (int s = 0; s < 8; ++s) { s0 = MFMA(kf[s], qf[s], s0); s1 = MFMA(kf[8 + s], qf[s], s1); }
    float mx = fmaxf(s0[0], s1[0]);
#pragma unroll
    for (int r = 1; r < 16; ++r) mx = fmaxf(fmaxf(mx, s0[r]), s1[r]);
    mx = xhalf_max(mx);
    const float m_new = first ? mx : fmaxf(mx, m_run);
    if (!first) {
      const float alpha = __builtin_amdgcn_exp2f(m_run - m_new);
      l_run *= alpha;
#pragma unroll
      for (int d = 0; d < 4; ++d)
#pragma unroll
        for (int r = 0; r < 16; ++r) o[d][r] *= alpha;
    }
    m_run = m_new;
    ps = 0.f;
#pragma unroll
    for (int r = 0; r < 16; ++r) { negm[r] = -m_new; s0[r] = __builtin_amdgcn_exp2f(s0[r] - m_new); s1[r] = __builtin_amdgcn_exp2f(s1[r] - m_new); ps += s0[r] + s1[r]; }
    pf[0] = pack8(s0, 0); pf[1] = pack8(s0, 1); pf[2] = pack8(s1, 0); pf[3] = pack8(s1, 1);
  };
  auto scores = [&](bool first, auto inloop) {
    float ps = 0.f;
    PRIO(2);
    if (!first) {
      SBAR();
      f32x16 s0 = MFMA(kf[0], qf[0], negm);
#pragma unroll
      for (int s = 1; s < 8; ++s) s0 = MFMA(kf[s], qf[s], s0);
      SBAR();
      f32x16 s1 = MFMA(kf[8], qf[0], negm);
      SBAR();
      float pa = 0.f, pb = 0.f;
      unsigned w[16];
#define SMSTEP(S, e, wi) do { S[e] = __builtin_amdgcn_exp2f(S[e]); S[e + 1] = __builtin_amdgcn_exp2f(S[e + 1]); pa += S[e]; pb += S[e + 1]; w[wi] = cvtpk(S[e], S[e + 1]); PIN(pa); PIN(pb); PIN(w[wi]); } while (0)
#pragma unroll
      for (int s = 1; s < 8; ++s) {
        s1 = MFMA(kf[8 + s], qf[s], s1);
        SMSTEP(s0, 2 * (s - 1), s - 1);
        SBAR();
      }
      SMSTEP(s0, 14, 7);
      SBAR();
#pragma unroll
      for (int i = 0; i < 8; ++i) { SMSTEP(s1, 2 * i, 8 + i); SBAR(); }
#undef SMSTEP
      ps = pa + pb;
#pragma unroll
      for (int i = 0; i < 4; ++i) pf[i] = __builtin_bit_cast(bf16x8, (u32x4){w[4 * i], w[4 * i + 1], w[4 * i + 2], w[4 * i + 3]});
    }
    if (__builtin_expect(first || __any(!(ps <= PSLIM)), 0)) scores_slow(first, ps);
    l_run += ps;
    PIN(pf[0]); PIN(pf[1]); PIN(pf[2]); PIN(pf[3]); PIN(l_run); if (decltype(inloop)::value) PIN(negm);
    PRIO(PVPRIO);
  };

  __builtin_amdgcn_s_waitcnt(0xC07F);
  unsigned sp = 1u + g;
  unsigned dist = typeA ? 4u : 2u;
  const int swit = SWIT0 + (int)b;
  for (int it = 0; it < 15; ++it) {
    const unsigned p = 2u * (unsigned)it + g + 1u;
    scores(it == 0, std::true_type{});
    if (dist == 4u) WAIT_BAR(8); else WAIT_BAR(0);
    bool go = true;
    if (dist == 4u && it == swit) { dist = 2u; go = false; }
    const unsigned ni = p + dist, ns = (sp + dist >= 5u ? sp + dist - 5u : sp + dist) * SL;
    const unsigned av = trV + sp * SL;
#pragma unroll
    for (int i = 0; i < 16; ++i) vf[i] = vfrag(av, i >> 2, i & 3);
    SBAR();
    if (go && ni <= 30u) issueK(ni + 1u, ns);
    SBAR();
#pragma unroll
    for (int i = 0; i < 8; ++i) kf[i] = rdk(sp * SL, i);
    SBAR();
#pragma unroll
    for (int i = 0; i < 8; ++i) o[i & 3] = MFMA(vf[i], pf[i >> 2], o[i & 3]);
    SBAR();
#pragma unroll
    for (int i = 8; i < 16; ++i) { o[i & 3] = MFMA(vf[i], pf[i >> 2], o[i & 3]); if (i < 12) { kf[2 * i - 8] = rdk(sp * SL, 2 * i - 8); kf[2 * i - 7] = rdk(sp * SL, 2 * i - 7); } SBAR(); }
    if (go) issueV(ni - 1u, ns + U);
    if (it == 14) issueWo(g, 3u + g);
    sp = sp >= 3u ? sp - 3u : sp + 2u;
    BAR();
  }
  scores(false, std::false_type{});
  unsigned ln; asm volatile("v_mbcnt_lo_u32_b32 %0, -1, 0\n\tv_mbcnt_hi_u32_b32 %0, -1, %0" : "=v"(ln));
  const unsigned x0 = lds0 + w4 * 8192u + ln * 16u;
  l_run = xhalf_sum(l_run);
  const unsigned stats = lds0 + 2u * SL + (ln & 31u) * 8u;
  if (ln < 32u) *(LAS f32x2*)(stats + wid * 256u) = (f32x2){m_run, l_run};
  bf16x8 of0[4];
  {
    WAIT_BAR(8);
    STAMP(2);
    const unsigned av = trV + (1u + g) * SL;
#pragma unroll
    for (int ks = 0; ks < 4; ++ks) { vf[4 * ks + 2] = vfrag(av, ks, 2); vf[4 * ks + 3] = vfrag(av, ks, 3); }
    SBAR();
#pragma unroll
    for (int ks = 0; ks < 4; ++ks) { vf[4 * ks] = vfrag(av, ks, 0); vf[4 * ks + 1] = vfrag(av, ks, 1); }
    SBAR();
#pragma unroll
    for (int ks = 0; ks < 4; ++ks) { o[2] = MFMA(vf[4 * ks + 2], pf[ks], o[2]); o[3] = MFMA(vf[4 * ks + 3], pf[ks], o[3]); }
    SBAR();
#pragma unroll
    for (int ks = 0; ks < 2; ++ks) { o[0] = MFMA(vf[4 * ks], pf[ks], o[0]); o[1] = MFMA(vf[4 * ks + 1], pf[ks], o[1]); }
    SBAR();
    const unsigned xo = x0 + g * SL, xi = x0 + (1u - g) * SL;
#pragma unroll
    for (int q = 0; q < 4; ++q) { lds_st4(xo + q * 1024, o[2], q); lds_st4(xo + 4096 + q * 1024, o[3], q); }
    SBAR();
#pragma unroll
    for (int ks = 2; ks < 4; ++ks) { o[0] = MFMA(vf[4 * ks], pf[ks], o[0]); o[1] = MFMA(vf[4 * ks + 1], pf[ks], o[1]); }
    SBAR();
    if (g == 0) BAR();
    const f32x2 st = *(const LAS f32x2*)(stats + (wid ^ 4u) * 256u);
    const float ms = fmaxf(m_run, st[0]);
    const float e_me = __builtin_amdgcn_exp2f(m_run - ms), e_ot = __builtin_amdgcn_exp2f(st[0] - ms);
    const float inv = 1.f / (l_run * e_me + st[1] * e_ot);
    float a_me = e_me * inv, a_ot = e_ot * inv;
    PIN(a_me); PIN(a_ot);
    WAIT_BAR(0);
    f32x4 w0[4], w1[4];
#pragma unroll
    for (int q = 0; q < 4; ++q) { w0[q] = lds_f4(xi + q * 1024); w1[q] = lds_f4(xi + 4096 + q * 1024); }
#pragma unroll
    for (int q = 0; q < 4; ++q)
#pragma unroll
      for (int e = 0; e < 4; ++e) { o[0][4 * q + e] = fmaf(w0[q][e], a_ot, o[0][4 * q + e] * a_me); o[1][4 * q + e] = fmaf(w1[q][e], a_ot, o[1][4 * q + e] * a_me); }
    of0[0] = pack8(o[0], 0); of0[1] = pack8(o[0], 1); of0[2] = pack8(o[1], 0); of0[3] = pack8(o[1], 1);
  }
  PRIO(0);
  const __amdgpu_buffer_rsrc_t srdO = __builtin_amdgcn_make_buffer_rsrc((void*)(ws + WS_O + (size_t)(b * NH + hg * 4u) * 32768u), (short)0, 4 * 32768, 0x00020000);
#pragma unroll
  for (int s = 0; s < 4; ++s) __builtin_amdgcn_raw_buffer_store_b128(__builtin_bit_cast(u32x4, of0[s]), srdO, j * 32768u + wid * 4096u + (unsigned)s * 1024u + ln * 16u, 0, 16);
  STAMP(3);
  issueWo(2u + g, 1u - g);
  f32x16 c[4] = {};
  const unsigned trW2 = trV - U + (ln >> 5) * 1024u;
  bf16x8 wa[2][8];
  auto rdw = [&](int buf, unsigned slot, int half) {
    const unsigned aw = trW2 + slot * SL + g * U;
#pragma unroll
    for (int s = 0; s < 2; ++s)
#pragma unroll
      for (int nb = 0; nb < 4; ++nb) { const unsigned a = (aw ^ (unsigned)(nb << 6)) + (2 * half + s) * 4096; wa[buf][4 * s + nb] = lds_tr2(a, a + 1024); }
  };
  auto mmw = [&](int buf, const bf16x8& f0, const bf16x8& f1) {
#pragma unroll
    for (int nb = 0; nb < 4; ++nb) c[nb] = MFMA(wa[buf][nb], f0, c[nb]);
#pragma unroll
    for (int nb = 0; nb < 4; ++nb) c[nb] = MFMA(wa[buf][4 + nb], f1, c[nb]);
  };
  rdw(0, 3, 0); rdw(1, 3, 1); SBAR();
  mmw(0, of0[0], of0[1]); SBAR();
  rdw(0, 4, 0); SBAR();
  mmw(1, of0[2], of0[3]); SBAR();
  rdw(1, 4, 1);
  PIN(c[0]); PIN(c[1]); PIN(c[2]); PIN(c[3]);
  WAIT_BAR(0);
  STAMP(4);
  GAS unsigned* flags = (GAS unsigned*)(ws + WS_FLAG);
  if (wid == 0) {
    if (ln < 4u) __hip_atomic_store(flags + (size_t)((bid & ~24u) | (ln << 3)) * 32u + j, 1u, RLX_AGENT);
    wait_flags(flags + (size_t)bid * 32u + (ln & 3u), ln < 4u);
  }
  BAR();
  STAMP(5);
  {
    bf16x8 of[3][4];
#pragma unroll
    for (int cc = 1; cc < 4; ++cc)
#pragma unroll
      for (int s = 0; s < 4; ++s)
        of[cc - 1][s] = __builtin_bit_cast(bf16x8, __builtin_amdgcn_raw_buffer_load_b128(srdO, wid * 4096u + (unsigned)s * 1024u + ln * 16u, ((j + (unsigned)cc) & 3u) * 32768u, 16));
    SBAR();
    mmw(0, of[0][0], of[0][1]); SBAR(); rdw(0, 1, 0); SBAR();
    mmw(1, of[0][2], of[0][3]); SBAR(); rdw(1, 1, 1); SBAR();
    mmw(0, of[1][0], of[1][1]); SBAR(); rdw(0, 0, 0); SBAR();
    mmw(1, of[1][2], of[1][3]); SBAR(); rdw(1, 0, 1); SBAR();
    mmw(0, of[2][0], of[2][1]); SBAR();
    mmw(1, of[2][2], of[2][3]);
  }
  STAMP(6);
  {
#pragma unroll
    for (int q = 0; q < 4; ++q) { lds_st4(x0 + (2u + g) * SL + q * 1024, c[2], q); lds_st4(x0 + (2u + g) * SL + 4096 + q * 1024, c[3], q); }
    BAR();
    const unsigned ra = x0 + (3u - g) * SL;
    const __amdgpu_buffer_rsrc_t srdP = __builtin_amdgcn_make_buffer_rsrc((void*)(ws + WS_P + (size_t)(b * 4u + j) * 524288u), (short)0, 524288, 0x00020000);
#pragma unroll
    for (int q = 0; q < 4; ++q) {
      const f32x4 va = lds_f4(ra + q * 1024), vb = lds_f4(ra + 4096 + q * 1024);
      const f32x4 sa = {c[0][4 * q] + va[0], c[0][4 * q + 1] + va[1], c[0][4 * q + 2] + va[2], c[0][4 * q + 3] + va[3]};
      const f32x4 sb = {c[1][4 * q] + vb[0], c[1][4 * q + 1] + vb[1], c[1][4 * q + 2] + vb[2], c[1][4 * q + 3] + vb[3]};
      __builtin_amdgcn_raw_buffer_store_b128(__builtin_bit_cast(u32x4, sa), srdP, hg * 65536u + wid * 8192u + (unsigned)q * 1024u + ln * 16u, 0, 16);
      __builtin_amdgcn_raw_buffer_store_b128(__builtin_bit_cast(u32x4, sb), srdP, hg * 65536u + wid * 8192u + (unsigned)(4 + q) * 1024u + ln * 16u, 0, 16);
    }
    STAMP(7);
    WAIT_BAR(0);
    STAMP(8);
    if (wid == 0) {
      if (ln < 8u) __hip_atomic_store(flags + (size_t)((bid & ~7u) | ln) * 32u + 8u + hg, 1u, RLX_AGENT);
      wait_flags(flags + (size_t)bid * 32u + 8u + (ln & 7u), ln < 8u);
    }
    BAR();
    STAMP(9);
    f32x4 pv[8];
#pragma unroll
    for (int u = 0; u < 8; ++u)
      pv[u] = __builtin_bit_cast(f32x4, __builtin_amdgcn_raw_buffer_load_b128(srdP, hg * 8192u + wid * 1024u + ln * 16u, (unsigned)u * 65536u, 16));
    f32x4 acc = pv[0];
#pragma unroll
    for (int u = 1; u < 8; ++u) acc += pv[u];
    const unsigned qrow = (hg & 3u) * 32u + (ln & 31u), n = j * 128u + ((hg >> 2) * 2u + g) * 32u + (w4 >> 1) * 16u + (ln >> 5) * 8u + (w4 & 1u) * 4u;
    *(u32x2*)(out + ((size_t)b * SQ + qrow) * DM + n) = (u32x2){cvtpk(acc[0], acc[1]), cvtpk(acc[2], acc[3])};
    STAMP(10);
  }
}

extern "C" void kernel_launch(void* const* d_in, const int* in_sizes, int n_in, void* d_out, int out_size, void* d_ws, size_t ws_size, hipStream_t stream) {
  (void)in_sizes; (void)n_in; (void)out_size; (void)ws_size;
  (void)hipFuncSetAttribute((const void*)mha_fused, hipFuncAttributeMaxDynamicSharedMemorySize, (int)LDS_BYTES);
  (void)hipMemsetAsync((char*)d_ws + WS_FLAG, 0, FLAG_BYTES, stream);
  hipLaunchKernelGGL(mha_fused, dim3(256), dim3(512), LDS_BYTES, stream, (const unsigned short*)d_in[0], (const unsigned short*)d_in[1], (const unsigned short*)d_in[2],
                     (const unsigned short*)d_in[3], (const unsigned short*)d_in[4], (unsigned short*)d_out, (char*)d_ws);
}
```

```cpp
#include <hip/hip_runtime.h>
#include <hip/hip_bf16.h>
#include <stdint.h>
#include <type_traits>

typedef __bf16 bf16x8 __attribute__((ext_vector_type(8)));
typedef __bf16 bf16x2 __attribute__((ext_vector_type(2)));
typedef short s16x4 __attribute__((ext_vector_type(4)));
typedef float f32x16 __attribute__((ext_vector_type(16)));
typedef float f32x4 __attribute__((ext_vector_type(4)));
typedef float f32x2 __attribute__((ext_vector_type(2)));
typedef unsigned u32x4 __attribute__((ext_vector_type(4)));
typedef unsigned u32x2 __attribute__((ext_vector_type(2)));
#define LAS __attribute__((address_space(3)))
#define GAS __attribute__((address_space(1)))
#define DI __device__ __forceinline__
#define MFMA(a, b, c) __builtin_amdgcn_mfma_f32_32x32x16_bf16((a), (b), (c), 0, 0, 0)
#define RLX_AGENT __ATOMIC_RELAXED, __HIP_MEMORY_SCOPE_AGENT
#define PIN(x) asm volatile("" : "+v"(x))
#define SBAR() __builtin_amdgcn_sched_barrier(0)
#define PRIO(n) __builtin_amdgcn_s_setprio(n)

constexpr int SQ = 128, DM = 512, NH = 32, SK = 2048, DH = 128;
constexpr unsigned U = 16384u, SL = 32768u;
constexpr unsigned LDS_BYTES = 5 * SL;
#define PVPRIO 0
#define PSLIM 8192.0f
constexpr float CSC = 0.088388347648318447f * 1.4426950408889634f;
constexpr size_t WS_FLAG = 0;
constexpr unsigned FB = 128;
constexpr size_t WS_O = 1 << 20;
constexpr size_t WS_P = WS_O + (8u << 20);
constexpr size_t WS_END = WS_P + (16u << 20);

DI unsigned rfl(unsigned x) { return __builtin_amdgcn_readfirstlane(x); }
DI unsigned cvtpk(float lo, float hi) { f32x2 v = {lo, hi}; bf16x2 b = __builtin_convertvector(v, bf16x2); return __builtin_bit_cast(unsigned, b); }
DI bf16x8 pack8(const f32x16& a, int s) {
  u32x4 w = {cvtpk(a[8 * s], a[8 * s + 1]), cvtpk(a[8 * s + 2], a[8 * s + 3]), cvtpk(a[8 * s + 4], a[8 * s + 5]), cvtpk(a[8 * s + 6], a[8 * s + 7])};
  return __builtin_bit_cast(bf16x8, w);
}
DI bf16x8 lds_b128(unsigned a) { return *(const LAS bf16x8*)a; }
DI f32x4 lds_f4(unsigned a) { return *(const LAS f32x4*)a; }
DI void lds_st4(unsigned a, const f32x16& v, int q) { *(LAS f32x4*)a = (f32x4){v[4 * q], v[4 * q + 1], v[4 * q + 2], v[4 * q + 3]}; }
DI s16x4 lds_tr(unsigned a) { return __builtin_amdgcn_ds_read_tr16_b64_v4i16((LAS s16x4*)a); }
DI bf16x8 lds_tr2(unsigned a0, unsigned a1) {
  s16x4 lo = lds_tr(a0), hi = lds_tr(a1);
  return __builtin_bit_cast(bf16x8, __builtin_shufflevector(lo, hi, 0, 1, 2, 3, 4, 5, 6, 7));
}
DI void dma4_kv(unsigned m0v, unsigned voff, __amdgpu_buffer_rsrc_t srd, unsigned soff) {
  unsigned keep;
  asm volatile("s_mov_b32 %0, m0\n\ts_mov_b32 m0, %1\n\ts_nop 3\n\t"
               "buffer_load_dwordx4 %2, %3, %4 offen nt lds\n\t"
               "buffer_load_dwordx4 %2, %3, %4 offen offset:1024 nt lds\n\t"
               "buffer_load_dwordx4 %2, %3, %4 offen offset:2048 nt lds\n\t"
               "buffer_load_dwordx4 %2, %3, %4 offen offset:3072 nt lds\n\t"
               "s_mov_b32 m0, %0"
               : "=&s"(keep) : "s"(rfl(m0v)), "v"(voff), "s"(srd), "s"(rfl(soff)) : "memory");
}
DI void dma1(unsigned m0v, unsigned voff, __amdgpu_buffer_rsrc_t srd, unsigned soff) {
  unsigned keep;
  asm volatile("s_mov_b32 %0, m0\n\ts_mov_b32 m0, %1\n\ts_nop 3\n\t"
               "buffer_load_dwordx4 %2, %3, %4 offen lds\n\t"
               "s_mov_b32 m0, %0"
               : "=&s"(keep) : "s"(rfl(m0v)), "v"(voff), "s"(srd), "s"(rfl(soff)) : "memory");
}
DI void dma1nt(unsigned m0v, unsigned voff, __amdgpu_buffer_rsrc_t srd, unsigned soff) {
  unsigned keep;
  asm volatile("s_mov_b32 %0, m0\n\ts_mov_b32 m0, %1\n\ts_nop 3\n\t"
               "buffer_load_dwordx4 %2, %3, %4 offen nt lds\n\t"
               "s_mov_b32 m0, %0"
               : "=&s"(keep) : "s"(rfl(m0v)), "v"(voff), "s"(srd), "s"(rfl(soff)) : "memory");
}
#define DMAWQ dma1nt
#define DMAX dma1
#define WAIT_BAR(N) asm volatile("s_waitcnt vmcnt(" #N ") lgkmcnt(0)\n\ts_barrier" ::: "memory")
#define BAR() asm volatile("s_waitcnt lgkmcnt(0)\n\ts_barrier" ::: "memory")
#define WAITVM(N) asm volatile("s_waitcnt vmcnt(" #N ")" ::: "memory")

DI float xhalf_max(float v) { auto r = __builtin_amdgcn_permlane32_swap(__float_as_uint(v), __float_as_uint(v), false, false); return fmaxf(__uint_as_float(r[0]), __uint_as_float(r[1])); }
DI float xhalf_sum(float v) { auto r = __builtin_amdgcn_permlane32_swap(__float_as_uint(v), __float_as_uint(v), false, false); return __uint_as_float(r[0]) + __uint_as_float(r[1]); }

struct Hs { GAS unsigned* base; unsigned mine, peer; bool act; };
DI Hs hs_make(char* ws, unsigned bid, unsigned L) {
  Hs h; h.base = (GAS unsigned*)(ws + WS_FLAG);
  const bool h1 = L < 4u; h.act = h1 || (L >= 8u && L < 16u);
  const unsigned pb = h1 ? ((bid & ~24u) | (L << 3)) : ((bid & ~7u) | (L & 7u));
  h.mine = bid * FB + (L & 15u); h.peer = pb * FB + (h1 ? ((bid >> 3) & 3u) : 8u + (bid & 7u));
  return h;
}
DI void hs_echo(const Hs& h) { if (h.act) __hip_atomic_store(h.base + h.peer + 64u, __hip_atomic_load(h.base + h.mine + 32u, RLX_AGENT), RLX_AGENT); }
DI void hs_wait_armed(const Hs& h) {
  for (unsigned spins = 0; spins < (1u << 20); ++spins) {
    hs_echo(h);
    bool ok = true;
    if (h.act) ok = __hip_atomic_load(h.base + h.mine + 64u, RLX_AGENT) == __hip_atomic_load(h.base + h.peer + 32u, RLX_AGENT);
    if (__all(ok)) break;
    __builtin_amdgcn_s_sleep(4);
  }
}
DI void hs_wait_flags(const Hs& h, bool sel) {
  for (unsigned spins = 0; spins < (1u << 20); ++spins) {
    unsigned v = 1u;
    if (sel) v = __hip_atomic_load(h.base + h.mine, RLX_AGENT);
    if (__all(v != 0u)) break;
    if ((spins & 31u) == 31u) hs_echo(h);
    __builtin_amdgcn_s_sleep(1);
  }
  asm volatile("" ::: "memory");
}

#define STAMP(i) do { } while (0)

__global__ __launch_bounds__(512, 2) void mha_fused(const unsigned short* __restrict__ x, const unsigned short* __restrict__ kc, const unsigned short* __restrict__ vc,
                                                    const unsigned short* __restrict__ wq, const unsigned short* __restrict__ wo, unsigned short* __restrict__ out, char* __restrict__ ws) {
  extern __shared__ __attribute__((aligned(16))) char lds[];
  const unsigned tid = threadIdx.x, lane = tid & 63u, r32 = lane & 31u, hi = lane >> 5;
  const unsigned wid = rfl(tid >> 6), g = wid >> 2, w4 = wid & 3u;
  const unsigned bid = blockIdx.x, hg = bid & 7u, b = bid >> 5, j = (bid >> 3) & 3u, h = hg * 4u + j;
  const unsigned lds0 = (unsigned)(uintptr_t)lds;
  STAMP(0);
  asm volatile("" :: "s"(out), "s"(ws), "s"(wo));

  const __amdgpu_buffer_rsrc_t srdK = __builtin_amdgcn_make_buffer_rsrc((void*)(kc + (size_t)(b * NH + h) * SK * DH), (short)0, SK * DH * 2, 0x00020000);
  const __amdgpu_buffer_rsrc_t srdV = __builtin_amdgcn_make_buffer_rsrc((void*)(vc + (size_t)(b * NH + h) * SK * DH), (short)0, SK * DH * 2, 0x00020000);
  const __amdgpu_buffer_rsrc_t srdX = __builtin_amdgcn_make_buffer_rsrc((void*)(x + (size_t)b * SQ * DM), (short)0, SQ * DM * 2, 0x00020000);
  const __amdgpu_buffer_rsrc_t srdWq = __builtin_amdgcn_make_buffer_rsrc((void*)wq, (short)0, DM * NH * DH * 2, 0x00020000);
  const __amdgpu_buffer_rsrc_t srdWo = __builtin_amdgcn_make_buffer_rsrc((void*)(wo + (size_t)hg * 512 * DM), (short)0, 512 * DM * 2, 0x00020000);

  const unsigned l4 = lane >> 4, l15 = lane & 15u;
  const unsigned voffK = w4 * 4096u + l4 * 256u + ((l15 ^ (l4 | (w4 << 2))) << 4);
  const unsigned voffV = w4 * 4096u + l4 * 256u + ((l15 ^ (l4 << 2)) << 4);
  const unsigned voffWq = l4 * 8192u + ((l15 ^ (l4 << 2)) << 4);
  const unsigned voffWo = l4 * 1024u + ((l15 ^ (l4 << 2)) << 4);
  const unsigned voffX = (lane >> 3) * 1024u + (((lane & 7u) ^ (((lane >> 4) & 1u) | (w4 << 1))) << 4);

  auto issueK = [&](unsigned t, unsigned off) { dma4_kv(lds0 + off + w4 * 4096u, voffK, srdK, t * U); };
  auto issueV = [&](unsigned t, unsigned off) { dma4_kv(lds0 + off + w4 * 4096u, voffV, srdV, t * U); };
  auto issueWo = [&](unsigned cc, unsigned slot) {
    const unsigned c = (j + cc) & 3u;
#pragma unroll
    for (int q = 0; q < 8; ++q) dma1(lds0 + slot * SL + (w4 * 8u + q) * 1024u, voffWo, srdWo, (c * 128u + w4 * 32u + q * 4u) * 1024u + j * 256u);
  };
#define ROT ((b + 2u * j) & 7u)
  const unsigned rot = ROT;
  auto qslot = [](int c) { return (c % 3 == 0) ? 3u : (c % 3 == 1) ? 4u : 2u; };
  auto issueQP = [&](int c) {
    const unsigned kcn = ((unsigned)c + rot) & 7u, sb = lds0 + qslot(c) * SL;
#pragma unroll
    for (int q = 0; q < 4; ++q) {
      DMAWQ(sb + (w4 * 4u + q) * 1024u, voffWq, srdWq, (kcn * 64u + w4 * 16u + q * 4u) * 8192u + h * 256u);
      DMAX(sb + U + (q * 4u + w4) * 1024u, voffX, srdX, (q * 4u + w4) * 8192u + kcn * 128u);
    }
  };
  const unsigned blk = (lane >> 4) & 1u, q4 = l15 >> 2, p4 = lane & 3u, pp = ((p4 & 1u) << 1) | (p4 >> 1);
  const unsigned trV = lds0 + U + (4u * hi + q4) * 256u + ((q4 ^ (2u * g)) << 6) + blk * 32u + pp * 8u;
  const unsigned rdK = lds0 + ((r32 & 7u) + 16u * (r32 >> 3)) * 256u + ((hi ^ ((r32 & 3u) | ((r32 >> 3) << 2))) << 4);
  const unsigned qx = lds0 + 3u * SL + w4 * 4096u + lane * 16u;

  bf16x8 qf[8], kf[16];
  auto rdk = [&](unsigned off, int i) { return lds_b128(((rdK + off) ^ (unsigned)((i & 7) << 5)) + (i >> 3) * 2048); };
  if (g == 0) {
    const unsigned trW = lds0 + (8u * hi + q4) * 256u + (q4 << 6) + blk * 32u + pp * 8u;
    const unsigned rdX = lds0 + U + (w4 * 32u + r32) * 128u + ((hi ^ (((r32 >> 1) & 1u) | ((r32 >> 3) << 1))) << 4);
    f32x16 qa[4] = {};
    bf16x8 xb[2][4], wa_[2][16];
    auto rdq = [&](int c, int s) {
      const unsigned u0 = qslot(c) * SL;
      xb[c & 1][s] = lds_b128((rdX + u0) ^ (unsigned)(s << 5));
#pragma unroll
      for (int d = 0; d < 4; ++d) { const unsigned a = ((trW + u0) ^ (unsigned)(d << 6)) + s * 4096; wa_[c & 1][4 * s + d] = lds_tr2(a, a + 1024); }
    };
    if (wid == 0) {
      const Hs hs = hs_make(ws, bid, lane);
      if (hs.act) {
        const unsigned r0 = __hip_atomic_load(hs.base + hs.mine + 64u, RLX_AGENT);
        __hip_atomic_store(hs.base + hs.mine, 0u, RLX_AGENT);
        __hip_atomic_store(hs.base + hs.peer + 32u, r0 + 1u, RLX_AGENT);
      }
      WAITVM(0);
    }
    BAR();
#pragma unroll
    for (int s = 0; s < 4; ++s) rdq(0, s);
#pragma unroll
    for (int c = 0; c < 8; ++c) {
      if (c < 7) BAR();
#pragma unroll
      for (int s = 0; s < 4; ++s)
#pragma unroll
        for (int d = 0; d < 4; ++d) {
          SBAR();
          qa[d] = MFMA(wa_[c & 1][4 * s + d], xb[c & 1][s], qa[d]);
          SBAR();
          if (c < 7) {
            const unsigned u0 = qslot(c + 1) * SL, a = ((trW + u0) ^ (unsigned)(d << 6)) + s * 4096;
            if (d == 0) xb[(c + 1) & 1][s] = lds_b128((rdX + u0) ^ (unsigned)(s << 5));
            wa_[(c + 1) & 1][4 * s + d] = lds_tr2(a, a + 1024);
          }
        }
      SBAR();
    }
#pragma unroll
    for (int d = 0; d < 4; ++d) {
#pragma unroll
      for (int r = 0; r < 16; ++r) qa[d][r] *= CSC;
      qf[2 * d] = pack8(qa[d], 0); qf[2 * d + 1] = pack8(qa[d], 1);
    }
#pragma unroll
    for (int f = 0; f < 8; ++f) *(LAS bf16x8*)(qx + (f >> 2) * U + (f & 3) * 1024) = qf[f];
    BAR();
#pragma unroll
    for (int i = 0; i < 16; ++i) kf[i] = rdk(U, i);
  } else {
    issueQP(0); issueQP(1);
    WAIT_BAR(8); issueQP(2);
#pragma unroll
    for (int c = 0; c < 5; ++c) { WAIT_BAR(8); issueQP(c + 3); }
    issueK(0, U); issueK(1, 0);
    WAIT_BAR(16);
    WAIT_BAR(8);
    const Hs hs = hs_make(ws, bid, lane);
    unsigned rq;
    asm volatile("global_load_dword %0, %1, off sc1" : "=v"(rq) : "v"(hs.base + hs.mine + 32u) : "memory");
    issueK(2, SL); issueV(0, SL + U);
    asm volatile("s_waitcnt vmcnt(8) lgkmcnt(0)\n\ts_barrier" : "+v"(rq) :: "memory");
    if (wid == 4 && hs.act) __hip_atomic_store(hs.base + hs.peer + 64u, rq, RLX_AGENT);
#pragma unroll
    for (int f = 0; f < 8; ++f) qf[f] = lds_b128(qx + (f >> 2) * U + (f & 3) * 1024);
#pragma unroll
    for (int i = 0; i < 16; ++i) kf[i] = rdk(0, i);
    issueK(3, 2u * SL); issueV(1, 2u * SL + U);
    WAIT_BAR(8);
  }
  STAMP(1);

  f32x16 o[4] = {};
  float m_run = 0.f, l_run = 0.f;
  f32x16 negm = {};
  bf16x8 pf[4], vf[16];
  auto vfrag = [&](unsigned av, int ks, int d) { const unsigned a = (av ^ (unsigned)(d << 6)) + (32 * (ks & 1) + 8 * (ks >> 1)) * 256; return lds_tr2(a, a + 4096); };
  auto scores_slow = [&](bool first, float& ps) {
    f32x16 s0 = {}, s1 = {};
#pragma unroll
    for (int s = 0; s < 8; ++s) { s0 = MFMA(kf[s], qf[s], s0); s1 = MFMA(kf[8 + s], qf[s], s1); }
    float mx = fmaxf(s0[0], s1[0]);
#pragma unroll
    for (int r = 1; r < 16; ++r) mx = fmaxf(fmaxf(mx, s0[r]), s1[r]);
    mx = xhalf_max(mx);
    const float m_new = first ? mx : fmaxf(mx, m_run);
    if (!first) {
      const float alpha = __builtin_amdgcn_exp2f(m_run - m_new);
      l_run *= alpha;
#pragma unroll
      for (int d = 0; d < 4; ++d)
#pragma unroll
        for (int r = 0; r < 16; ++r) o[d][r] *= alpha;
    }
    m_run = m_new;
    ps = 0.f;
#pragma unroll
    for (int r = 0; r < 16; ++r) { negm[r] = -m_new; s0[r] = __builtin_amdgcn_exp2f(s0[r] - m_new); s1[r] = __builtin_amdgcn_exp2f(s1[r] - m_new); ps += s0[r] + s1[r]; }
    pf[0] = pack8(s0, 0); pf[1] = pack8(s0, 1); pf[2] = pack8(s1, 0); pf[3] = pack8(s1, 1);
  };
  auto scores = [&](bool first, auto inloop) {
    float ps = 0.f;
    PRIO(2);
    if (!first) {
      SBAR();
      f32x16 s0 = MFMA(kf[0], qf[0], negm);
#pragma unroll
      for (int s = 1; s < 8; ++s) s0 = MFMA(kf[s], qf[s], s0);
      SBAR();
      f32x16 s1 = MFMA(kf[8], qf[0], negm);
      SBAR();
      float pa = 0.f, pb = 0.f;
      unsigned w[16];
#define SMSTEP(S, e, wi) do { S[e] = __builtin_amdgcn_exp2f(S[e]); S[e + 1] = __builtin_amdgcn_exp2f(S[e + 1]); pa += S[e]; pb += S[e + 1]; w[wi] = cvtpk(S[e], S[e + 1]); PIN(pa); PIN(pb); PIN(w[wi]); } while (0)
#pragma unroll
      for (int s = 1; s < 8; ++s) {
        s1 = MFMA(kf[8 + s], qf[s], s1);
        SMSTEP(s0, 2 * (s - 1), s - 1);
        SBAR();
      }
      SMSTEP(s0, 14, 7);
      SBAR();
#pragma unroll
      for (int i = 0; i < 8; ++i) { SMSTEP(s1, 2 * i, 8 + i); SBAR(); }
#undef SMSTEP
      ps = pa + pb;
#pragma unroll
      for (int i = 0; i < 4; ++i) pf[i] = __builtin_bit_cast(bf16x8, (u32x4){w[4 * i], w[4 * i + 1], w[4 * i + 2], w[4 * i + 3]});
    }
    if (__builtin_expect(first || __any(!(ps <= PSLIM)), 0)) scores_slow(first, ps);
    l_run += ps;
    PIN(pf[0]); PIN(pf[1]); PIN(pf[2]); PIN(pf[3]); PIN(l_run); if (decltype(inloop)::value) PIN(negm);
    PRIO(PVPRIO);
  };

  __builtin_amdgcn_s_waitcnt(0xC07F);
  unsigned sp = 1u + g;
  for (int it = 0; it < 15; ++it) {
    const unsigned p = 2u * (unsigned)it + g + 1u;
    scores(it == 0, std::true_type{});
    WAIT_BAR(0);
    const unsigned ni = p + 2u, sn = sp >= 3u ? sp - 3u : sp + 2u, ns = sn * SL;
    const unsigned av = trV + sp * SL;
#pragma unroll
    for (int i = 0; i < 16; ++i) vf[i] = vfrag(av, i >> 2, i & 3);
    SBAR();
    if (ni <= 30u) issueK(ni + 1u, ns);
    SBAR();
#pragma unroll
    for (int i = 0; i < 8; ++i) kf[i] = rdk(sp * SL, i);
    SBAR();
#pragma unroll
    for (int i = 0; i < 8; ++i) o[i & 3] = MFMA(vf[i], pf[i >> 2], o[i & 3]);
    SBAR();
#pragma unroll
    for (int i = 8; i < 16; ++i) { o[i & 3] = MFMA(vf[i], pf[i >> 2], o[i & 3]); if (i < 12) { kf[2 * i - 8] = rdk(sp * SL, 2 * i - 8); kf[2 * i - 7] = rdk(sp * SL, 2 * i - 7); } SBAR(); }
    issueV(ni - 1u, ns + U);
    if (it == 14) issueWo(g, 3u + g);
    sp = sn;
    BAR();
  }
  scores(false, std::false_type{});
  unsigned ln; asm volatile("v_mbcnt_lo_u32_b32 %0, -1, 0\n\tv_mbcnt_hi_u32_b32 %0, -1, %0" : "=v"(ln));
  const unsigned x0 = lds0 + w4 * 8192u + ln * 16u;
  l_run = xhalf_sum(l_run);
  const unsigned stats = lds0 + 2u * SL + (ln & 31u) * 8u;
  if (ln < 32u) *(LAS f32x2*)(stats + wid * 256u) = (f32x2){m_run, l_run};
  const Hs hs = hs_make(ws, bid, ln);
  unsigned hs_r, hs_q;
  asm volatile("global_load_dword %0, %2, off offset:256 sc1\n\tglobal_load_dword %1, %3, off offset:128 sc1" : "=&v"(hs_r), "=&v"(hs_q) : "v"(hs.base + hs.mine), "v"(hs.base + hs.peer) : "memory");
  bf16x8 of0[4];
  {
    WAIT_BAR(10);
    STAMP(2);
    const unsigned av = trV + (1u + g) * SL;
#pragma unroll
    for (int ks = 0; ks < 4; ++ks) { vf[4 * ks + 2] = vfrag(av, ks, 2); vf[4 * ks + 3] = vfrag(av, ks, 3); }
    SBAR();
#pragma unroll
    for (int ks = 0; ks < 4; ++ks) { vf[4 * ks] = vfrag(av, ks, 0); vf[4 * ks + 1] = vfrag(av, ks, 1); }
    SBAR();
#pragma unroll
    for (int ks = 0; ks < 4; ++ks) { o[2] = MFMA(vf[4 * ks + 2], pf[ks], o[2]); o[3] = MFMA(vf[4 * ks + 3], pf[ks], o[3]); }
    SBAR();
#pragma unroll
    for (int ks = 0; ks < 2; ++ks) { o[0] = MFMA(vf[4 * ks], pf[ks], o[0]); o[1] = MFMA(vf[4 * ks + 1], pf[ks], o[1]); }
    SBAR();
    const unsigned xo = x0 + g * SL, xi = x0 + (1u - g) * SL;
#pragma unroll
    for (int q = 0; q < 4; ++q) { lds_st4(xo + q * 1024, o[2], q); lds_st4(xo + 4096 + q * 1024, o[3], q); }
    SBAR();
#pragma unroll
    for (int ks = 2; ks < 4; ++ks) { o[0] = MFMA(vf[4 * ks], pf[ks], o[0]); o[1] = MFMA(vf[4 * ks + 1], pf[ks], o[1]); }
    SBAR();
    if (g == 0) BAR();
    const f32x2 st = *(const LAS f32x2*)(stats + (wid ^ 4u) * 256u);
    const float ms = fmaxf(m_run, st[0]);
    const float e_me = __builtin_amdgcn_exp2f(m_run - ms), e_ot = __builtin_amdgcn_exp2f(st[0] - ms);
    const float inv = 1.f / (l_run * e_me + st[1] * e_ot);
    float a_me = e_me * inv, a_ot = e_ot * inv;
    PIN(a_me); PIN(a_ot);
    asm volatile("s_waitcnt vmcnt(0) lgkmcnt(0)\n\ts_barrier" : "+v"(hs_r), "+v"(hs_q) :: "memory");
    f32x4 w0[4], w1[4];
#pragma unroll
    for (int q = 0; q < 4; ++q) { w0[q] = lds_f4(xi + q * 1024); w1[q] = lds_f4(xi + 4096 + q * 1024); }
#pragma unroll
    for (int q = 0; q < 4; ++q)
#pragma unroll
      for (int e = 0; e < 4; ++e) { o[0][4 * q + e] = fmaf(w0[q][e], a_ot, o[0][4 * q + e] * a_me); o[1][4 * q + e] = fmaf(w1[q][e], a_ot, o[1][4 * q + e] * a_me); }
    of0[0] = pack8(o[0], 0); of0[1] = pack8(o[0], 1); of0[2] = pack8(o[1], 0); of0[3] = pack8(o[1], 1);
  }
  PRIO(0);
  const __amdgpu_buffer_rsrc_t srdO = __builtin_amdgcn_make_buffer_rsrc((void*)(ws + WS_O + (size_t)(b * NH + hg * 4u) * 32768u), (short)0, 4 * 32768, 0x00020000);
#pragma unroll
  for (int s = 0; s < 4; ++s) __builtin_amdgcn_raw_buffer_store_b128(__builtin_bit_cast(u32x4, of0[s]), srdO, j * 32768u + wid * 4096u + (unsigned)s * 1024u + ln * 16u, 0, 16);
  STAMP(3);
  issueWo(2u + g, 1u - g);
  f32x16 c[4] = {};
  const unsigned trW2 = trV - U + (ln >> 5) * 1024u;
  bf16x8 wa[2][8];
  auto rdw = [&](int buf, unsigned slot, int half) {
    const unsigned aw = trW2 + slot * SL + g * U;
#pragma unroll
    for (int s = 0; s < 2; ++s)
#pragma unroll
      for (int nb = 0; nb < 4; ++nb) { const unsigned a = (aw ^ (unsigned)(nb << 6)) + (2 * half + s) * 4096; wa[buf][4 * s + nb] = lds_tr2(a, a + 1024); }
  };
  auto mmw = [&](int buf, const bf16x8& f0, const bf16x8& f1) {
#pragma unroll
    for (int nb = 0; nb < 4; ++nb) c[nb] = MFMA(wa[buf][nb], f0, c[nb]);
#pragma unroll
    for (int nb = 0; nb < 4; ++nb) c[nb] = MFMA(wa[buf][4 + nb], f1, c[nb]);
  };
  rdw(0, 3, 0); rdw(1, 3, 1); SBAR();
  mmw(0, of0[0], of0[1]); SBAR();
  rdw(0, 4, 0); SBAR();
  mmw(1, of0[2], of0[3]); SBAR();
  rdw(1, 4, 1);
  PIN(c[0]); PIN(c[1]); PIN(c[2]); PIN(c[3]);
  WAIT_BAR(0);
  STAMP(4);
  if (wid == 0) {
    if (!__all(hs_r == hs_q || !hs.act)) hs_wait_armed(hs);
    if (ln < 4u) __hip_atomic_store(hs.base + hs.peer, 1u, RLX_AGENT);
    hs_wait_flags(hs, ln < 4u);
  }
  BAR();
  STAMP(5);
  {
    bf16x8 of[3][4];
#pragma unroll
    for (int cc = 1; cc < 4; ++cc)
#pragma unroll
      for (int s = 0; s < 4; ++s)
        of[cc - 1][s] = __builtin_bit_cast(bf16x8, __builtin_amdgcn_raw_buffer_load_b128(srdO, wid * 4096u + (unsigned)s * 1024u + ln * 16u, ((j + (unsigned)cc) & 3u) * 32768u, 16));
    SBAR();
    mmw(0, of[0][0], of[0][1]); SBAR(); rdw(0, 1, 0); SBAR();
    mmw(1, of[0][2], of[0][3]); SBAR(); rdw(1, 1, 1); SBAR();
    mmw(0, of[1][0], of[1][1]); SBAR(); rdw(0, 0, 0); SBAR();
    mmw(1, of[1][2], of[1][3]); SBAR(); rdw(1, 0, 1); SBAR();
    mmw(0, of[2][0], of[2][1]); SBAR();
    mmw(1, of[2][2], of[2][3]);
  }
  STAMP(6);
  {
#pragma unroll
    for (int q = 0; q < 4; ++q) { lds_st4(x0 + (2u + g) * SL + q * 1024, c[2], q); lds_st4(x0 + (2u + g) * SL + 4096 + q * 1024, c[3], q); }
    BAR();
    const unsigned ra = x0 + (3u - g) * SL;
    const __amdgpu_buffer_rsrc_t srdP = __builtin_amdgcn_make_buffer_rsrc((void*)(ws + WS_P + (size_t)(b * 4u + j) * 524288u), (short)0, 524288, 0x00020000);
#pragma unroll
    for (int q = 0; q < 4; ++q) {
      const f32x4 va = lds_f4(ra + q * 1024), vb = lds_f4(ra + 4096 + q * 1024);
      const f32x4 sa = {c[0][4 * q] + va[0], c[0][4 * q + 1] + va[1], c[0][4 * q + 2] + va[2], c[0][4 * q + 3] + va[3]};
      const f32x4 sb = {c[1][4 * q] + vb[0], c[1][4 * q + 1] + vb[1], c[1][4 * q + 2] + vb[2], c[1][4 * q + 3] + vb[3]};
      __builtin_amdgcn_raw_buffer_store_b128(__builtin_bit_cast(u32x4, sa), srdP, hg * 65536u + wid * 8192u + (unsigned)q * 1024u + ln * 16u, 0, 16);
      __builtin_amdgcn_raw_buffer_store_b128(__builtin_bit_cast(u32x4, sb), srdP, hg * 65536u + wid * 8192u + (unsigned)(4 + q) * 1024u + ln * 16u, 0, 16);
    }
    STAMP(7);
    WAIT_BAR(0);
    STAMP(8);
    if (wid == 0) {
      if (hs.act && ln >= 8u) __hip_atomic_store(hs.base + hs.peer, 1u, RLX_AGENT);
      hs_wait_flags(hs, hs.act && ln >= 8u);
    }
    BAR();
    STAMP(9);
    f32x4 pv[8];
#pragma unroll
    for (int u = 0; u < 8; ++u)
      pv[u] = __builtin_bit_cast(f32x4, __builtin_amdgcn_raw_buffer_load_b128(srdP, hg * 8192u + wid * 1024u + ln * 16u, (unsigned)u * 65536u, 16));
    f32x4 acc = pv[0];
#pragma unroll
    for (int u = 1; u < 8; ++u) acc += pv[u];
    const unsigned qrow = (hg & 3u) * 32u + (ln & 31u), n = j * 128u + ((hg >> 2) * 2u + g) * 32u + (w4 >> 1) * 16u + (ln >> 5) * 8u + (w4 & 1u) * 4u;
    *(u32x2*)(out + ((size_t)b * SQ + qrow) * DM + n) = (u32x2){cvtpk(acc[0], acc[1]), cvtpk(acc[2], acc[3])};
    STAMP(10);
  }
}

extern "C" void kernel_launch(void* const* d_in, const int* in_sizes, int n_in, void* d_out, int out_size, void* d_ws, size_t ws_size, hipStream_t stream) {
  (void)in_sizes; (void)n_in; (void)out_size; (void)ws_size;
  (void)hipFuncSetAttribute((const void*)mha_fused, hipFuncAttributeMaxDynamicSharedMemorySize, (int)LDS_BYTES);
  hipLaunchKernelGGL(mha_fused, dim3(256), dim3(512), LDS_BYTES, stream, (const unsigned short*)d_in[0], (const unsigned short*)d_in[1], (const unsigned short*)d_in[2],
                     (const unsigned short*)d_in[3], (const unsigned short*)d_in[4], (unsigned short*)d_out, (char*)d_ws);
}
```

```cpp
#include <hip/hip_runtime.h>
#include <hip/hip_bf16.h>
#include <stdint.h>
#include <type_traits>

typedef __bf16 bf16x8 __attribute__((ext_vector_type(8)));
typedef __bf16 bf16x2 __attribute__((ext_vector_type(2)));
typedef short s16x4 __attribute__((ext_vector_type(4)));
typedef float f32x16 __attribute__((ext_vector_type(16)));
typedef float f32x4 __attribute__((ext_vector_type(4)));
typedef float f32x2 __attribute__((ext_vector_type(2)));
typedef unsigned u32x4 __attribute__((ext_vector_type(4)));
typedef unsigned u32x2 __attribute__((ext_vector_type(2)));
#define LAS __attribute__((address_space(3)))
#define GAS __attribute__((address_space(1)))
#define DI __device__ __forceinline__
#define MFMA(a, b, c) __builtin_amdgcn_mfma_f32_32x32x16_bf16((a), (b), (c), 0, 0, 0)
#define RLX_AGENT __ATOMIC_RELAXED, __HIP_MEMORY_SCOPE_AGENT
#define PIN(x) asm volatile("" : "+v"(x))
#define SBAR() __builtin_amdgcn_sched_barrier(0)
#define PRIO(n) __builtin_amdgcn_s_setprio(n)

constexpr int SQ = 128, DM = 512, NH = 32, SK = 2048, DH = 128;
constexpr unsigned U = 16384u, SL = 32768u;
constexpr unsigned LDS_BYTES = 5 * SL;
#define PVPRIO 0
#define PSLIM 8192.0f
constexpr float CSC = 0.088388347648318447f * 1.4426950408889634f;
constexpr size_t WS_FLAG = 0;
constexpr unsigned FB = 128;
constexpr size_t WS_O = 1 << 20;
constexpr size_t WS_P = WS_O + (8u << 20);
constexpr size_t WS_END = WS_P + (16u << 20);

DI unsigned rfl(unsigned x) { return __builtin_amdgcn_readfirstlane(x); }
DI unsigned cvtpk(float lo, float hi) { f32x2 v = {lo, hi}; bf16x2 b = __builtin_convertvector(v, bf16x2); return __builtin_bit_cast(unsigned, b); }
DI bf16x8 pack8(const f32x16& a, int s) {
  u32x4 w = {cvtpk(a[8 * s], a[8 * s + 1]), cvtpk(a[8 * s + 2], a[8 * s + 3]), cvtpk(a[8 * s + 4], a[8 * s + 5]), cvtpk(a[8 * s + 6], a[8 * s + 7])};
  return __builtin_bit_cast(bf16x8, w);
}
DI bf16x8 lds_b128(unsigned a) { return *(const LAS bf16x8*)a; }
DI f32x4 lds_f4(unsigned a) { return *(const LAS f32x4*)a; }
DI void lds_st4(unsigned a, const f32x16& v, int q) { *(LAS f32x4*)a = (f32x4){v[4 * q], v[4 * q + 1], v[4 * q + 2], v[4 * q + 3]}; }
DI s16x4 lds_tr(unsigned a) { return __builtin_amdgcn_ds_read_tr16_b64_v4i16((LAS s16x4*)a); }
DI bf16x8 lds_tr2(unsigned a0, unsigned a1) {
  s16x4 lo = lds_tr(a0), hi = lds_tr(a1);
  return __builtin_bit_cast(bf16x8, __builtin_shufflevector(lo, hi, 0, 1, 2, 3, 4, 5, 6, 7));
}
DI void dma4_kv(unsigned m0v, unsigned voff, __amdgpu_buffer_rsrc_t srd, unsigned soff) {
  unsigned keep;
  asm volatile("s_mov_b32 %0, m0\n\ts_mov_b32 m0, %1\n\ts_nop 3\n\t"
               "buffer_load_dwordx4 %2, %3, %4 offen sc1 nt lds\n\t"
               "buffer_load_dwordx4 %2, %3, %4 offen offset:1024 sc1 nt lds\n\t"
               "buffer_load_dwordx4 %2, %3, %4 offen offset:2048 sc1 nt lds\n\t"
               "buffer_load_dwordx4 %2, %3, %4 offen offset:3072 sc1 nt lds\n\t"
               "s_mov_b32 m0, %0"
               : "=&s"(keep) : "s"(rfl(m0v)), "v"(voff), "s"(srd), "s"(rfl(soff)) : "memory");
}
DI void dma1(unsigned m0v, unsigned voff, __amdgpu_buffer_rsrc_t srd, unsigned soff) {
  unsigned keep;
  asm volatile("s_mov_b32 %0, m0\n\ts_mov_b32 m0, %1\n\ts_nop 3\n\t"
               "buffer_load_dwordx4 %2, %3, %4 offen lds\n\t"
               "s_mov_b32 m0, %0"
               : "=&s"(keep) : "s"(rfl(m0v)), "v"(voff), "s"(srd), "s"(rfl(soff)) : "memory");
}
DI void dma1nt(unsigned m0v, unsigned voff, __amdgpu_buffer_rsrc_t srd, unsigned soff) {
  unsigned keep;
  asm volatile("s_mov_b32 %0, m0\n\ts_mov_b32 m0, %1\n\ts_nop 3\n\t"
               "buffer_load_dwordx4 %2, %3, %4 offen nt lds\n\t"
               "s_mov_b32 m0, %0"
               : "=&s"(keep) : "s"(rfl(m0v)), "v"(voff), "s"(srd), "s"(rfl(soff)) : "memory");
}
#define DMAWQ dma1nt
#define DMAX dma1
#define WAIT_BAR(N) asm volatile("s_waitcnt vmcnt(" #N ") lgkmcnt(0)\n\ts_barrier" ::: "memory")
#define BAR() asm volatile("s_waitcnt lgkmcnt(0)\n\ts_barrier" ::: "memory")
#define WAITVM(N) asm volatile("s_waitcnt vmcnt(" #N ")" ::: "memory")

DI float xhalf_max(float v) { auto r = __builtin_amdgcn_permlane32_swap(__float_as_uint(v), __float_as_uint(v), false, false); return fmaxf(__uint_as_float(r[0]), __uint_as_float(r[1])); }
DI float xhalf_sum(float v) { auto r = __builtin_amdgcn_permlane32_swap(__float_as_uint(v), __float_as_uint(v), false, false); return __uint_as_float(r[0]) + __uint_as_float(r[1]); }

struct Hs { GAS unsigned* base; unsigned mine, peer; bool act; };
DI Hs hs_make(char* ws, unsigned bid, unsigned L) {
  Hs h; h.base = (GAS unsigned*)(ws + WS_FLAG);
  const bool h1 = L < 4u; h.act = h1 || (L >= 8u && L < 16u);
  const unsigned pb = h1 ? ((bid & ~24u) | (L << 3)) : ((bid & ~7u) | (L & 7u));
  h.mine = bid * FB + (L & 15u); h.peer = pb * FB + (h1 ? ((bid >> 3) & 3u) : 8u + (bid & 7u));
  return h;
}
DI void hs_echo(const Hs& h) { if (h.act) __hip_atomic_store(h.base + h.peer + 64u, __hip_atomic_load(h.base + h.mine + 32u, RLX_AGENT), RLX_AGENT); }
DI void hs_wait_armed(const Hs& h) {
  for (unsigned spins = 0; spins < (1u << 20); ++spins) {
    hs_echo(h);
    bool ok = true;
    if (h.act) ok = __hip_atomic_load(h.base + h.mine + 64u, RLX_AGENT) == __hip_atomic_load(h.base + h.peer + 32u, RLX_AGENT);
    if (__all(ok)) break;
    __builtin_amdgcn_s_sleep(4);
  }
}
DI void hs_wait_flags(const Hs& h, bool sel) {
  for (unsigned spins = 0; spins < (1u << 20); ++spins) {
    unsigned v = 1u;
    if (sel) v = __hip_atomic_load(h.base + h.mine, RLX_AGENT);
    if (__all(v != 0u)) break;
    if ((spins & 31u) == 31u) hs_echo(h);
    __builtin_amdgcn_s_sleep(1);
  }
  asm volatile("" ::: "memory");
}

#define STAMP(i) do { } while (0)

__global__ __launch_bounds__(512, 2) void mha_fused(const unsigned short* __restrict__ x, const unsigned short* __restrict__ kc, const unsigned short* __restrict__ vc,
                                                    const unsigned short* __restrict__ wq, const unsigned short* __restrict__ wo, unsigned short* __restrict__ out, char* __restrict__ ws) {
  extern __shared__ __attribute__((aligned(16))) char lds[];
  const unsigned tid = threadIdx.x, lane = tid & 63u, r32 = lane & 31u, hi = lane >> 5;
  const unsigned wid = rfl(tid >> 6), g = wid >> 2, w4 = wid & 3u;
  const unsigned bid = blockIdx.x, hg = bid & 7u, b = bid >> 5, j = (bid >> 3) & 3u, h = hg * 4u + j;
  const unsigned lds0 = (unsigned)(uintptr_t)lds;
  STAMP(0);
  asm volatile("" :: "s"(out), "s"(ws), "s"(wo));

  const __amdgpu_buffer_rsrc_t srdK = __builtin_amdgcn_make_buffer_rsrc((void*)(kc + (size_t)(b * NH + h) * SK * DH), (short)0, SK * DH * 2, 0x00020000);
  const __amdgpu_buffer_rsrc_t srdV = __builtin_amdgcn_make_buffer_rsrc((void*)(vc + (size_t)(b * NH + h) * SK * DH), (short)0, SK * DH * 2, 0x00020000);
  const __amdgpu_buffer_rsrc_t srdX = __builtin_amdgcn_make_buffer_rsrc((void*)(x + (size_t)b * SQ * DM), (short)0, SQ * DM * 2, 0x00020000);
  const __amdgpu_buffer_rsrc_t srdWq = __builtin_amdgcn_make_buffer_rsrc((void*)wq, (short)0, DM * NH * DH * 2, 0x00020000);
  const __amdgpu_buffer_rsrc_t srdWo = __builtin_amdgcn_make_buffer_rsrc((void*)(wo + (size_t)hg * 512 * DM), (short)0, 512 * DM * 2, 0x00020000);

  const unsigned l4 = lane >> 4, l15 = lane & 15u;
  const unsigned voffK = w4 * 4096u + l4 * 256u + ((l15 ^ (l4 | (w4 << 2))) << 4);
  const unsigned voffV = w4 * 4096u + l4 * 256u + ((l15 ^ (l4 << 2)) << 4);
  const unsigned voffWq = l4 * 8192u + ((l15 ^ (l4 << 2)) << 4);
  const unsigned voffWo = l4 * 1024u + ((l15 ^ (l4 << 2)) << 4);
  const unsigned voffX = (lane >> 3) * 1024u + (((lane & 7u) ^ (((lane >> 4) & 1u) | (w4 << 1))) << 4);

  auto issueK = [&](unsigned t, unsigned off) { dma4_kv(lds0 + off + w4 * 4096u, voffK, srdK, t * U); };
  auto issueV = [&](unsigned t, unsigned off) { dma4_kv(lds0 + off + w4 * 4096u, voffV, srdV, t * U); };
  auto issueWo = [&](unsigned cc, unsigned slot) {
    const unsigned c = (j + cc) & 3u;
#pragma unroll
    for (int q = 0; q < 8; ++q) dma1(lds0 + slot * SL + (w4 * 8u + q) * 1024u, voffWo, srdWo, (c * 128u + w4 * 32u + q * 4u) * 1024u + j * 256u);
  };
#define ROT ((b + 2u * j) & 7u)
  const unsigned rot = ROT;
  auto qslot = [](int c) { return (c % 3 == 0) ? 3u : (c % 3 == 1) ? 4u : 2u; };
  auto issueQP = [&](int c) {
    const unsigned kcn = ((unsigned)c + rot) & 7u, sb = lds0 + qslot(c) * SL;
#pragma unroll
    for (int q = 0; q < 4; ++q) {
      DMAWQ(sb + (w4 * 4u + q) * 1024u, voffWq, srdWq, (kcn * 64u + w4 * 16u + q * 4u) * 8192u + h * 256u);
      DMAX(sb + U + (q * 4u + w4) * 1024u, voffX, srdX, (q * 4u + w4) * 8192u + kcn * 128u);
    }
  };
  const unsigned blk = (lane >> 4) & 1u, q4 = l15 >> 2, p4 = lane & 3u, pp = ((p4 & 1u) << 1) | (p4 >> 1);
  const unsigned trV = lds0 + U + (4u * hi + q4) * 256u + ((q4 ^ (2u * g)) << 6) + blk * 32u + pp * 8u;
  const unsigned rdK = lds0 + ((r32 & 7u) + 16u * (r32 >> 3)) * 256u + ((hi ^ ((r32 & 3u) | ((r32 >> 3) << 2))) << 4);
  const unsigned qx = lds0 + 3u * SL + w4 * 4096u + lane * 16u;

  bf16x8 qf[8], kf[16];
  auto rdk = [&](unsigned off, int i) { return lds_b128(((rdK + off) ^ (unsigned)((i & 7) << 5)) + (i >> 3) * 2048); };
  if (g == 0) {
    const unsigned trW = lds0 + (8u * hi + q4) * 256u + (q4 << 6) + blk * 32u + pp * 8u;
    const unsigned rdX = lds0 + U + (w4 * 32u + r32) * 128u + ((hi ^ (((r32 >> 1) & 1u) | ((r32 >> 3) << 1))) << 4);
    f32x16 qa[4] = {};
    bf16x8 xb[2][4], wa_[2][16];
    auto rdq = [&](int c, int s) {
      const unsigned u0 = qslot(c) * SL;
      xb[c & 1][s] = lds_b128((rdX + u0) ^ (unsigned)(s << 5));
#pragma unroll
      for (int d = 0; d < 4; ++d) { const unsigned a = ((trW + u0) ^ (unsigned)(d << 6)) + s * 4096; wa_[c & 1][4 * s + d] = lds_tr2(a, a + 1024); }
    };
    if (wid == 0) {
      const Hs hs = hs_make(ws, bid, lane);
      if (hs.act) {
        const unsigned r0 = __hip_atomic_load(hs.base + hs.mine + 64u, RLX_AGENT);
        __hip_atomic_store(hs.base + hs.mine, 0u, RLX_AGENT);
        __hip_atomic_store(hs.base + hs.peer + 32u, r0 + 1u, RLX_AGENT);
      }
      WAITVM(0);
    }
    BAR();
#pragma unroll
    for (int s = 0; s < 4; ++s) rdq(0, s);
#pragma unroll
    for (int c = 0; c < 8; ++c) {
      if (c < 7) BAR();
#pragma unroll
      for (int s = 0; s < 4; ++s)
#pragma unroll
        for (int d = 0; d < 4; ++d) {
          SBAR();
          qa[d] = MFMA(wa_[c & 1][4 * s + d], xb[c & 1][s], qa[d]);
          SBAR();
          if (c < 7) {
            const unsigned u0 = qslot(c + 1) * SL, a = ((trW + u0) ^ (unsigned)(d << 6)) + s * 4096;
            if (d == 0) xb[(c + 1) & 1][s] = lds_b128((rdX + u0) ^ (unsigned)(s << 5));
            wa_[(c + 1) & 1][4 * s + d] = lds_tr2(a, a + 1024);
          }
        }
      SBAR();
    }
#pragma unroll
    for (int d = 0; d < 4; ++d) {
#pragma unroll
      for (int r = 0; r < 16; ++r) qa[d][r] *= CSC;
      qf[2 * d] = pack8(qa[d], 0); qf[2 * d + 1] = pack8(qa[d], 1);
    }
#pragma unroll
    for (int f = 0; f < 8; ++f) *(LAS bf16x8*)(qx + (f >> 2) * U + (f & 3) * 1024) = qf[f];
    BAR();
#pragma unroll
    for (int i = 0; i < 16; ++i) kf[i] = rdk(U, i);
  } else {
    issueQP(0); issueQP(1);
    WAIT_BAR(8); issueQP(2);
#pragma unroll
    for (int c = 0; c < 5; ++c) { WAIT_BAR(8); issueQP(c + 3); }
    issueK(0, U); issueK(1, 0);
    WAIT_BAR(16);
    WAIT_BAR(8);
    const Hs hs = hs_make(ws, bid, lane);
    unsigned rq;
    asm volatile("global_load_dword %0, %1, off sc1" : "=v"(rq) : "v"(hs.base + hs.mine + 32u) : "memory");
    issueK(2, SL); issueV(0, SL + U);
    asm volatile("s_waitcnt vmcnt(8) lgkmcnt(0)\n\ts_barrier" : "+v"(rq) :: "memory");
    if (wid == 4 && hs.act) __hip_atomic_store(hs.base + hs.peer + 64u, rq, RLX_AGENT);
#pragma unroll
    for (int f = 0; f < 8; ++f) qf[f] = lds_b128(qx + (f >> 2) * U + (f & 3) * 1024);
#pragma unroll
    for (int i = 0; i < 16; ++i) kf[i] = rdk(0, i);
    issueK(3, 2u * SL); issueV(1, 2u * SL + U);
    WAIT_BAR(8);
  }
  STAMP(1);

  f32x16 o[4] = {};
  float m_run = 0.f, l_run = 0.f;
  f32x16 negm = {};
  bf16x8 pf[4], vf[16];
  auto vfrag = [&](unsigned av, int ks, int d) { const unsigned a = (av ^ (unsigned)(d << 6)) + (32 * (ks & 1) + 8 * (ks >> 1)) * 256; return lds_tr2(a, a + 4096); };
  auto scores_slow = [&](bool first, float& ps) {
    f32x16 s0 = {}, s1 = {};
#pragma unroll
    for (int s = 0; s < 8; ++s) { s0 = MFMA(kf[s], qf[s], s0); s1 = MFMA(kf[8 + s], qf[s], s1); }
    float mx = fmaxf(s0[0], s1[0]);
#pragma unroll
    for (int r = 1; r < 16; ++r) mx = fmaxf(fmaxf(mx, s0[r]), s1[r]);
    mx = xhalf_max(mx);
    const float m_new = first ? mx : fmaxf(mx, m_run);
    if (!first) {
      const float alpha = __builtin_amdgcn_exp2f(m_run - m_new);
      l_run *= alpha;
#pragma unroll
      for (int d = 0; d < 4; ++d)
#pragma unroll
        for (int r = 0; r < 16; ++r) o[d][r] *= alpha;
    }
    m_run = m_new;
    ps = 0.f;
#pragma unroll
    for (int r = 0; r < 16; ++r) { negm[r] = -m_new; s0[r] = __builtin_amdgcn_exp2f(s0[r] - m_new); s1[r] = __builtin_amdgcn_exp2f(s1[r] - m_new); ps += s0[r] + s1[r]; }
    pf[0] = pack8(s0, 0); pf[1] = pack8(s0, 1); pf[2] = pack8(s1, 0); pf[3] = pack8(s1, 1);
  };
  auto scores = [&](bool first, auto inloop) {
    float ps = 0.f;
    PRIO(2);
    if (!first) {
      SBAR();
      f32x16 s0 = MFMA(kf[0], qf[0], negm);
#pragma unroll
      for (int s = 1; s < 8; ++s) s0 = MFMA(kf[s], qf[s], s0);
      SBAR();
      f32x16 s1 = MFMA(kf[8], qf[0], negm);
      SBAR();
      float pa = 0.f, pb = 0.f;
      unsigned w[16];
#define SMSTEP(S, e, wi) do { S[e] = __builtin_amdgcn_exp2f(S[e]); S[e + 1] = __builtin_amdgcn_exp2f(S[e + 1]); pa += S[e]; pb += S[e + 1]; w[wi] = cvtpk(S[e], S[e + 1]); PIN(pa); PIN(pb); PIN(w[wi]); } while (0)
#pragma unroll
      for (int s = 1; s < 8; ++s) {
        s1 = MFMA(kf[8 + s], qf[s], s1);
        SMSTEP(s0, 2 * (s - 1), s - 1);
        SBAR();
      }
      SMSTEP(s0, 14, 7);
      SBAR();
#pragma unroll
      for (int i = 0; i < 8; ++i) { SMSTEP(s1, 2 * i, 8 + i); SBAR(); }
#undef SMSTEP
      ps = pa + pb;
#pragma unroll
      for (int i = 0; i < 4; ++i) pf[i] = __builtin_bit_cast(bf16x8, (u32x4){w[4 * i], w[4 * i + 1], w[4 * i + 2], w[4 * i + 3]});
    }
    if (__builtin_expect(first || __any(!(ps <= PSLIM)), 0)) scores_slow(first, ps);
    l_run += ps;
    PIN(pf[0]); PIN(pf[1]); PIN(pf[2]); PIN(pf[3]); PIN(l_run); if (decltype(inloop)::value) PIN(negm);
    PRIO(PVPRIO);
  };

  __builtin_amdgcn_s_waitcnt(0xC07F);
  unsigned sp = 1u + g;
  for (int it = 0; it < 15; ++it) {
    const unsigned p = 2u * (unsigned)it + g + 1u;
    scores(it == 0, std::true_type{});
    WAIT_BAR(0);
    const unsigned ni = p + 2u, sn = sp >= 3u ? sp - 3u : sp + 2u, ns = sn * SL;
    const unsigned av = trV + sp * SL;
#pragma unroll
    for (int i = 0; i < 16; ++i) vf[i] = vfrag(av, i >> 2, i & 3);
    SBAR();
    if (ni <= 30u) issueK(ni + 1u, ns);
    SBAR();
#pragma unroll
    for (int i = 0; i < 8; ++i) kf[i] = rdk(sp * SL, i);
    SBAR();
#pragma unroll
    for (int i = 0; i < 8; ++i) o[i & 3] = MFMA(vf[i], pf[i >> 2], o[i & 3]);
    SBAR();
#pragma unroll
    for (int i = 8; i < 16; ++i) { o[i & 3] = MFMA(vf[i], pf[i >> 2], o[i & 3]); if (i < 12) { kf[2 * i - 8] = rdk(sp * SL, 2 * i - 8); kf[2 * i - 7] = rdk(sp * SL, 2 * i - 7); } SBAR(); }
    issueV(ni - 1u, ns + U);
    if (it == 14) issueWo(g, 3u + g);
    sp = sn;
    BAR();
  }
  scores(false, std::false_type{});
  unsigned ln; asm volatile("v_mbcnt_lo_u32_b32 %0, -1, 0\n\tv_mbcnt_hi_u32_b32 %0, -1, %0" : "=v"(ln));
  const unsigned x0 = lds0 + w4 * 8192u + ln * 16u;
  l_run = xhalf_sum(l_run);
  const unsigned stats = lds0 + 2u * SL + (ln & 31u) * 8u;
  if (ln < 32u) *(LAS f32x2*)(stats + wid * 256u) = (f32x2){m_run, l_run};
  const Hs hs = hs_make(ws, bid, ln);
  unsigned hs_r, hs_q;
  asm volatile("global_load_dword %0, %2, off offset:256 sc1\n\tglobal_load_dword %1, %3, off offset:128 sc1" : "=&v"(hs_r), "=&v"(hs_q) : "v"(hs.base + hs.mine), "v"(hs.base + hs.peer) : "memory");
  bf16x8 of0[4];
  {
    WAIT_BAR(10);
    STAMP(2);
    const unsigned av = trV + (1u + g) * SL;
#pragma unroll
    for (int ks = 0; ks < 4; ++ks) { vf[4 * ks + 2] = vfrag(av, ks, 2); vf[4 * ks + 3] = vfrag(av, ks, 3); }
    SBAR();
#pragma unroll
    for (int ks = 0; ks < 4; ++ks) { vf[4 * ks] = vfrag(av, ks, 0); vf[4 * ks + 1] = vfrag(av, ks, 1); }
    SBAR();
#pragma unroll
    for (int ks = 0; ks < 4; ++ks) { o[2] = MFMA(vf[4 * ks + 2], pf[ks], o[2]); o[3] = MFMA(vf[4 * ks + 3], pf[ks], o[3]); }
    SBAR();
#pragma unroll
    for (int ks = 0; ks < 2; ++ks) { o[0] = MFMA(vf[4 * ks], pf[ks], o[0]); o[1] = MFMA(vf[4 * ks + 1], pf[ks], o[1]); }
    SBAR();
    const unsigned xo = x0 + g * SL, xi = x0 + (1u - g) * SL;
#pragma unroll
    for (int q = 0; q < 4; ++q) { lds_st4(xo + q * 1024, o[2], q); lds_st4(xo + 4096 + q * 1024, o[3], q); }
    SBAR();
#pragma unroll
    for (int ks = 2; ks < 4; ++ks) { o[0] = MFMA(vf[4 * ks], pf[ks], o[0]); o[1] = MFMA(vf[4 * ks + 1], pf[ks], o[1]); }
    SBAR();
    if (g == 0) BAR();
    const f32x2 st = *(const LAS f32x2*)(stats + (wid ^ 4u) * 256u);
    const float ms = fmaxf(m_run, st[0]);
    const float e_me = __builtin_amdgcn_exp2f(m_run - ms), e_ot = __builtin_amdgcn_exp2f(st[0] - ms);
    const float inv = 1.f / (l_run * e_me + st[1] * e_ot);
    float a_me = e_me * inv, a_ot = e_ot * inv;
    PIN(a_me); PIN(a_ot);
    asm volatile("s_waitcnt vmcnt(0) lgkmcnt(0)\n\ts_barrier" : "+v"(hs_r), "+v"(hs_q) :: "memory");
    f32x4 w0[4], w1[4];
#pragma unroll
    for (int q = 0; q < 4; ++q) { w0[q] = lds_f4(xi + q * 1024); w1[q] = lds_f4(xi + 4096 + q * 1024); }
#pragma unroll
    for (int q = 0; q < 4; ++q)
#pragma unroll
      for (int e = 0; e < 4; ++e) { o[0][4 * q + e] = fmaf(w0[q][e], a_ot, o[0][4 * q + e] * a_me); o[1][4 * q + e] = fmaf(w1[q][e], a_ot, o[1][4 * q + e] * a_me); }
    of0[0] = pack8(o[0], 0); of0[1] = pack8(o[0], 1); of0[2] = pack8(o[1], 0); of0[3] = pack8(o[1], 1);
  }
  PRIO(0);
  const __amdgpu_buffer_rsrc_t srdO = __builtin_amdgcn_make_buffer_rsrc((void*)(ws + WS_O + (size_t)(b * NH + hg * 4u) * 32768u), (short)0, 4 * 32768, 0x00020000);
#pragma unroll
  for (int s = 0; s < 4; ++s) __builtin_amdgcn_raw_buffer_store_b128(__builtin_bit_cast(u32x4, of0[s]), srdO, j * 32768u + wid * 4096u + (unsigned)s * 1024u + ln * 16u, 0, 16);
  STAMP(3);
  issueWo(2u + g, 1u - g);
  f32x16 c[4] = {};
  const unsigned trW2 = trV - U + (ln >> 5) * 1024u;
  bf16x8 wa[2][8];
  auto rdw = [&](int buf, unsigned slot, int half) {
    const unsigned aw = trW2 + slot * SL + g * U;
#pragma unroll
    for (int s = 0; s < 2; ++s)
#pragma unroll
      for (int nb = 0; nb < 4; ++nb) { const unsigned a = (aw ^ (unsigned)(nb << 6)) + (2 * half + s) * 4096; wa[buf][4 * s + nb] = lds_tr2(a, a + 1024); }
  };
  auto mmw = [&](int buf, const bf16x8& f0, const bf16x8& f1) {
#pragma unroll
    for (int nb = 0; nb < 4; ++nb) c[nb] = MFMA(wa[buf][nb], f0, c[nb]);
#pragma unroll
    for (int nb = 0; nb < 4; ++nb) c[nb] = MFMA(wa[buf][4 + nb], f1, c[nb]);
  };
  rdw(0, 3, 0); rdw(1, 3, 1); SBAR();
  mmw(0, of0[0], of0[1]); SBAR();
  rdw(0, 4, 0); SBAR();
  mmw(1, of0[2], of0[3]); SBAR();
  rdw(1, 4, 1);
  PIN(c[0]); PIN(c[1]); PIN(c[2]); PIN(c[3]);
  WAIT_BAR(0);
  STAMP(4);
  if (wid == 0) {
    if (!__all(hs_r == hs_q || !hs.act)) hs_wait_armed(hs);
    if (ln < 4u) __hip_atomic_store(hs.base + hs.peer, 1u, RLX_AGENT);
    hs_wait_flags(hs, ln < 4u);
  }
  BAR();
  STAMP(5);
  {
    bf16x8 of[3][4];
#pragma unroll
    for (int cc = 1; cc < 4; ++cc)
#pragma unroll
      for (int s = 0; s < 4; ++s)
        of[cc - 1][s] = __builtin_bit_cast(bf16x8, __builtin_amdgcn_raw_buffer_load_b128(srdO, wid * 4096u + (unsigned)s * 1024u + ln * 16u, ((j + (unsigned)cc) & 3u) * 32768u, 16));
    SBAR();
    mmw(0, of[0][0], of[0][1]); SBAR(); rdw(0, 1, 0); SBAR();
    mmw(1, of[0][2], of[0][3]); SBAR(); rdw(1, 1, 1); SBAR();
    mmw(0, of[1][0], of[1][1]); SBAR(); rdw(0, 0, 0); SBAR();
    mmw(1, of[1][2], of[1][3]); SBAR(); rdw(1, 0, 1); SBAR();
    mmw(0, of[2][0], of[2][1]); SBAR();
    mmw(1, of[2][2], of[2][3]);
  }
  STAMP(6);
  {
#pragma unroll
    for (int q = 0; q < 4; ++q) { lds_st4(x0 + (2u + g) * SL + q * 1024, c[2], q); lds_st4(x0 + (2u + g) * SL + 4096 + q * 1024, c[3], q); }
    BAR();
    const unsigned ra = x0 + (3u - g) * SL;
    const __amdgpu_buffer_rsrc_t srdP = __builtin_amdgcn_make_buffer_rsrc((void*)(ws + WS_P + (size_t)(b * 4u + j) * 524288u), (short)0, 524288, 0x00020000);
#pragma unroll
    for (int q = 0; q < 4; ++q) {
      const f32x4 va = lds_f4(ra + q * 1024), vb = lds_f4(ra + 4096 + q * 1024);
      const f32x4 sa = {c[0][4 * q] + va[0], c[0][4 * q + 1] + va[1], c[0][4 * q + 2] + va[2], c[0][4 * q + 3] + va[3]};
      const f32x4 sb = {c[1][4 * q] + vb[0], c[1][4 * q + 1] + vb[1], c[1][4 * q + 2] + vb[2], c[1][4 * q + 3] + vb[3]};
      __builtin_amdgcn_raw_buffer_store_b128(__builtin_bit_cast(u32x4, sa), srdP, hg * 65536u + wid * 8192u + (unsigned)q * 1024u + ln * 16u, 0, 16);
      __builtin_amdgcn_raw_buffer_store_b128(__builtin_bit_cast(u32x4, sb), srdP, hg * 65536u + wid * 8192u + (unsigned)(4 + q) * 1024u + ln * 16u, 0, 16);
    }
    STAMP(7);
    WAIT_BAR(0);
    STAMP(8);
    if (wid == 0) {
      if (hs.act && ln >= 8u) __hip_atomic_store(hs.base + hs.peer, 1u, RLX_AGENT);
      hs_wait_flags(hs, hs.act && ln >= 8u);
    }
    BAR();
    STAMP(9);
    f32x4 pv[8];
#pragma unroll
    for (int u = 0; u < 8; ++u)
      pv[u] = __builtin_bit_cast(f32x4, __builtin_amdgcn_raw_buffer_load_b128(srdP, hg * 8192u + wid * 1024u + ln * 16u, (unsigned)u * 65536u, 16));
    f32x4 acc = pv[0];
#pragma unroll
    for (int u = 1; u < 8; ++u) acc += pv[u];
    const unsigned qrow = (hg & 3u) * 32u + (ln & 31u), n = j * 128u + ((hg >> 2) * 2u + g) * 32u + (w4 >> 1) * 16u + (ln >> 5) * 8u + (w4 & 1u) * 4u;
    *(u32x2*)(out + ((size_t)b * SQ + qrow) * DM + n) = (u32x2){cvtpk(acc[0], acc[1]), cvtpk(acc[2], acc[3])};
    STAMP(10);
  }
}

extern "C" void kernel_launch(void* const* d_in, const int* in_sizes, int n_in, void* d_out, int out_size, void* d_ws, size_t ws_size, hipStream_t stream) {
  (void)in_sizes; (void)n_in; (void)out_size; (void)ws_size;
  (void)hipFuncSetAttribute((const void*)mha_fused, hipFuncAttributeMaxDynamicSharedMemorySize, (int)LDS_BYTES);
  hipLaunchKernelGGL(mha_fused, dim3(256), dim3(512), LDS_BYTES, stream, (const unsigned short*)d_in[0], (const unsigned short*)d_in[1], (const unsigned short*)d_in[2],
                     (const unsigned short*)d_in[3], (const unsigned short*)d_in[4], (unsigned short*)d_out, (char*)d_ws);
}
```

```cpp
#define CODEPF 16
#include <hip/hip_runtime.h>
#include <hip/hip_bf16.h>
#include <stdint.h>
#include <type_traits>

typedef __bf16 bf16x8 __attribute__((ext_vector_type(8)));
typedef __bf16 bf16x2 __attribute__((ext_vector_type(2)));
typedef short s16x4 __attribute__((ext_vector_type(4)));
typedef float f32x16 __attribute__((ext_vector_type(16)));
typedef float f32x4 __attribute__((ext_vector_type(4)));
typedef float f32x2 __attribute__((ext_vector_type(2)));
typedef unsigned u32x4 __attribute__((ext_vector_type(4)));
typedef unsigned u32x2 __attribute__((ext_vector_type(2)));
#define LAS __attribute__((address_space(3)))
#define GAS __attribute__((address_space(1)))
#define DI __device__ __forceinline__
#define MFMA(a, b, c) __builtin_amdgcn_mfma_f32_32x32x16_bf16((a), (b), (c), 0, 0, 0)
#define RLX_AGENT __ATOMIC_RELAXED, __HIP_MEMORY_SCOPE_AGENT
#define PIN(x) asm volatile("" : "+v"(x))
#define SBAR() __builtin_amdgcn_sched_barrier(0)
#define PRIO(n) __builtin_amdgcn_s_setprio(n)

constexpr int SQ = 128, DM = 512, NH = 32, SK = 2048, DH = 128;
constexpr unsigned U = 16384u, SL = 32768u;
constexpr unsigned LDS_BYTES = 5 * SL;
#define PVPRIO 0
#define PSLIM 8192.0f
constexpr float CSC = 0.088388347648318447f * 1.4426950408889634f;
constexpr size_t WS_FLAG = 0;
constexpr unsigned FB = 128;
constexpr size_t WS_O = 1 << 20;
constexpr size_t WS_P = WS_O + (8u << 20);
constexpr size_t WS_END = WS_P + (16u << 20);

DI unsigned rfl(unsigned x) { return __builtin_amdgcn_readfirstlane(x); }
DI unsigned cvtpk(float lo, float hi) { f32x2 v = {lo, hi}; bf16x2 b = __builtin_convertvector(v, bf16x2); return __builtin_bit_cast(unsigned, b); }
DI bf16x8 pack8(const f32x16& a, int s) {
  u32x4 w = {cvtpk(a[8 * s], a[8 * s + 1]), cvtpk(a[8 * s + 2], a[8 * s + 3]), cvtpk(a[8 * s + 4], a[8 * s + 5]), cvtpk(a[8 * s + 6], a[8 * s + 7])};
  return __builtin_bit_cast(bf16x8, w);
}
DI bf16x8 lds_b128(unsigned a) { return *(const LAS bf16x8*)a; }
DI f32x4 lds_f4(unsigned a) { return *(const LAS f32x4*)a; }
DI void lds_st4(unsigned a, const f32x16& v, int q) { *(LAS f32x4*)a = (f32x4){v[4 * q], v[4 * q + 1], v[4 * q + 2], v[4 * q + 3]}; }
DI s16x4 lds_tr(unsigned a) { return __builtin_amdgcn_ds_read_tr16_b64_v4i16((LAS s16x4*)a); }
DI bf16x8 lds_tr2(unsigned a0, unsigned a1) {
  s16x4 lo = lds_tr(a0), hi = lds_tr(a1);
  return __builtin_bit_cast(bf16x8, __builtin_shufflevector(lo, hi, 0, 1, 2, 3, 4, 5, 6, 7));
}
DI void dma4_kv(unsigned m0v, unsigned voff, __amdgpu_buffer_rsrc_t srd, unsigned soff) {
  unsigned keep;
  asm volatile("s_mov_b32 %0, m0\n\ts_mov_b32 m0, %1\n\ts_nop 3\n\t"
               "buffer_load_dwordx4 %2, %3, %4 offen nt lds\n\t"
               "buffer_load_dwordx4 %2, %3, %4 offen offset:1024 nt lds\n\t"
               "buffer_load_dwordx4 %2, %3, %4 offen offset:2048 nt lds\n\t"
               "buffer_load_dwordx4 %2, %3, %4 offen offset:3072 nt lds\n\t"
               "s_mov_b32 m0, %0"
               : "=&s"(keep) : "s"(rfl(m0v)), "v"(voff), "s"(srd), "s"(rfl(soff)) : "memory");
}
DI void dma4_def(unsigned m0v, unsigned voff, __amdgpu_buffer_rsrc_t srd, unsigned soff) {
  unsigned keep;
  asm volatile("s_mov_b32 %0, m0\n\ts_mov_b32 m0, %1\n\ts_nop 3\n\t"
               "buffer_load_dwordx4 %2, %3, %4 offen lds\n\t"
               "buffer_load_dwordx4 %2, %3, %4 offen offset:1024 lds\n\t"
               "buffer_load_dwordx4 %2, %3, %4 offen offset:2048 lds\n\t"
               "buffer_load_dwordx4 %2, %3, %4 offen offset:3072 lds\n\t"
               "s_mov_b32 m0, %0"
               : "=&s"(keep) : "s"(rfl(m0v)), "v"(voff), "s"(srd), "s"(rfl(soff)) : "memory");
}
#define DMA_K dma4_kv
#define DMA_V dma4_kv
DI void dma1(unsigned m0v, unsigned voff, __amdgpu_buffer_rsrc_t srd, unsigned soff) {
  unsigned keep;
  asm volatile("s_mov_b32 %0, m0\n\ts_mov_b32 m0, %1\n\ts_nop 3\n\t"
               "buffer_load_dwordx4 %2, %3, %4 offen lds\n\t"
               "s_mov_b32 m0, %0"
               : "=&s"(keep) : "s"(rfl(m0v)), "v"(voff), "s"(srd), "s"(rfl(soff)) : "memory");
}
DI void dma1nt(unsigned m0v, unsigned voff, __amdgpu_buffer_rsrc_t srd, unsigned soff) {
  unsigned keep;
  asm volatile("s_mov_b32 %0, m0\n\ts_mov_b32 m0, %1\n\ts_nop 3\n\t"
               "buffer_load_dwordx4 %2, %3, %4 offen nt lds\n\t"
               "s_mov_b32 m0, %0"
               : "=&s"(keep) : "s"(rfl(m0v)), "v"(voff), "s"(srd), "s"(rfl(soff)) : "memory");
}
#define DMAWQ dma1nt
#define DMAX dma1
#define WAIT_BAR(N) asm volatile("s_waitcnt vmcnt(" #N ") lgkmcnt(0)\n\ts_barrier" ::: "memory")
#define BAR() asm volatile("s_waitcnt lgkmcnt(0)\n\ts_barrier" ::: "memory")
#define WAITVM(N) asm volatile("s_waitcnt vmcnt(" #N ")" ::: "memory")

DI float xhalf_max(float v) { auto r = __builtin_amdgcn_permlane32_swap(__float_as_uint(v), __float_as_uint(v), false, false); return fmaxf(__uint_as_float(r[0]), __uint_as_float(r[1])); }
DI float xhalf_sum(float v) { auto r = __builtin_amdgcn_permlane32_swap(__float_as_uint(v), __float_as_uint(v), false, false); return __uint_as_float(r[0]) + __uint_as_float(r[1]); }

struct Hs { GAS unsigned* base; unsigned mine, peer; bool act; };
DI Hs hs_make(char* ws, unsigned bid, unsigned L) {
  Hs h; h.base = (GAS unsigned*)(ws + WS_FLAG);
  const bool h1 = L < 4u; h.act = h1 || (L >= 8u && L < 16u);
  const unsigned pb = h1 ? ((bid & ~24u) | (L << 3)) : ((bid & ~7u) | (L & 7u));
  h.mine = bid * FB + (L & 15u); h.peer = pb * FB + (h1 ? ((bid >> 3) & 3u) : 8u + (bid & 7u));
  return h;
}
DI void hs_echo(const Hs& h) { if (h.act) __hip_atomic_store(h.base + h.peer + 64u, __hip_atomic_load(h.base + h.mine + 32u, RLX_AGENT), RLX_AGENT); }
DI void hs_wait_armed(const Hs& h) {
  for (unsigned spins = 0; spins < (1u << 20); ++spins) {
    hs_echo(h);
    bool ok = true;
    if (h.act) ok = __hip_atomic_load(h.base + h.mine + 64u, RLX_AGENT) == __hip_atomic_load(h.base + h.peer + 32u, RLX_AGENT);
    if (__all(ok)) break;
    __builtin_amdgcn_s_sleep(4);
  }
}
DI void hs_wait_flags(const Hs& h, bool sel) {
  for (unsigned spins = 0; spins < (1u << 20); ++spins) {
    unsigned v = 1u;
    if (sel) v = __hip_atomic_load(h.base + h.mine, RLX_AGENT);
    if (__all(v != 0u)) break;
    if ((spins & 31u) == 31u) hs_echo(h);
    __builtin_amdgcn_s_sleep(1);
  }
  asm volatile("" ::: "memory");
}

#define STAMP(i) do { } while (0)

__global__ __launch_bounds__(512, 2) void mha_fused(const unsigned short* __restrict__ x, const unsigned short* __restrict__ kc, const unsigned short* __restrict__ vc,
                                                    const unsigned short* __restrict__ wq, const unsigned short* __restrict__ wo, unsigned short* __restrict__ out, char* __restrict__ ws) {
  extern __shared__ __attribute__((aligned(16))) char lds[];
  const unsigned tid = threadIdx.x, lane = tid & 63u, r32 = lane & 31u, hi = lane >> 5;
  const unsigned wid = rfl(tid >> 6), g = wid >> 2, w4 = wid & 3u;
  const unsigned bid = blockIdx.x, hg = bid & 7u, b = bid >> 5, j = (bid >> 3) & 3u, h = hg * 4u + j;
  const unsigned lds0 = (unsigned)(uintptr_t)lds;
  STAMP(0);
  asm volatile("" :: "s"(out), "s"(ws), "s"(wo));

  const __amdgpu_buffer_rsrc_t srdK = __builtin_amdgcn_make_buffer_rsrc((void*)(kc + (size_t)(b * NH + h) * SK * DH), (short)0, SK * DH * 2, 0x00020000);
  const __amdgpu_buffer_rsrc_t srdV = __builtin_amdgcn_make_buffer_rsrc((void*)(vc + (size_t)(b * NH + h) * SK * DH), (short)0, SK * DH * 2, 0x00020000);
  const __amdgpu_buffer_rsrc_t srdX = __builtin_amdgcn_make_buffer_rsrc((void*)(x + (size_t)b * SQ * DM), (short)0, SQ * DM * 2, 0x00020000);
  const __amdgpu_buffer_rsrc_t srdWq = __builtin_amdgcn_make_buffer_rsrc((void*)wq, (short)0, DM * NH * DH * 2, 0x00020000);
  const __amdgpu_buffer_rsrc_t srdWo = __builtin_amdgcn_make_buffer_rsrc((void*)(wo + (size_t)hg * 512 * DM), (short)0, 512 * DM * 2, 0x00020000);

  const unsigned l4 = lane >> 4, l15 = lane & 15u;
  const unsigned voffK = w4 * 4096u + l4 * 256u + ((l15 ^ (l4 | (w4 << 2))) << 4);
  const unsigned voffV = w4 * 4096u + l4 * 256u + ((l15 ^ (l4 << 2)) << 4);
  const unsigned voffWq = l4 * 8192u + ((l15 ^ (l4 << 2)) << 4);
  const unsigned voffWo = l4 * 1024u + ((l15 ^ (l4 << 2)) << 4);
  const unsigned voffX = (lane >> 3) * 1024u + (((lane & 7u) ^ (((lane >> 4) & 1u) | (w4 << 1))) << 4);

  auto issueK = [&](unsigned t, unsigned off) { DMA_K(lds0 + off + w4 * 4096u, voffK, srdK, t * U); };
  auto issueV = [&](unsigned t, unsigned off) { DMA_V(lds0 + off + w4 * 4096u, voffV, srdV, t * U); };
  auto issueWo = [&](unsigned cc, unsigned slot) {
    const unsigned c = (j + cc) & 3u;
#pragma unroll
    for (int q = 0; q < 8; ++q) dma1(lds0 + slot * SL + (w4 * 8u + q) * 1024u, voffWo, srdWo, (c * 128u + w4 * 32u + q * 4u) * 1024u + j * 256u);
  };
#define ROT ((b + 2u * j) & 7u)
  const unsigned rot = ROT;
  auto qslot = [](int c) { return (c % 3 == 0) ? 3u : (c % 3 == 1) ? 4u : 2u; };
  auto issueQP = [&](int c) {
    const unsigned kcn = ((unsigned)c + rot) & 7u, sb = lds0 + qslot(c) * SL;
#pragma unroll
    for (int q = 0; q < 4; ++q) {
      DMAWQ(sb + (w4 * 4u + q) * 1024u, voffWq, srdWq, (kcn * 64u + w4 * 16u + q * 4u) * 8192u + h * 256u);
      DMAX(sb + U + (q * 4u + w4) * 1024u, voffX, srdX, (q * 4u + w4) * 8192u + kcn * 128u);
    }
  };
  const unsigned blk = (lane >> 4) & 1u, q4 = l15 >> 2, p4 = lane & 3u, pp = ((p4 & 1u) << 1) | (p4 >> 1);
  const unsigned trV = lds0 + U + (4u * hi + q4) * 256u + ((q4 ^ (2u * g)) << 6) + blk * 32u + pp * 8u;
  const unsigned rdK = lds0 + ((r32 & 7u) + 16u * (r32 >> 3)) * 256u + ((hi ^ ((r32 & 3u) | ((r32 >> 3) << 2))) << 4);
  const unsigned qx = lds0 + 3u * SL + w4 * 4096u + lane * 16u;

  bf16x8 qf[8], kf[16];
  auto rdk = [&](unsigned off, int i) { return lds_b128(((rdK + off) ^ (unsigned)((i & 7) << 5)) + (i >> 3) * 2048); };
  if (g == 0) {
    const unsigned trW = lds0 + (8u * hi + q4) * 256u + (q4 << 6) + blk * 32u + pp * 8u;
    const unsigned rdX = lds0 + U + (w4 * 32u + r32) * 128u + ((hi ^ (((r32 >> 1) & 1u) | ((r32 >> 3) << 1))) << 4);
    f32x16 qa[4] = {};
    bf16x8 xb[2][4], wa_[2][16];
    auto rdq = [&](int c, int s) {
      const unsigned u0 = qslot(c) * SL;
      xb[c & 1][s] = lds_b128((rdX + u0) ^ (unsigned)(s << 5));
#pragma unroll
      for (int d = 0; d < 4; ++d) { const unsigned a = ((trW + u0) ^ (unsigned)(d << 6)) + s * 4096; wa_[c & 1][4 * s + d] = lds_tr2(a, a + 1024); }
    };
    {
      unsigned long long pc; asm volatile("s_getpc_b64 %0" : "=s"(pc));
      const __amdgpu_buffer_rsrc_t srdC = __builtin_amdgcn_make_buffer_rsrc((void*)(pc & ~255ull), (short)0, CODEPF * 1024, 0x00020000);
      u32x4 acc = {};
      if (wid == 1) acc = __builtin_amdgcn_raw_buffer_load_b128(srdC, lane * 16u + ((bid >> 3) % CODEPF) * 1024u, 0, 0);
      asm volatile("" :: "v"(acc));
    }
    if (wid == 0) {
      const Hs hs = hs_make(ws, bid, lane);
      if (hs.act) {
        const unsigned r0 = __hip_atomic_load(hs.base + hs.mine + 64u, RLX_AGENT);
        __hip_atomic_store(hs.base + hs.mine, 0u, RLX_AGENT);
        __hip_atomic_store(hs.base + hs.peer + 32u, r0 + 1u, RLX_AGENT);
      }
      WAITVM(0);
    }
    BAR();
#pragma unroll
    for (int s = 0; s < 4; ++s) rdq(0, s);
#pragma unroll
    for (int c = 0; c < 8; ++c) {
      if (c < 7) BAR();
#pragma unroll
      for (int s = 0; s < 4; ++s)
#pragma unroll
        for (int d = 0; d < 4; ++d) {
          SBAR();
          qa[d] = MFMA(wa_[c & 1][4 * s + d], xb[c & 1][s], qa[d]);
          SBAR();
          if (c < 7) {
            const unsigned u0 = qslot(c + 1) * SL, a = ((trW + u0) ^ (unsigned)(d << 6)) + s * 4096;
            if (d == 0) xb[(c + 1) & 1][s] = lds_b128((rdX + u0) ^ (unsigned)(s << 5));
            wa_[(c + 1) & 1][4 * s + d] = lds_tr2(a, a + 1024);
          }
        }
      SBAR();
    }
#pragma unroll
    for (int d = 0; d < 4; ++d) {
#pragma unroll
      for (int r = 0; r < 16; ++r) qa[d][r] *= CSC;
      qf[2 * d] = pack8(qa[d], 0); qf[2 * d + 1] = pack8(qa[d], 1);
    }
#pragma unroll
    for (int f = 0; f < 8; ++f) *(LAS bf16x8*)(qx + (f >> 2) * U + (f & 3) * 1024) = qf[f];
    BAR();
#pragma unroll
    for (int i = 0; i < 16; ++i) kf[i] = rdk(U, i);
  } else {
    issueQP(0); issueQP(1);
    WAIT_BAR(8); issueQP(2);
#pragma unroll
    for (int c = 0; c < 5; ++c) { WAIT_BAR(8); issueQP(c + 3); }
    issueK(0, U); issueK(1, 0);
    WAIT_BAR(16);
    WAIT_BAR(8);
    const Hs hs = hs_make(ws, bid, lane);
    unsigned rq;
    asm volatile("global_load_dword %0, %1, off sc1" : "=v"(rq) : "v"(hs.base + hs.mine + 32u) : "memory");
    issueK(2, SL); issueV(0, SL + U);
    asm volatile("s_waitcnt vmcnt(8) lgkmcnt(0)\n\ts_barrier" : "+v"(rq) :: "memory");
    if (wid == 4 && hs.act) __hip_atomic_store(hs.base + hs.peer + 64u, rq, RLX_AGENT);
#pragma unroll
    for (int f = 0; f < 8; ++f) qf[f] = lds_b128(qx + (f >> 2) * U + (f & 3) * 1024);
#pragma unroll
    for (int i = 0; i < 16; ++i) kf[i] = rdk(0, i);
    issueK(3, 2u * SL); issueV(1, 2u * SL + U);
    WAIT_BAR(8);
  }
  STAMP(1);

  f32x16 o[4] = {};
  float m_run = 0.f, l_run = 0.f;
  f32x16 negm = {};
  bf16x8 pf[4], vf[16];
  auto vfrag = [&](unsigned av, int ks, int d) { const unsigned a = (av ^ (unsigned)(d << 6)) + (32 * (ks & 1) + 8 * (ks >> 1)) * 256; return lds_tr2(a, a + 4096); };
  auto scores_slow = [&](bool first, float& ps) {
    f32x16 s0 = {}, s1 = {};
#pragma unroll
    for (int s = 0; s < 8; ++s) { s0 = MFMA(kf[s], qf[s], s0); s1 = MFMA(kf[8 + s], qf[s], s1); }
    float mx = fmaxf(s0[0], s1[0]);
#pragma unroll
    for (int r = 1; r < 16; ++r) mx = fmaxf(fmaxf(mx, s0[r]), s1[r]);
    mx = xhalf_max(mx);
    const float m_new = first ? mx : fmaxf(mx, m_run);
    if (!first) {
      const float alpha = __builtin_amdgcn_exp2f(m_run - m_new);
      l_run *= alpha;
#pragma unroll
      for (int d = 0; d < 4; ++d)
#pragma unroll
        for (int r = 0; r < 16; ++r) o[d][r] *= alpha;
    }
    m_run = m_new;
    ps = 0.f;
#pragma unroll
    for (int r = 0; r < 16; ++r) { negm[r] = -m_new; s0[r] = __builtin_amdgcn_exp2f(s0[r] - m_new); s1[r] = __builtin_amdgcn_exp2f(s1[r] - m_new); ps += s0[r] + s1[r]; }
    pf[0] = pack8(s0, 0); pf[1] = pack8(s0, 1); pf[2] = pack8(s1, 0); pf[3] = pack8(s1, 1);
  };
  auto scores = [&](bool first, auto inloop) {
    float ps = 0.f;
    PRIO(2);
    if (!first) {
      SBAR();
      f32x16 s0 = MFMA(kf[0], qf[0], negm);
#pragma unroll
      for (int s = 1; s < 8; ++s) s0 = MFMA(kf[s], qf[s], s0);
      SBAR();
      f32x16 s1 = MFMA(kf[8], qf[0], negm);
      SBAR();
      float pa = 0.f, pb = 0.f;
      unsigned w[16];
#define SMSTEP(S, e, wi) do { S[e] = __builtin_amdgcn_exp2f(S[e]); S[e + 1] = __builtin_amdgcn_exp2f(S[e + 1]); pa += S[e]; pb += S[e + 1]; w[wi] = cvtpk(S[e], S[e + 1]); PIN(pa); PIN(pb); PIN(w[wi]); } while (0)
#pragma unroll
      for (int s = 1; s < 8; ++s) {
        s1 = MFMA(kf[8 + s], qf[s], s1);
        SMSTEP(s0, 2 * (s - 1), s - 1);
        SBAR();
      }
      SMSTEP(s0, 14, 7);
      SBAR();
#pragma unroll
      for (int i = 0; i < 8; ++i) { SMSTEP(s1, 2 * i, 8 + i); SBAR(); }
#undef SMSTEP
      ps = pa + pb;
#pragma unroll
      for (int i = 0; i < 4; ++i) pf[i] = __builtin_bit_cast(bf16x8, (u32x4){w[4 * i], w[4 * i + 1], w[4 * i + 2], w[4 * i + 3]});
    }
    if (__builtin_expect(first || __any(!(ps <= PSLIM)), 0)) scores_slow(first, ps);
    l_run += ps;
    PIN(pf[0]); PIN(pf[1]); PIN(pf[2]); PIN(pf[3]); PIN(l_run); if (decltype(inloop)::value) PIN(negm);
    PRIO(PVPRIO);
  };

  __builtin_amdgcn_s_waitcnt(0xC07F);
  unsigned sp = 1u + g;
  for (int it = 0; it < 15; ++it) {
    const unsigned p = 2u * (unsigned)it + g + 1u;
    scores(it == 0, std::true_type{});
    WAIT_BAR(0);
    const unsigned ni = p + 2u, sn = sp >= 3u ? sp - 3u : sp + 2u, ns = sn * SL;
    const unsigned av = trV + sp * SL;
#pragma unroll
    for (int i = 0; i < 16; ++i) vf[i] = vfrag(av, i >> 2, i & 3);
    SBAR();
    if (ni <= 30u) issueK(ni + 1u, ns);
    SBAR();
#pragma unroll
    for (int i = 0; i < 8; ++i) kf[i] = rdk(sp * SL, i);
    SBAR();
#pragma unroll
    for (int i = 0; i < 8; ++i) o[i & 3] = MFMA(vf[i], pf[i >> 2], o[i & 3]);
    SBAR();
#pragma unroll
    for (int i = 8; i < 16; ++i) { o[i & 3] = MFMA(vf[i], pf[i >> 2], o[i & 3]); if (i < 12) { kf[2 * i - 8] = rdk(sp * SL, 2 * i - 8); kf[2 * i - 7] = rdk(sp * SL, 2 * i - 7); } SBAR(); }
    issueV(ni - 1u, ns + U);
    if (it == 14) issueWo(g, 3u + g);
    sp = sn;
    BAR();
  }
  scores(false, std::false_type{});
  unsigned ln; asm volatile("v_mbcnt_lo_u32_b32 %0, -1, 0\n\tv_mbcnt_hi_u32_b32 %0, -1, %0" : "=v"(ln));
  const unsigned x0 = lds0 + w4 * 8192u + ln * 16u;
  l_run = xhalf_sum(l_run);
  const unsigned stats = lds0 + 2u * SL + (ln & 31u) * 8u;
  if (ln < 32u) *(LAS f32x2*)(stats + wid * 256u) = (f32x2){m_run, l_run};
  const Hs hs = hs_make(ws, bid, ln);
  unsigned hs_r, hs_q;
  asm volatile("global_load_dword %0, %2, off offset:256 sc1\n\tglobal_load_dword %1, %3, off offset:128 sc1" : "=&v"(hs_r), "=&v"(hs_q) : "v"(hs.base + hs.mine), "v"(hs.base + hs.peer) : "memory");
  bf16x8 of0[4];
  {
    WAIT_BAR(10);
    STAMP(2);
    const unsigned av = trV + (1u + g) * SL;
#pragma unroll
    for (int ks = 0; ks < 4; ++ks) { vf[4 * ks + 2] = vfrag(av, ks, 2); vf[4 * ks + 3] = vfrag(av, ks, 3); }
    SBAR();
#pragma unroll
    for (int ks = 0; ks < 4; ++ks) { vf[4 * ks] = vfrag(av, ks, 0); vf[4 * ks + 1] = vfrag(av, ks, 1); }
    SBAR();
#pragma unroll
    for (int ks = 0; ks < 4; ++ks) { o[2] = MFMA(vf[4 * ks + 2], pf[ks], o[2]); o[3] = MFMA(vf[4 * ks + 3], pf[ks], o[3]); }
    SBAR();
#pragma unroll
    for (int ks = 0; ks < 2; ++ks) { o[0] = MFMA(vf[4 * ks], pf[ks], o[0]); o[1] = MFMA(vf[4 * ks + 1], pf[ks], o[1]); }
    SBAR();
    const unsigned xo = x0 + g * SL, xi = x0 + (1u - g) * SL;
#pragma unroll
    for (int q = 0; q < 4; ++q) { lds_st4(xo + q * 1024, o[2], q); lds_st4(xo + 4096 + q * 1024, o[3], q); }
    SBAR();
#pragma unroll
    for (int ks = 2; ks < 4; ++ks) { o[0] = MFMA(vf[4 * ks], pf[ks], o[0]); o[1] = MFMA(vf[4 * ks + 1], pf[ks], o[1]); }
    SBAR();
    if (g == 0) BAR();
    const f32x2 st = *(const LAS f32x2*)(stats + (wid ^ 4u) * 256u);
    const float ms = fmaxf(m_run, st[0]);
    const float e_me = __builtin_amdgcn_exp2f(m_run - ms), e_ot = __builtin_amdgcn_exp2f(st[0] - ms);
    const float inv = 1.f / (l_run * e_me + st[1] * e_ot);
    float a_me = e_me * inv, a_ot = e_ot * inv;
    PIN(a_me); PIN(a_ot);
    asm volatile("s_waitcnt vmcnt(0) lgkmcnt(0)\n\ts_barrier" : "+v"(hs_r), "+v"(hs_q) :: "memory");
    f32x4 w0[4], w1[4];
#pragma unroll
    for (int q = 0; q < 4; ++q) { w0[q] = lds_f4(xi + q * 1024); w1[q] = lds_f4(xi + 4096 + q * 1024); }
#pragma unroll
    for (int q = 0; q < 4; ++q)
#pragma unroll
      for (int e = 0; e < 4; ++e) { o[0][4 * q + e] = fmaf(w0[q][e], a_ot, o[0][4 * q + e] * a_me); o[1][4 * q + e] = fmaf(w1[q][e], a_ot, o[1][4 * q + e] * a_me); }
    of0[0] = pack8(o[0], 0); of0[1] = pack8(o[0], 1); of0[2] = pack8(o[1], 0); of0[3] = pack8(o[1], 1);
  }
  PRIO(0);
  const __amdgpu_buffer_rsrc_t srdO = __builtin_amdgcn_make_buffer_rsrc((void*)(ws + WS_O + (size_t)(b * NH + hg * 4u) * 32768u), (short)0, 4 * 32768, 0x00020000);
#pragma unroll
  for (int s = 0; s < 4; ++s) __builtin_amdgcn_raw_buffer_store_b128(__builtin_bit_cast(u32x4, of0[s]), srdO, j * 32768u + wid * 4096u + (unsigned)s * 1024u + ln * 16u, 0, 16);
  STAMP(3);
  issueWo(2u + g, 1u - g);
  f32x16 c[4] = {};
  const unsigned trW2 = trV - U + (ln >> 5) * 1024u;
  bf16x8 wa[2][8];
  auto rdw = [&](int buf, unsigned slot, int half) {
    const unsigned aw = trW2 + slot * SL + g * U;
#pragma unroll
    for (int s = 0; s < 2; ++s)
#pragma unroll
      for (int nb = 0; nb < 4; ++nb) { const unsigned a = (aw ^ (unsigned)(nb << 6)) + (2 * half + s) * 4096; wa[buf][4 * s + nb] = lds_tr2(a, a + 1024); }
  };
  auto mmw = [&](int buf, const bf16x8& f0, const bf16x8& f1) {
#pragma unroll
    for (int nb = 0; nb < 4; ++nb) c[nb] = MFMA(wa[buf][nb], f0, c[nb]);
#pragma unroll
    for (int nb = 0; nb < 4; ++nb) c[nb] = MFMA(wa[buf][4 + nb], f1, c[nb]);
  };
  rdw(0, 3, 0); rdw(1, 3, 1); SBAR();
  mmw(0, of0[0], of0[1]); SBAR();
  rdw(0, 4, 0); SBAR();
  mmw(1, of0[2], of0[3]); SBAR();
  rdw(1, 4, 1);
  PIN(c[0]); PIN(c[1]); PIN(c[2]); PIN(c[3]);
  WAIT_BAR(0);
  STAMP(4);
  if (wid == 0) {
    if (!__all(hs_r == hs_q || !hs.act)) hs_wait_armed(hs);
    if (ln < 4u) __hip_atomic_store(hs.base + hs.peer, 1u, RLX_AGENT);
    hs_wait_flags(hs, ln < 4u);
  }
  BAR();
  STAMP(5);
  {
    bf16x8 of[3][4];
#pragma unroll
    for (int cc = 1; cc < 4; ++cc)
#pragma unroll
      for (int s = 0; s < 4; ++s)
        of[cc - 1][s] = __builtin_bit_cast(bf16x8, __builtin_amdgcn_raw_buffer_load_b128(srdO, wid * 4096u + (unsigned)s * 1024u + ln * 16u, ((j + (unsigned)cc) & 3u) * 32768u, 16));
    SBAR();
    mmw(0, of[0][0], of[0][1]); SBAR(); rdw(0, 1, 0); SBAR();
    mmw(1, of[0][2], of[0][3]); SBAR(); rdw(1, 1, 1); SBAR();
    mmw(0, of[1][0], of[1][1]); SBAR(); rdw(0, 0, 0); SBAR();
    mmw(1, of[1][2], of[1][3]); SBAR(); rdw(1, 0, 1); SBAR();
    mmw(0, of[2][0], of[2][1]); SBAR();
    mmw(1, of[2][2], of[2][3]);
  }
  STAMP(6);
  {
#pragma unroll
    for (int q = 0; q < 4; ++q) { lds_st4(x0 + (2u + g) * SL + q * 1024, c[2], q); lds_st4(x0 + (2u + g) * SL + 4096 + q * 1024, c[3], q); }
    BAR();
    const unsigned ra = x0 + (3u - g) * SL;
    const __amdgpu_buffer_rsrc_t srdP = __builtin_amdgcn_make_buffer_rsrc((void*)(ws + WS_P + (size_t)(b * 4u + j) * 524288u), (short)0, 524288, 0x00020000);
#pragma unroll
    for (int q = 0; q < 4; ++q) {
      const f32x4 va = lds_f4(ra + q * 1024), vb = lds_f4(ra + 4096 + q * 1024);
      const f32x4 sa = {c[0][4 * q] + va[0], c[0][4 * q + 1] + va[1], c[0][4 * q + 2] + va[2], c[0][4 * q + 3] + va[3]};
      const f32x4 sb = {c[1][4 * q] + vb[0], c[1][4 * q + 1] + vb[1], c[1][4 * q + 2] + vb[2], c[1][4 * q + 3] + vb[3]};
      __builtin_amdgcn_raw_buffer_store_b128(__builtin_bit_cast(u32x4, sa), srdP, hg * 65536u + wid * 8192u + (unsigned)q * 1024u + ln * 16u, 0, 16);
      __builtin_amdgcn_raw_buffer_store_b128(__builtin_bit_cast(u32x4, sb), srdP, hg * 65536u + wid * 8192u + (unsigned)(4 + q) * 1024u + ln * 16u, 0, 16);
    }
    STAMP(7);
    WAIT_BAR(0);
    STAMP(8);
    if (wid == 0) {
      if (hs.act && ln >= 8u) __hip_atomic_store(hs.base + hs.peer, 1u, RLX_AGENT);
      hs_wait_flags(hs, hs.act && ln >= 8u);
    }
    BAR();
    STAMP(9);
    f32x4 pv[8];
#pragma unroll
    for (int u = 0; u < 8; ++u)
      pv[u] = __builtin_bit_cast(f32x4, __builtin_amdgcn_raw_buffer_load_b128(srdP, hg * 8192u + wid * 1024u + ln * 16u, (unsigned)u * 65536u, 16));
    f32x4 acc = pv[0];
#pragma unroll
    for (int u = 1; u < 8; ++u) acc += pv[u];
    const unsigned qrow = (hg & 3u) * 32u + (ln & 31u), n = j * 128u + ((hg >> 2) * 2u + g) * 32u + (w4 >> 1) * 16u + (ln >> 5) * 8u + (w4 & 1u) * 4u;
    *(u32x2*)(out + ((size_t)b * SQ + qrow) * DM + n) = (u32x2){cvtpk(acc[0], acc[1]), cvtpk(acc[2], acc[3])};
    STAMP(10);
  }
}

extern "C" void kernel_launch(void* const* d_in, const int* in_sizes, int n_in, void* d_out, int out_size, void* d_ws, size_t ws_size, hipStream_t stream) {
  (void)in_sizes; (void)n_in; (void)out_size; (void)ws_size;
  (void)hipFuncSetAttribute((const void*)mha_fused, hipFuncAttributeMaxDynamicSharedMemorySize, (int)LDS_BYTES);
  hipLaunchKernelGGL(mha_fused, dim3(256), dim3(512), LDS_BYTES, stream, (const unsigned short*)d_in[0], (const unsigned short*)d_in[1], (const unsigned short*)d_in[2],
                     (const unsigned short*)d_in[3], (const unsigned short*)d_in[4], (unsigned short*)d_out, (char*)d_ws);
}
```

```cpp
#define CODEPF 15
#define PSTAG (((bid>>3)*PACE)>>5)
#define PACE 114u
#define PACE0 700
#define QSLOW 4
#define QNOP() asm volatile("s_nop 12")
#include <hip/hip_runtime.h>
#include <hip/hip_bf16.h>
#include <stdint.h>
#include <type_traits>

typedef __bf16 bf16x8 __attribute__((ext_vector_type(8)));
typedef __bf16 bf16x2 __attribute__((ext_vector_type(2)));
typedef _Float16 f16x2 __attribute__((ext_vector_type(2)));
typedef short s16x4 __attribute__((ext_vector_type(4)));
typedef float f32x16 __attribute__((ext_vector_type(16)));
typedef float f32x4 __attribute__((ext_vector_type(4)));
typedef float f32x2 __attribute__((ext_vector_type(2)));
typedef unsigned u32x4 __attribute__((ext_vector_type(4)));
typedef unsigned u32x2 __attribute__((ext_vector_type(2)));
#define LAS __attribute__((address_space(3)))
#define GAS __attribute__((address_space(1)))
#define DI __device__ __forceinline__
#define MFMA(a, b, c) __builtin_amdgcn_mfma_f32_32x32x16_bf16((a), (b), (c), 0, 0, 0)
#define RLX_AGENT __ATOMIC_RELAXED, __HIP_MEMORY_SCOPE_AGENT
#define PIN(x) asm volatile("" : "+v"(x))
#define SBAR() __builtin_amdgcn_sched_barrier(0)
#define PRIO(n) __builtin_amdgcn_s_setprio(n)

constexpr int SQ = 128, DM = 512, NH = 32, SK = 2048, DH = 128;
constexpr unsigned U = 16384u, SL = 32768u;
constexpr unsigned LDS_BYTES = 5 * SL;
#define MNOP() do { } while (0)
#define PVPRIO 0
#define PSLIM 8192.0f
constexpr float CSC = 0.088388347648318447f * 1.4426950408889634f;
constexpr size_t WS_FLAG = 0;
constexpr unsigned FB = 128;
constexpr size_t WS_O = 1 << 20;
constexpr size_t WS_P = WS_O + (8u << 20);
constexpr size_t WS_END = WS_P + (16u << 20);

DI unsigned rfl(unsigned x) { return __builtin_amdgcn_readfirstlane(x); }
DI unsigned cvtpk(float lo, float hi) { f32x2 v = {lo, hi}; bf16x2 b = __builtin_convertvector(v, bf16x2); return __builtin_bit_cast(unsigned, b); }
DI bf16x8 pack8(const f32x16& a, int s) {
  u32x4 w = {cvtpk(a[8 * s], a[8 * s + 1]), cvtpk(a[8 * s + 2], a[8 * s + 3]), cvtpk(a[8 * s + 4], a[8 * s + 5]), cvtpk(a[8 * s + 6], a[8 * s + 7])};
  return __builtin_bit_cast(bf16x8, w);
}
DI bf16x8 lds_b128(unsigned a) { return *(const LAS bf16x8*)a; }
DI f32x4 lds_f4(unsigned a) { return *(const LAS f32x4*)a; }
DI void lds_st4(unsigned a, const f32x16& v, int q) { *(LAS f32x4*)a = (f32x4){v[4 * q], v[4 * q + 1], v[4 * q + 2], v[4 * q + 3]}; }
DI s16x4 lds_tr(unsigned a) { return __builtin_amdgcn_ds_read_tr16_b64_v4i16((LAS s16x4*)a); }
DI bf16x8 lds_tr2(unsigned a0, unsigned a1) {
  s16x4 lo = lds_tr(a0), hi = lds_tr(a1);
  return __builtin_bit_cast(bf16x8, __builtin_shufflevector(lo, hi, 0, 1, 2, 3, 4, 5, 6, 7));
}
DI void dma4_kv(unsigned m0v, unsigned voff, __amdgpu_buffer_rsrc_t srd, unsigned soff) {
  unsigned keep;
  asm volatile("s_mov_b32 %0, m0\n\ts_mov_b32 m0, %1\n\ts_nop 3\n\t"
               "buffer_load_dwordx4 %2, %3, %4 offen nt lds\n\t"
               "buffer_load_dwordx4 %2, %3, %4 offen offset:1024 nt lds\n\t"
               "buffer_load_dwordx4 %2, %3, %4 offen offset:2048 nt lds\n\t"
               "buffer_load_dwordx4 %2, %3, %4 offen offset:3072 nt lds\n\t"
               "s_mov_b32 m0, %0"
               : "=&s"(keep) : "s"(rfl(m0v)), "v"(voff), "s"(srd), "s"(rfl(soff)) : "memory");
}
DI void dma4_def(unsigned m0v, unsigned voff, __amdgpu_buffer_rsrc_t srd, unsigned soff) {
  unsigned keep;
  asm volatile("s_mov_b32 %0, m0\n\ts_mov_b32 m0, %1\n\ts_nop 3\n\t"
               "buffer_load_dwordx4 %2, %3, %4 offen lds\n\t"
               "buffer_load_dwordx4 %2, %3, %4 offen offset:1024 lds\n\t"
               "buffer_load_dwordx4 %2, %3, %4 offen offset:2048 lds\n\t"
               "buffer_load_dwordx4 %2, %3, %4 offen offset:3072 lds\n\t"
               "s_mov_b32 m0, %0"
               : "=&s"(keep) : "s"(rfl(m0v)), "v"(voff), "s"(srd), "s"(rfl(soff)) : "memory");
}
#define DMA_K dma4_kv
#define DMA_V dma4_kv
DI void dma1(unsigned m0v, unsigned voff, __amdgpu_buffer_rsrc_t srd, unsigned soff) {
  unsigned keep;
  asm volatile("s_mov_b32 %0, m0\n\ts_mov_b32 m0, %1\n\ts_nop 3\n\t"
               "buffer_load_dwordx4 %2, %3, %4 offen lds\n\t"
               "s_mov_b32 m0, %0"
               : "=&s"(keep) : "s"(rfl(m0v)), "v"(voff), "s"(srd), "s"(rfl(soff)) : "memory");
}
DI void dma1nt(unsigned m0v, unsigned voff, __amdgpu_buffer_rsrc_t srd, unsigned soff) {
  unsigned keep;
  asm volatile("s_mov_b32 %0, m0\n\ts_mov_b32 m0, %1\n\ts_nop 3\n\t"
               "buffer_load_dwordx4 %2, %3, %4 offen nt lds\n\t"
               "s_mov_b32 m0, %0"
               : "=&s"(keep) : "s"(rfl(m0v)), "v"(voff), "s"(srd), "s"(rfl(soff)) : "memory");
}
#define DMAWQ dma1nt
#define DMAX dma1
#define WAIT_BAR(N) asm volatile("s_waitcnt vmcnt(" #N ") lgkmcnt(0)\n\ts_barrier" ::: "memory")
#define BAR() asm volatile("s_waitcnt lgkmcnt(0)\n\ts_barrier" ::: "memory")
#define WAITVM(N) asm volatile("s_waitcnt vmcnt(" #N ")" ::: "memory")

DI float xhalf_max(float v) { auto r = __builtin_amdgcn_permlane32_swap(__float_as_uint(v), __float_as_uint(v), false, false); return fmaxf(__uint_as_float(r[0]), __uint_as_float(r[1])); }
DI float xhalf_sum(float v) { auto r = __builtin_amdgcn_permlane32_swap(__float_as_uint(v), __float_as_uint(v), false, false); return __uint_as_float(r[0]) + __uint_as_float(r[1]); }

DI unsigned wave_umax(unsigned v) {
#define DPPMAX(ctrl, rmask) v = max(v, (unsigned)__builtin_amdgcn_update_dpp(0, (int)v, ctrl, rmask, 0xF, false))
  DPPMAX(0x111, 0xF); DPPMAX(0x112, 0xF); DPPMAX(0x114, 0xF); DPPMAX(0x118, 0xF);
  DPPMAX(0x142, 0xA); DPPMAX(0x143, 0xC);
#undef DPPMAX
  return (unsigned)__builtin_amdgcn_readlane((int)v, 63);
}
struct Hs { GAS unsigned* base; unsigned mine, peer; bool act; };
DI Hs hs_make(char* ws, unsigned bid, unsigned L) {
  Hs h; h.base = (GAS unsigned*)(ws + WS_FLAG);
  const bool h1 = L < 4u; h.act = h1 || (L >= 8u && L < 16u);
  const unsigned pb = h1 ? ((bid & ~24u) | (L << 3)) : ((bid & ~7u) | (L & 7u));
  h.mine = bid * FB + (L & 15u); h.peer = pb * FB + (h1 ? ((bid >> 3) & 3u) : 8u + (bid & 7u));
  return h;
}
DI void hs_echo(const Hs& h) { if (h.act) __hip_atomic_store(h.base + h.peer + 64u, __hip_atomic_load(h.base + h.mine + 32u, RLX_AGENT), RLX_AGENT); }
DI void hs_wait_armed(const Hs& h) {
#pragma nounroll
  for (unsigned spins = 0; spins < (1u << 20); ++spins) {
    hs_echo(h);
    bool ok = true;
    if (h.act) ok = __hip_atomic_load(h.base + h.mine + 64u, RLX_AGENT) == __hip_atomic_load(h.base + h.peer + 32u, RLX_AGENT);
    if (__all(ok)) break;
    __builtin_amdgcn_s_sleep(4);
  }
}
DI unsigned hs_wait_flags(const Hs& h, bool sel) {
  unsigned v = 1u;
#pragma nounroll
  for (unsigned spins = 0; spins < (1u << 20); ++spins) {
    if (sel) v = __hip_atomic_load(h.base + h.mine, RLX_AGENT);
    if (__all(v != 0u)) break;
    if ((spins & 31u) == 31u) hs_echo(h);
    __builtin_amdgcn_s_sleep(1);
  }
  asm volatile("" ::: "memory");
  return v;
}

#define STAMP(i) do { } while (0)

__global__ __launch_bounds__(512, 2) void mha_fused(const unsigned short* __restrict__ x, const unsigned short* __restrict__ kc, const unsigned short* __restrict__ vc,
                                                    const unsigned short* __restrict__ wq, const unsigned short* __restrict__ wo, unsigned short* __restrict__ out, char* __restrict__ ws) {
  extern __shared__ __attribute__((aligned(16))) char lds[];
  const unsigned tid = threadIdx.x, lane = tid & 63u, r32 = lane & 31u, hi = lane >> 5;
  const unsigned wid = rfl(tid >> 6), g = wid >> 2, w4 = wid & 3u;
  const unsigned bid = blockIdx.x, hg = bid & 7u, b = bid >> 5, j = (bid >> 3) & 3u, h = hg * 4u + j;
  const unsigned lds0 = (unsigned)(uintptr_t)lds;
  STAMP(0);
  const unsigned tstart = (unsigned)__builtin_amdgcn_s_memrealtime();
  asm volatile("" :: "s"(out), "s"(ws), "s"(wo));

  const __amdgpu_buffer_rsrc_t srdK = __builtin_amdgcn_make_buffer_rsrc((void*)(kc + (size_t)(b * NH + h) * SK * DH), (short)0, SK * DH * 2, 0x00020000);
  const __amdgpu_buffer_rsrc_t srdV = __builtin_amdgcn_make_buffer_rsrc((void*)(vc + (size_t)(b * NH + h) * SK * DH), (short)0, SK * DH * 2, 0x00020000);
  const __amdgpu_buffer_rsrc_t srdX = __builtin_amdgcn_make_buffer_rsrc((void*)(x + (size_t)b * SQ * DM), (short)0, SQ * DM * 2, 0x00020000);
  const __amdgpu_buffer_rsrc_t srdWq = __builtin_amdgcn_make_buffer_rsrc((void*)wq, (short)0, DM * NH * DH * 2, 0x00020000);
  const __amdgpu_buffer_rsrc_t srdWo = __builtin_amdgcn_make_buffer_rsrc((void*)(wo + (size_t)hg * 512 * DM), (short)0, 512 * DM * 2, 0x00020000);

  const unsigned l4 = lane >> 4, l15 = lane & 15u;
  const unsigned voffK = w4 * 4096u + l4 * 256u + ((l15 ^ (l4 | (w4 << 2))) << 4);
  const unsigned voffV = w4 * 4096u + l4 * 256u + ((l15 ^ (l4 << 2)) << 4);
  const unsigned voffWq = l4 * 8192u + ((l15 ^ (l4 << 2)) << 4);
  const unsigned voffWo = l4 * 1024u + ((l15 ^ (l4 << 2)) << 4);
  const unsigned voffX = (lane >> 3) * 1024u + (((lane & 7u) ^ (((lane >> 4) & 1u) | (w4 << 1))) << 4);

  auto issueK = [&](unsigned t, unsigned off) { DMA_K(lds0 + off + w4 * 4096u, voffK, srdK, t * U); };
  auto issueV = [&](unsigned t, unsigned off) { DMA_V(lds0 + off + w4 * 4096u, voffV, srdV, t * U); };
  auto issueWo = [&](unsigned cc, unsigned slot) {
    const unsigned c = (j + cc) & 3u;
#pragma unroll
    for (int q = 0; q < 8; ++q) dma1(lds0 + slot * SL + (w4 * 8u + q) * 1024u, voffWo, srdWo, (c * 128u + w4 * 32u + q * 4u) * 1024u + j * 256u);
  };
#define ROT ((b + 2u * j) & 7u)
  const unsigned rot = ROT;
  auto qslot = [](int c) { return (c % 3 == 0) ? 3u : (c % 3 == 1) ? 4u : 2u; };
  auto issueQP = [&](int c) {
    const unsigned kcn = ((unsigned)c + rot) & 7u, sb = lds0 + qslot(c) * SL;
#pragma unroll
    for (int q = 0; q < 4; ++q) {
      DMAWQ(sb + (w4 * 4u + q) * 1024u, voffWq, srdWq, (kcn * 64u + w4 * 16u + q * 4u) * 8192u + h * 256u);
      DMAX(sb + U + (q * 4u + w4) * 1024u, voffX, srdX, (q * 4u + w4) * 8192u + kcn * 128u);
    }
  };
  const unsigned blk = (lane >> 4) & 1u, q4 = l15 >> 2, p4 = lane & 3u, pp = ((p4 & 1u) << 1) | (p4 >> 1);
  const unsigned trV = lds0 + U + (4u * hi + q4) * 256u + ((q4 ^ (2u * g)) << 6) + blk * 32u + pp * 8u;
  const unsigned rdK = lds0 + ((r32 & 7u) + 16u * (r32 >> 3)) * 256u + ((hi ^ ((r32 & 3u) | ((r32 >> 3) << 2))) << 4);
  const unsigned qx = lds0 + 3u * SL + w4 * 4096u + lane * 16u;

  bf16x8 qf[8], kf[16];
  auto rdk = [&](unsigned off, int i) { return lds_b128(((rdK + off) ^ (unsigned)((i & 7) << 5)) + (i >> 3) * 2048); };
  if (g == 0) {
    const unsigned trW = lds0 + (8u * hi + q4) * 256u + (q4 << 6) + blk * 32u + pp * 8u;
    const unsigned rdX = lds0 + U + (w4 * 32u + r32) * 128u + ((hi ^ (((r32 >> 1) & 1u) | ((r32 >> 3) << 1))) << 4);
    f32x16 qa[4] = {};
    bf16x8 xb[2][4], wa_[2][16];
    auto rdq = [&](int c, int s) {
      const unsigned u0 = qslot(c) * SL;
      xb[c & 1][s] = lds_b128((rdX + u0) ^ (unsigned)(s << 5));
#pragma unroll
      for (int d = 0; d < 4; ++d) { const unsigned a = ((trW + u0) ^ (unsigned)(d << 6)) + s * 4096; wa_[c & 1][4 * s + d] = lds_tr2(a, a + 1024); }
    };
    {
      unsigned long long pc; asm volatile("s_getpc_b64 %0" : "=s"(pc));
      const __amdgpu_buffer_rsrc_t srdC = __builtin_amdgcn_make_buffer_rsrc((void*)(pc & ~255ull), (short)0, CODEPF * 1024, 0x00020000);
      if (wid == 1) dma1(lds0, lane * 16u, srdC, ((bid >> 3) % CODEPF) * 1024u);
    }
    if (wid == 0) {
      const Hs hs = hs_make(ws, bid, lane);
      if (hs.act) {
        const unsigned r0 = __hip_atomic_load(hs.base + hs.mine + 64u, RLX_AGENT);
        __hip_atomic_store(hs.base + hs.mine, 0u, RLX_AGENT);
        __hip_atomic_store(hs.base + hs.peer + 32u, r0 + 1u, RLX_AGENT);
      }
      WAITVM(0);
    }
    BAR();
#pragma unroll
    for (int s = 0; s < 4; ++s) rdq(0, s);
#pragma unroll
    for (int c = 0; c < 8; ++c) {
      if (c == 3) WAIT_BAR(0); else if (c < 7) BAR();
#pragma unroll
      for (int s = 0; s < 4; ++s)
#pragma unroll
        for (int d = 0; d < 4; ++d) {
          SBAR();
          qa[d] = MFMA(wa_[c & 1][4 * s + d], xb[c & 1][s], qa[d]);
          SBAR();
          if (c >= QSLOW) { QNOP(); SBAR(); }
          if (c < 7) {
            const unsigned u0 = qslot(c + 1) * SL, a = ((trW + u0) ^ (unsigned)(d << 6)) + s * 4096;
            if (d == 0) xb[(c + 1) & 1][s] = lds_b128((rdX + u0) ^ (unsigned)(s << 5));
            wa_[(c + 1) & 1][4 * s + d] = lds_tr2(a, a + 1024);
          }
        }
      SBAR();
    }
#pragma unroll
    for (int d = 0; d < 4; ++d) {
#pragma unroll
      for (int r = 0; r < 16; ++r) qa[d][r] *= CSC;
      qf[2 * d] = pack8(qa[d], 0); qf[2 * d + 1] = pack8(qa[d], 1);
    }
#pragma unroll
    for (int f = 0; f < 8; ++f) *(LAS bf16x8*)(qx + (f >> 2) * U + (f & 3) * 1024) = qf[f];
    BAR();
#pragma unroll
    for (int i = 0; i < 16; ++i) kf[i] = rdk(U, i);
  } else {
    issueQP(0); issueQP(1);
    WAIT_BAR(8); issueQP(2);
#pragma unroll
    for (int c = 0; c < 5; ++c) { WAIT_BAR(8); issueQP(c + 3); }
    issueK(0, U); issueK(1, 0);
    WAIT_BAR(16);
    WAIT_BAR(8);
    const Hs hs = hs_make(ws, bid, lane);
    unsigned rq;
    asm volatile("global_load_dword %0, %1, off sc1" : "=v"(rq) : "v"(hs.base + hs.mine + 32u) : "memory");
    issueK(2, SL); issueV(0, SL + U);
    asm volatile("s_waitcnt vmcnt(8) lgkmcnt(0)\n\ts_barrier" : "+v"(rq) :: "memory");
    if (wid == 4 && hs.act) __hip_atomic_store(hs.base + hs.peer + 64u, rq, RLX_AGENT);
#pragma unroll
    for (int f = 0; f < 8; ++f) qf[f] = lds_b128(qx + (f >> 2) * U + (f & 3) * 1024);
#pragma unroll
    for (int i = 0; i < 16; ++i) kf[i] = rdk(0, i);
    issueK(3, 2u * SL); issueV(1, 2u * SL + U);
    WAIT_BAR(8);
  }
  STAMP(1);

  f32x16 o[4] = {};
  float m_run = 0.f, l_run = 0.f;
  f32x16 negm = {};
  bf16x8 pf[4], vf[16];
  auto vfrag = [&](unsigned av, int ks, int d) { const unsigned a = (av ^ (unsigned)(d << 6)) + (32 * (ks & 1) + 8 * (ks >> 1)) * 256; return lds_tr2(a, a + 4096); };
  auto scores_slow = [&](bool first, float& ps) {
    f32x16 s0 = {}, s1 = {};
#pragma unroll
    for (int s = 0; s < 8; ++s) { s0 = MFMA(kf[s], qf[s], s0); s1 = MFMA(kf[8 + s], qf[s], s1); }
    float mx = fmaxf(s0[0], s1[0]);
#pragma unroll
    for (int r = 1; r < 16; ++r) mx = fmaxf(fmaxf(mx, s0[r]), s1[r]);
    mx = xhalf_max(mx);
    const float m_new = first ? mx : fmaxf(mx, m_run);
    if (!first) {
      const float alpha = __builtin_amdgcn_exp2f(m_run - m_new);
      l_run *= alpha;
#pragma unroll
      for (int d = 0; d < 4; ++d)
#pragma unroll
        for (int r = 0; r < 16; ++r) o[d][r] *= alpha;
    }
    m_run = m_new;
    ps = 0.f;
#pragma unroll
    for (int r = 0; r < 16; ++r) { negm[r] = -m_new; s0[r] = __builtin_amdgcn_exp2f(s0[r] - m_new); s1[r] = __builtin_amdgcn_exp2f(s1[r] - m_new); ps += s0[r] + s1[r]; }
    pf[0] = pack8(s0, 0); pf[1] = pack8(s0, 1); pf[2] = pack8(s1, 0); pf[3] = pack8(s1, 1);
  };
  auto scores = [&](bool first, auto inloop) {
    float ps = 0.f;
    PRIO(2);
    if (!first) {
      SBAR();
      f32x16 s0 = MFMA(kf[0], qf[0], negm);
#pragma unroll
      for (int s = 1; s < 8; ++s) { MNOP(); SBAR(); s0 = MFMA(kf[s], qf[s], s0); SBAR(); }
      SBAR();
      f32x16 s1 = MFMA(kf[8], qf[0], negm);
      SBAR();
      float pa = 0.f, pb = 0.f;
      unsigned w[16];
#define SMSTEP(S, e, wi) do { S[e] = __builtin_amdgcn_exp2f(S[e]); S[e + 1] = __builtin_amdgcn_exp2f(S[e + 1]); pa += S[e]; pb += S[e + 1]; w[wi] = cvtpk(S[e], S[e + 1]); PIN(pa); PIN(pb); PIN(w[wi]); } while (0)
#pragma unroll
      for (int s = 1; s < 8; ++s) {
        s1 = MFMA(kf[8 + s], qf[s], s1);
        SMSTEP(s0, 2 * (s - 1), s - 1);
        SBAR();
      }
      SMSTEP(s0, 14, 7);
      SBAR();
#pragma unroll
      for (int i = 0; i < 8; ++i) { SMSTEP(s1, 2 * i, 8 + i); SBAR(); }
#undef SMSTEP
      ps = pa + pb;
#pragma unroll
      for (int i = 0; i < 4; ++i) pf[i] = __builtin_bit_cast(bf16x8, (u32x4){w[4 * i], w[4 * i + 1], w[4 * i + 2], w[4 * i + 3]});
    }
    if (__builtin_expect(first || __any(!(ps <= PSLIM)), 0)) scores_slow(first, ps);
    l_run += ps;
    PIN(pf[0]); PIN(pf[1]); PIN(pf[2]); PIN(pf[3]); PIN(l_run); if (decltype(inloop)::value) PIN(negm);
    PRIO(PVPRIO);
  };

  __builtin_amdgcn_s_waitcnt(0xC07F);
  unsigned sp = 1u + g;
  for (int it = 0; it < 15; ++it) {
    const unsigned p = 2u * (unsigned)it + g + 1u;
    scores(it == 0, std::true_type{});
    WAIT_BAR(0);
    { const unsigned target = tstart + PACE0 + p * PACE + PSTAG;
      for (;;) { unsigned long long t_; asm volatile("s_memrealtime %0\n\ts_waitcnt lgkmcnt(0)" : "=s"(t_) :: "memory"); if ((int)((unsigned)t_ - target) >= 0) break; __builtin_amdgcn_s_sleep(2); } }
    const unsigned ni = p + 2u, sn = sp >= 3u ? sp - 3u : sp + 2u, ns = sn * SL;
    const unsigned av = trV + sp * SL;
#pragma unroll
    for (int i = 0; i < 16; ++i) vf[i] = vfrag(av, i >> 2, i & 3);
    SBAR();
    if (ni <= 30u) issueK(ni + 1u, ns);
    SBAR();
#pragma unroll
    for (int i = 0; i < 8; ++i) kf[i] = rdk(sp * SL, i);
    SBAR();
#pragma unroll
    for (int i = 0; i < 8; ++i) { o[i & 3] = MFMA(vf[i], pf[i >> 2], o[i & 3]); SBAR(); MNOP(); SBAR(); }
    SBAR();
#pragma unroll
    for (int i = 8; i < 16; ++i) { o[i & 3] = MFMA(vf[i], pf[i >> 2], o[i & 3]); if (i < 12) { kf[2 * i - 8] = rdk(sp * SL, 2 * i - 8); kf[2 * i - 7] = rdk(sp * SL, 2 * i - 7); } SBAR(); MNOP(); SBAR(); }
    issueV(ni - 1u, ns + U);
    if (it == 14) issueWo(g, 3u + g);
    sp = sn;
    BAR();
  }
  scores(false, std::false_type{});
  unsigned ln; asm volatile("v_mbcnt_lo_u32_b32 %0, -1, 0\n\tv_mbcnt_hi_u32_b32 %0, -1, %0" : "=v"(ln));
  const unsigned x0 = lds0 + w4 * 8192u + ln * 16u;
  l_run = xhalf_sum(l_run);
  const unsigned stats = lds0 + 2u * SL + (ln & 31u) * 8u;
  if (ln < 32u) *(LAS f32x2*)(stats + wid * 256u) = (f32x2){m_run, l_run};
  const Hs hs = hs_make(ws, bid, ln);
  unsigned hs_r, hs_q;
  asm volatile("global_load_dword %0, %2, off offset:256 sc1\n\tglobal_load_dword %1, %3, off offset:128 sc1" : "=&v"(hs_r), "=&v"(hs_q) : "v"(hs.base + hs.mine), "v"(hs.base + hs.peer) : "memory");
  bf16x8 of0[4];
  {
    WAIT_BAR(10);
    STAMP(2);
    const unsigned av = trV + (1u + g) * SL;
#pragma unroll
    for (int ks = 0; ks < 4; ++ks) { vf[4 * ks + 2] = vfrag(av, ks, 2); vf[4 * ks + 3] = vfrag(av, ks, 3); }
    SBAR();
#pragma unroll
    for (int ks = 0; ks < 4; ++ks) { vf[4 * ks] = vfrag(av, ks, 0); vf[4 * ks + 1] = vfrag(av, ks, 1); }
    SBAR();
#pragma unroll
    for (int ks = 0; ks < 4; ++ks) { o[2] = MFMA(vf[4 * ks + 2], pf[ks], o[2]); o[3] = MFMA(vf[4 * ks + 3], pf[ks], o[3]); }
    SBAR();
#pragma unroll
    for (int ks = 0; ks < 2; ++ks) { o[0] = MFMA(vf[4 * ks], pf[ks], o[0]); o[1] = MFMA(vf[4 * ks + 1], pf[ks], o[1]); }
    SBAR();
    const unsigned xo = x0 + g * SL, xi = x0 + (1u - g) * SL;
#pragma unroll
    for (int q = 0; q < 4; ++q) { lds_st4(xo + q * 1024, o[2], q); lds_st4(xo + 4096 + q * 1024, o[3], q); }
    SBAR();
#pragma unroll
    for (int ks = 2; ks < 4; ++ks) { o[0] = MFMA(vf[4 * ks], pf[ks], o[0]); o[1] = MFMA(vf[4 * ks + 1], pf[ks], o[1]); }
    SBAR();
    if (g == 0) BAR();
    const f32x2 st = *(const LAS f32x2*)(stats + (wid ^ 4u) * 256u);
    const float ms = fmaxf(m_run, st[0]);
    const float e_me = __builtin_amdgcn_exp2f(m_run - ms), e_ot = __builtin_amdgcn_exp2f(st[0] - ms);
    const float inv = 1.f / (l_run * e_me + st[1] * e_ot);
    float a_me = e_me * inv, a_ot = e_ot * inv;
    PIN(a_me); PIN(a_ot);
    asm volatile("s_waitcnt vmcnt(0) lgkmcnt(0)\n\ts_barrier" : "+v"(hs_r), "+v"(hs_q) :: "memory");
    f32x4 w0[4], w1[4];
#pragma unroll
    for (int q = 0; q < 4; ++q) { w0[q] = lds_f4(xi + q * 1024); w1[q] = lds_f4(xi + 4096 + q * 1024); }
#pragma unroll
    for (int q = 0; q < 4; ++q)
#pragma unroll
      for (int e = 0; e < 4; ++e) { o[0][4 * q + e] = fmaf(w0[q][e], a_ot, o[0][4 * q + e] * a_me); o[1][4 * q + e] = fmaf(w1[q][e], a_ot, o[1][4 * q + e] * a_me); }
    of0[0] = pack8(o[0], 0); of0[1] = pack8(o[0], 1); of0[2] = pack8(o[1], 0); of0[3] = pack8(o[1], 1);
  }
  PRIO(0);
  const __amdgpu_buffer_rsrc_t srdO = __builtin_amdgcn_make_buffer_rsrc((void*)(ws + WS_O + (size_t)(b * NH + hg * 4u) * 32768u), (short)0, 4 * 32768, 0x00020000);
#pragma unroll
  for (int s = 0; s < 4; ++s) __builtin_amdgcn_raw_buffer_store_b128(__builtin_bit_cast(u32x4, of0[s]), srdO, j * 32768u + wid * 4096u + (unsigned)s * 1024u + ln * 16u, 0, 16);
  STAMP(3);
  issueWo(2u + g, 1u - g);
  f32x16 c[4] = {};
  const unsigned trW2 = trV - U + (ln >> 5) * 1024u + g * U;
  auto wfrag = [&](unsigned slot, int s, int nb) { const unsigned a = ((trW2 + slot * SL) ^ (unsigned)(nb << 6)) + s * 4096; return lds_tr2(a, a + 1024); };
  bf16x8 ws_[24], wk_[24];
  {
    bf16x8 wa[16];
#pragma unroll
    for (int i = 0; i < 16; ++i) wa[i] = wfrag(3, i >> 2, i & 3);
    SBAR();
#pragma unroll
    for (int i = 0; i < 16; ++i) { c[i & 3] = MFMA(wa[i], of0[i >> 2], c[i & 3]); if (i < 8) ws_[i] = wfrag(4, i >> 1, 2 + (i & 1)); SBAR(); }
  }
  PIN(c[0]); PIN(c[1]); PIN(c[2]); PIN(c[3]);
  WAIT_BAR(0);
  STAMP(4);
  if (wid == 0) {
    if (!__all(hs_r == hs_q || !hs.act)) hs_wait_armed(hs);
    if (ln < 4u) __hip_atomic_store(hs.base + hs.peer, 1u, RLX_AGENT);
  }
#pragma unroll
  for (int i = 0; i < 16; ++i) ws_[8 + i] = wfrag(i < 8 ? 1 : 0, (i >> 1) & 3, 2 + (i & 1));
  SBAR();
  if (wid == 0) hs_wait_flags(hs, ln < 4u);
  BAR();
  STAMP(5);
  const unsigned xch = lds0 + 4u * SL;
  const __amdgpu_buffer_rsrc_t srdP = __builtin_amdgcn_make_buffer_rsrc((void*)(ws + WS_P + (size_t)(b * 4u + j) * 262144u), (short)0, 262144, 0x00020000);
  {
    bf16x8 of[12];
#pragma unroll
    for (int i = 0; i < 12; ++i)
      of[i] = __builtin_bit_cast(bf16x8, __builtin_amdgcn_raw_buffer_load_b128(srdO, wid * 4096u + (unsigned)(i & 3) * 1024u + ln * 16u, ((j + 1u + (unsigned)(i >> 2)) & 3u) * 32768u, 16));
    SBAR();
#pragma unroll
    for (int i = 0; i < 24; ++i) {
      c[2 + (i & 1)] = MFMA(ws_[i], of[i >> 1], c[2 + (i & 1)]);
      wk_[i] = wfrag(i < 8 ? 4 : i < 16 ? 1 : 0, (i >> 1) & 3, i & 1);
      SBAR();
    }
#pragma unroll
    for (int i = 0; i < 4; ++i) c[0] = MFMA(wk_[2 * i], of[i], c[0]);
    SBAR();
#pragma unroll
    for (int q = 0; q < 4; ++q) { lds_st4(x0 + (2u + g) * SL + q * 1024, c[2], q); lds_st4(x0 + (2u + g) * SL + 4096 + q * 1024, c[3], q); }
    BAR();
    STAMP(6);
    const unsigned ra = x0 + (3u - g) * SL;
    f32x4 va[8];
#pragma unroll
    for (int q = 0; q < 8; ++q) va[q] = lds_f4(ra + q * 1024);
    SBAR();
#pragma unroll
    for (int i = 4; i < 12; ++i) c[0] = MFMA(wk_[2 * i], of[i], c[0]);
    SBAR();
    auto put = [&](int nb) {
      float mx = 0.f;
#pragma unroll
      for (int r = 0; r < 16; ++r) { c[nb][r] += va[4 * nb + (r >> 2)][r & 3]; mx = fmaxf(mx, fabsf(c[nb][r])); }
      const unsigned e = max(wave_umax(__float_as_uint(mx)) >> 23, 15u);
      const float sc = __uint_as_float((268u - e) << 23);
#pragma unroll
      for (int s = 0; s < 2; ++s) {
        u32x4 w;
#pragma unroll
        for (int q = 0; q < 4; ++q) { f32x2 v = {c[nb][8 * s + 2 * q] * sc, c[nb][8 * s + 2 * q + 1] * sc}; w[q] = __builtin_bit_cast(unsigned, __builtin_convertvector(v, f16x2)); }
        __builtin_amdgcn_raw_buffer_store_b128(w, srdP, hg * 32768u + wid * 4096u + (unsigned)(2 * nb + s) * 1024u + ln * 16u, 0, 16);
      }
      if (ln == 0) *(LAS unsigned char*)(xch + wid * 4u + (unsigned)nb) = (unsigned char)e;
    };
#pragma unroll
    for (int i = 0; i < 3; ++i) c[1] = MFMA(wk_[2 * i + 1], of[i], c[1]);
    PIN(c[1]);
    SBAR();
    put(0);
    SBAR();
#pragma unroll
    for (int i = 3; i < 12; ++i) c[1] = MFMA(wk_[2 * i + 1], of[i], c[1]);
    SBAR();
    put(1);
  }
  STAMP(7);
  WAIT_BAR(0);
  STAMP(8);
  if (wid == 0) {
    const bool sel = hs.act && ln >= 8u;
    if (sel) __hip_atomic_store(hs.base + hs.peer, (*(const LAS unsigned*)(xch + (ln & 7u) * 4u) & 0xFFFFu) | 0x10000u, RLX_AGENT);
    const unsigned fv = hs_wait_flags(hs, sel);
    if (sel) *(LAS unsigned*)(xch + 64u + (ln & 7u) * 4u) = fv;
  }
  BAR();
  STAMP(9);
  if (g == 0) {
    u32x4 pv[8];
#pragma unroll
    for (int u = 0; u < 8; ++u) pv[u] = __builtin_amdgcn_raw_buffer_load_b128(srdP, hg * 4096u + w4 * 1024u + ln * 16u, (unsigned)u * 32768u, 16);
    const u32x4 f0 = *(const LAS u32x4*)(xch + 64u), f1 = *(const LAS u32x4*)(xch + 80u);
    float acc[8] = {};
#pragma unroll
    for (int u = 0; u < 8; ++u) {
      const unsigned e = ((u < 4 ? f0[u & 3] : f1[u & 3]) >> (8u * (w4 >> 1))) & 0xFFu;
      const float isc = __uint_as_float((e - 14u) << 23);
#pragma unroll
      for (int q = 0; q < 4; ++q) { const unsigned wv = pv[u][q]; const f16x2 hv = __builtin_bit_cast(f16x2, wv); acc[2 * q] = fmaf((float)hv[0], isc, acc[2 * q]); acc[2 * q + 1] = fmaf((float)hv[1], isc, acc[2 * q + 1]); }
    }
    const unsigned qrow = (hg & 3u) * 32u + (ln & 31u), n = j * 128u + ((hg >> 2) * 2u + (w4 >> 1)) * 32u + (w4 & 1u) * 16u + (ln >> 5) * 8u;
    *(u32x4*)(out + ((size_t)b * SQ + qrow) * DM + n) = (u32x4){cvtpk(acc[0], acc[1]), cvtpk(acc[2], acc[3]), cvtpk(acc[4], acc[5]), cvtpk(acc[6], acc[7])};
  }
  STAMP(10);
}

extern "C" void kernel_launch(void* const* d_in, const int* in_sizes, int n_in, void* d_out, int out_size, void* d_ws, size_t ws_size, hipStream_t stream) {
  (void)in_sizes; (void)n_in; (void)out_size; (void)ws_size;
  (void)hipFuncSetAttribute((const void*)mha_fused, hipFuncAttributeMaxDynamicSharedMemorySize, (int)LDS_BYTES);
  hipLaunchKernelGGL(mha_fused, dim3(256), dim3(512), LDS_BYTES, stream, (const unsigned short*)d_in[0], (const unsigned short*)d_in[1], (const unsigned short*)d_in[2],
                     (const unsigned short*)d_in[3], (const unsigned short*)d_in[4], (unsigned short*)d_out, (char*)d_ws);
}
```

```cpp
#define QSLOW 4
#define QNOP() asm volatile("s_nop 12")
#define PACE 115u
#include <hip/hip_runtime.h>
#include <hip/hip_bf16.h>
#include <stdint.h>
#include <type_traits>

typedef __bf16 bf16x8 __attribute__((ext_vector_type(8)));
typedef __bf16 bf16x2 __attribute__((ext_vector_type(2)));
typedef _Float16 f16x2 __attribute__((ext_vector_type(2)));
typedef short s16x4 __attribute__((ext_vector_type(4)));
typedef float f32x16 __attribute__((ext_vector_type(16)));
typedef float f32x4 __attribute__((ext_vector_type(4)));
typedef float f32x2 __attribute__((ext_vector_type(2)));
typedef unsigned u32x4 __attribute__((ext_vector_type(4)));
typedef unsigned u32x2 __attribute__((ext_vector_type(2)));
#define LAS __attribute__((address_space(3)))
#define GAS __attribute__((address_space(1)))
#define DI __device__ __forceinline__
#define MFMA(a, b, c) __builtin_amdgcn_mfma_f32_32x32x16_bf16((a), (b), (c), 0, 0, 0)
#define RLX_AGENT __ATOMIC_RELAXED, __HIP_MEMORY_SCOPE_AGENT
#define PIN(x) asm volatile("" : "+v"(x))
#define SBAR() __builtin_amdgcn_sched_barrier(0)
#define PRIO(n) __builtin_amdgcn_s_setprio(n)

constexpr int SQ = 128, DM = 512, NH = 32, SK = 2048, DH = 128;
constexpr unsigned U = 16384u, SL = 32768u;
constexpr unsigned LDS_BYTES = 5 * SL;
#define CODEPF 15
#define PACE0 700u
#define PSTAG (((bid >> 3) * PACE) >> 5)
#define MNOP() do { } while (0)
#define PVPRIO 0
#define PSLIM 8192.0f
constexpr float CSC = 0.088388347648318447f * 1.4426950408889634f;
constexpr size_t WS_FLAG = 0;
constexpr unsigned FB = 128;
constexpr size_t WS_O = 1 << 20;
constexpr size_t WS_P = WS_O + (8u << 20);
constexpr size_t WS_END = WS_P + (16u << 20);

DI unsigned rfl(unsigned x) { return __builtin_amdgcn_readfirstlane(x); }
DI unsigned cvtpk(float lo, float hi) { f32x2 v = {lo, hi}; bf16x2 b = __builtin_convertvector(v, bf16x2); return __builtin_bit_cast(unsigned, b); }
DI bf16x8 pack8(const f32x16& a, int s) {
  u32x4 w = {cvtpk(a[8 * s], a[8 * s + 1]), cvtpk(a[8 * s + 2], a[8 * s + 3]), cvtpk(a[8 * s + 4], a[8 * s + 5]), cvtpk(a[8 * s + 6], a[8 * s + 7])};
  return __builtin_bit_cast(bf16x8, w);
}
DI bf16x8 lds_b128(unsigned a) { return *(const LAS bf16x8*)a; }
DI f32x4 lds_f4(unsigned a) { return *(const LAS f32x4*)a; }
DI void lds_st4(unsigned a, const f32x16& v, int q) { *(LAS f32x4*)a = (f32x4){v[4 * q], v[4 * q + 1], v[4 * q + 2], v[4 * q + 3]}; }
DI s16x4 lds_tr(unsigned a) { return __builtin_amdgcn_ds_read_tr16_b64_v4i16((LAS s16x4*)a); }
DI bf16x8 lds_tr2(unsigned a0, unsigned a1) {
  s16x4 lo = lds_tr(a0), hi = lds_tr(a1);
  return __builtin_bit_cast(bf16x8, __builtin_shufflevector(lo, hi, 0, 1, 2, 3, 4, 5, 6, 7));
}
DI void dma4_kv(unsigned m0v, unsigned voff, __amdgpu_buffer_rsrc_t srd, unsigned soff) {
  unsigned keep;
  asm volatile("s_mov_b32 %0, m0\n\ts_mov_b32 m0, %1\n\ts_nop 3\n\t"
               "buffer_load_dwordx4 %2, %3, %4 offen nt lds\n\t"
               "buffer_load_dwordx4 %2, %3, %4 offen offset:1024 nt lds\n\t"
               "buffer_load_dwordx4 %2, %3, %4 offen offset:2048 nt lds\n\t"
               "buffer_load_dwordx4 %2, %3, %4 offen offset:3072 nt lds\n\t"
               "s_mov_b32 m0, %0"
               : "=&s"(keep) : "s"(rfl(m0v)), "v"(voff), "s"(srd), "s"(rfl(soff)) : "memory");
}
DI void dma4_def(unsigned m0v, unsigned voff, __amdgpu_buffer_rsrc_t srd, unsigned soff) {
  unsigned keep;
  asm volatile("s_mov_b32 %0, m0\n\ts_mov_b32 m0, %1\n\ts_nop 3\n\t"
               "buffer_load_dwordx4 %2, %3, %4 offen lds\n\t"
               "buffer_load_dwordx4 %2, %3, %4 offen offset:1024 lds\n\t"
               "buffer_load_dwordx4 %2, %3, %4 offen offset:2048 lds\n\t"
               "buffer_load_dwordx4 %2, %3, %4 offen offset:3072 lds\n\t"
               "s_mov_b32 m0, %0"
               : "=&s"(keep) : "s"(rfl(m0v)), "v"(voff), "s"(srd), "s"(rfl(soff)) : "memory");
}
#define DMA_K dma4_kv
#define DMA_V dma4_kv
DI void dma2_kv(unsigned m0v, unsigned voff, __amdgpu_buffer_rsrc_t srd, unsigned soff) {
  unsigned keep;
  asm volatile("s_mov_b32 %0, m0\n\ts_mov_b32 m0, %1\n\ts_nop 3\n\t"
               "buffer_load_dwordx4 %2, %3, %4 offen nt lds\n\t"
               "buffer_load_dwordx4 %2, %3, %4 offen offset:1024 nt lds\n\t"
               "s_mov_b32 m0, %0"
               : "=&s"(keep) : "s"(rfl(m0v)), "v"(voff), "s"(srd), "s"(rfl(soff)) : "memory");
}
DI void dma1(unsigned m0v, unsigned voff, __amdgpu_buffer_rsrc_t srd, unsigned soff) {
  unsigned keep;
  asm volatile("s_mov_b32 %0, m0\n\ts_mov_b32 m0, %1\n\ts_nop 3\n\t"
               "buffer_load_dwordx4 %2, %3, %4 offen lds\n\t"
               "s_mov_b32 m0, %0"
               : "=&s"(keep) : "s"(rfl(m0v)), "v"(voff), "s"(srd), "s"(rfl(soff)) : "memory");
}
DI void dma1nt(unsigned m0v, unsigned voff, __amdgpu_buffer_rsrc_t srd, unsigned soff) {
  unsigned keep;
  asm volatile("s_mov_b32 %0, m0\n\ts_mov_b32 m0, %1\n\ts_nop 3\n\t"
               "buffer_load_dwordx4 %2, %3, %4 offen nt lds\n\t"
               "s_mov_b32 m0, %0"
               : "=&s"(keep) : "s"(rfl(m0v)), "v"(voff), "s"(srd), "s"(rfl(soff)) : "memory");
}
#define DMAWQ dma1nt
#define DMAX dma1
#define WAIT_BAR(N) asm volatile("s_waitcnt vmcnt(" #N ") lgkmcnt(0)\n\ts_barrier" ::: "memory")
#define BAR() asm volatile("s_waitcnt lgkmcnt(0)\n\ts_barrier" ::: "memory")
#define WAITVM(N) asm volatile("s_waitcnt vmcnt(" #N ")" ::: "memory")

DI float xhalf_max(float v) { auto r = __builtin_amdgcn_permlane32_swap(__float_as_uint(v), __float_as_uint(v), false, false); return fmaxf(__uint_as_float(r[0]), __uint_as_float(r[1])); }
DI float xhalf_sum(float v) { auto r = __builtin_amdgcn_permlane32_swap(__float_as_uint(v), __float_as_uint(v), false, false); return __uint_as_float(r[0]) + __uint_as_float(r[1]); }

DI unsigned wave_umax(unsigned v) {
#define DPPMAX(ctrl, rmask) v = max(v, (unsigned)__builtin_amdgcn_update_dpp(0, (int)v, ctrl, rmask, 0xF, false))
  DPPMAX(0x111, 0xF); DPPMAX(0x112, 0xF); DPPMAX(0x114, 0xF); DPPMAX(0x118, 0xF);
  DPPMAX(0x142, 0xA); DPPMAX(0x143, 0xC);
#undef DPPMAX
  return (unsigned)__builtin_amdgcn_readlane((int)v, 63);
}
struct Hs { GAS unsigned* base; unsigned mine, peer; bool act; };
DI Hs hs_make(char* ws, unsigned bid, unsigned L) {
  Hs h; h.base = (GAS unsigned*)(ws + WS_FLAG);
  const bool h1 = L < 4u; h.act = h1 || (L >= 8u && L < 16u);
  const unsigned pb = h1 ? ((bid & ~24u) | (L << 3)) : ((bid & ~7u) | (L & 7u));
  h.mine = bid * FB + (L & 15u); h.peer = pb * FB + (h1 ? ((bid >> 3) & 3u) : 8u + (bid & 7u));
  return h;
}
DI void hs_echo(const Hs& h) { if (h.act) __hip_atomic_store(h.base + h.peer + 64u, __hip_atomic_load(h.base + h.mine + 32u, RLX_AGENT), RLX_AGENT); }
DI void hs_wait_armed(const Hs& h) {
#pragma nounroll
  for (unsigned spins = 0; spins < (1u << 20); ++spins) {
    hs_echo(h);
    bool ok = true;
    if (h.act) ok = __hip_atomic_load(h.base + h.mine + 64u, RLX_AGENT) == __hip_atomic_load(h.base + h.peer + 32u, RLX_AGENT);
    if (__all(ok)) break;
    __builtin_amdgcn_s_sleep(4);
  }
}
DI unsigned hs_wait_flags(const Hs& h, bool sel) {
  unsigned v = 1u;
#pragma nounroll
  for (unsigned spins = 0; spins < (1u << 20); ++spins) {
    if (sel) v = __hip_atomic_load(h.base + h.mine, RLX_AGENT);
    if (__all(v != 0u)) break;
    if ((spins & 31u) == 31u) hs_echo(h);
    __builtin_amdgcn_s_sleep(1);
  }
  asm volatile("" ::: "memory");
  return v;
}

#define STAMP(i) do { } while (0)

__global__ __launch_bounds__(512, 2) void mha_fused(const unsigned short* __restrict__ x, const unsigned short* __restrict__ kc, const unsigned short* __restrict__ vc,
                                                    const unsigned short* __restrict__ wq, const unsigned short* __restrict__ wo, unsigned short* __restrict__ out, char* __restrict__ ws) {
  extern __shared__ __attribute__((aligned(16))) char lds[];
  const unsigned tid = threadIdx.x, lane = tid & 63u, r32 = lane & 31u, hi = lane >> 5;
  const unsigned wid = rfl(tid >> 6), g = wid >> 2, w4 = wid & 3u;
  const unsigned bid = blockIdx.x, hg = bid & 7u, b = bid >> 5, j = (bid >> 3) & 3u, h = hg * 4u + j;
  const unsigned lds0 = (unsigned)(uintptr_t)lds;
  STAMP(0);
  const unsigned tstart = (unsigned)__builtin_amdgcn_s_memrealtime();
  asm volatile("" :: "s"(out), "s"(ws), "s"(wo));

  const __amdgpu_buffer_rsrc_t srdK = __builtin_amdgcn_make_buffer_rsrc((void*)(kc + (size_t)(b * NH + h) * SK * DH), (short)0, SK * DH * 2, 0x00020000);
  const __amdgpu_buffer_rsrc_t srdV = __builtin_amdgcn_make_buffer_rsrc((void*)(vc + (size_t)(b * NH + h) * SK * DH), (short)0, SK * DH * 2, 0x00020000);
  const __amdgpu_buffer_rsrc_t srdX = __builtin_amdgcn_make_buffer_rsrc((void*)(x + (size_t)b * SQ * DM), (short)0, SQ * DM * 2, 0x00020000);
  const __amdgpu_buffer_rsrc_t srdWq = __builtin_amdgcn_make_buffer_rsrc((void*)wq, (short)0, DM * NH * DH * 2, 0x00020000);
  const __amdgpu_buffer_rsrc_t srdWo = __builtin_amdgcn_make_buffer_rsrc((void*)(wo + (size_t)hg * 512 * DM), (short)0, 512 * DM * 2, 0x00020000);

  const unsigned l4 = lane >> 4, l15 = lane & 15u;
  const unsigned voffK = w4 * 4096u + l4 * 256u + ((l15 ^ (l4 | (w4 << 2))) << 4);
  const unsigned voffV = w4 * 4096u + l4 * 256u + ((l15 ^ (l4 << 2)) << 4);
  const unsigned voffWq = l4 * 8192u + ((l15 ^ (l4 << 2)) << 4);
  const unsigned voffWo = l4 * 1024u + ((l15 ^ (l4 << 2)) << 4);
  const unsigned voffX = (lane >> 3) * 1024u + (((lane & 7u) ^ (((lane >> 4) & 1u) | (w4 << 1))) << 4);

  auto issueK = [&](unsigned t, unsigned off) { DMA_K(lds0 + off + w4 * 4096u, voffK, srdK, t * U); };
  auto issueV = [&](unsigned t, unsigned off) { DMA_V(lds0 + off + w4 * 4096u, voffV, srdV, t * U); };
  auto issueWo = [&](unsigned cc, unsigned slot) {
    const unsigned c = (j + cc) & 3u;
#pragma unroll
    for (int q = 0; q < 8; ++q) dma1(lds0 + slot * SL + (w4 * 8u + q) * 1024u, voffWo, srdWo, (c * 128u + w4 * 32u + q * 4u) * 1024u + j * 256u);
  };
#define ROT ((b + 2u * j) & 7u)
  const unsigned rot = ROT;
  auto qslot = [](int c) { return (c % 3 == 0) ? 3u : (c % 3 == 1) ? 4u : 2u; };
  auto issueQP = [&](int c) {
    const unsigned kcn = ((unsigned)c + rot) & 7u, sb = lds0 + qslot(c) * SL;
#pragma unroll
    for (int q = 0; q < 4; ++q) {
      DMAWQ(sb + (w4 * 4u + q) * 1024u, voffWq, srdWq, (kcn * 64u + w4 * 16u + q * 4u) * 8192u + h * 256u);
      DMAX(sb + U + (q * 4u + w4) * 1024u, voffX, srdX, (q * 4u + w4) * 8192u + kcn * 128u);
    }
  };
  const unsigned blk = (lane >> 4) & 1u, q4 = l15 >> 2, p4 = lane & 3u, pp = ((p4 & 1u) << 1) | (p4 >> 1);
  const unsigned trV = lds0 + U + (4u * hi + q4) * 256u + ((q4 ^ (2u * g)) << 6) + blk * 32u + pp * 8u;
  const unsigned rdK = lds0 + ((r32 & 7u) + 16u * (r32 >> 3)) * 256u + ((hi ^ ((r32 & 3u) | ((r32 >> 3) << 2))) << 4);
  const unsigned qx = lds0 + 3u * SL + w4 * 4096u + lane * 16u;

  bf16x8 qf[8], kf[16];
  auto rdk = [&](unsigned off, int i) { return lds_b128(((rdK + off) ^ (unsigned)((i & 7) << 5)) + (i >> 3) * 2048); };
  if (g == 0) {
    const unsigned trW = lds0 + (8u * hi + q4) * 256u + (q4 << 6) + blk * 32u + pp * 8u;
    const unsigned rdX = lds0 + U + (w4 * 32u + r32) * 128u + ((hi ^ (((r32 >> 1) & 1u) | ((r32 >> 3) << 1))) << 4);
    f32x16 qa[4] = {};
    bf16x8 xb[2][4], wa_[2][16];
    auto rdq = [&](int c, int s) {
      const unsigned u0 = qslot(c) * SL;
      xb[c & 1][s] = lds_b128((rdX + u0) ^ (unsigned)(s << 5));
#pragma unroll
      for (int d = 0; d < 4; ++d) { const unsigned a = ((trW + u0) ^ (unsigned)(d << 6)) + s * 4096; wa_[c & 1][4 * s + d] = lds_tr2(a, a + 1024); }
    };
    {
      unsigned long long pc; asm volatile("s_getpc_b64 %0" : "=s"(pc));
      const __amdgpu_buffer_rsrc_t srdC = __builtin_amdgcn_make_buffer_rsrc((void*)(pc & ~255ull), (short)0, CODEPF * 1024, 0x00020000);
      if (wid == 1) dma1(lds0, lane * 16u, srdC, ((bid >> 3) % CODEPF) * 1024u);
    }
    unsigned r0;
    { const Hs hs = hs_make(ws, bid, lane);
      if (wid == 0 && hs.act) __hip_atomic_store(hs.base + hs.mine, 0u, RLX_AGENT);
      asm volatile("global_load_dword %0, %1, off offset:256 sc1" : "=v"(r0) : "v"(hs.base + hs.mine) : "memory"); }
    BAR();
#pragma unroll
    for (int s = 0; s < 4; ++s) rdq(0, s);
#pragma unroll
    for (int c = 0; c < 8; ++c) {
      if (c == 2) {
        asm volatile("s_waitcnt vmcnt(0) lgkmcnt(0)\n\ts_barrier" : "+v"(r0) :: "memory");
        const Hs hs = hs_make(ws, bid, lane);
        if (wid == 0 && hs.act) __hip_atomic_store(hs.base + hs.peer + 32u, r0 + 1u, RLX_AGENT);
      } else if (c < 7) BAR();
#pragma unroll
      for (int s = 0; s < 4; ++s)
#pragma unroll
        for (int d = 0; d < 4; ++d) {
          SBAR();
          qa[d] = MFMA(wa_[c & 1][4 * s + d], xb[c & 1][s], qa[d]);
          SBAR();
          if (c >= QSLOW) { QNOP(); SBAR(); }
          if (c < 7) {
            const unsigned u0 = qslot(c + 1) * SL, a = ((trW + u0) ^ (unsigned)(d << 6)) + s * 4096;
            if (d == 0) xb[(c + 1) & 1][s] = lds_b128((rdX + u0) ^ (unsigned)(s << 5));
            wa_[(c + 1) & 1][4 * s + d] = lds_tr2(a, a + 1024);
          }
        }
      SBAR();
    }
#pragma unroll
    for (int d = 0; d < 4; ++d) {
#pragma unroll
      for (int r = 0; r < 16; ++r) qa[d][r] *= CSC;
      qf[2 * d] = pack8(qa[d], 0); qf[2 * d + 1] = pack8(qa[d], 1);
    }
#pragma unroll
    for (int f = 0; f < 8; ++f) *(LAS bf16x8*)(qx + (f >> 2) * U + (f & 3) * 1024) = qf[f];
    BAR();
#pragma unroll
    for (int i = 0; i < 16; ++i) kf[i] = rdk(U, i);
  } else {
    issueQP(0); issueQP(1);
    WAIT_BAR(8); issueQP(2);
#pragma unroll
    for (int c = 0; c < 5; ++c) { WAIT_BAR(8); issueQP(c + 3); }
    issueK(0, U); issueK(1, 0);
    WAIT_BAR(16);
    WAIT_BAR(8);
    const Hs hs = hs_make(ws, bid, lane);
    unsigned rq;
    asm volatile("global_load_dword %0, %1, off sc1" : "=v"(rq) : "v"(hs.base + hs.mine + 32u) : "memory");
    issueK(2, SL); issueV(0, SL + U);
    asm volatile("s_waitcnt vmcnt(8) lgkmcnt(0)\n\ts_barrier" : "+v"(rq) :: "memory");
    if (wid == 4 && hs.act) __hip_atomic_store(hs.base + hs.peer + 64u, rq, RLX_AGENT);
#pragma unroll
    for (int f = 0; f < 8; ++f) qf[f] = lds_b128(qx + (f >> 2) * U + (f & 3) * 1024);
#pragma unroll
    for (int i = 0; i < 16; ++i) kf[i] = rdk(0, i);
    issueK(3, 2u * SL); issueV(1, 2u * SL + U);
    WAIT_BAR(8);
  }
  STAMP(1);

  f32x16 o[4] = {};
  float m_run = 0.f, l_run = 0.f;
  f32x16 negm = {};
  bf16x8 pf[4], vf[16];
  auto vfrag = [&](unsigned av, int ks, int d) { const unsigned a = (av ^ (unsigned)(d << 6)) + (32 * (ks & 1) + 8 * (ks >> 1)) * 256; return lds_tr2(a, a + 4096); };
  auto scores_slow = [&](bool first, float& ps) {
    f32x16 s0 = {}, s1 = {};
#pragma unroll
    for (int s = 0; s < 8; ++s) { s0 = MFMA(kf[s], qf[s], s0); s1 = MFMA(kf[8 + s], qf[s], s1); }
    float mx = fmaxf(s0[0], s1[0]);
#pragma unroll
    for (int r = 1; r < 16; ++r) mx = fmaxf(fmaxf(mx, s0[r]), s1[r]);
    mx = xhalf_max(mx);
    const float m_new = first ? mx : fmaxf(mx, m_run);
    if (!first) {
      const float alpha = __builtin_amdgcn_exp2f(m_run - m_new);
      l_run *= alpha;
#pragma unroll
      for (int d = 0; d < 4; ++d)
#pragma unroll
        for (int r = 0; r < 16; ++r) o[d][r] *= alpha;
    }
    m_run = m_new;
    ps = 0.f;
#pragma unroll
    for (int r = 0; r < 16; ++r) { negm[r] = -m_new; s0[r] = __builtin_amdgcn_exp2f(s0[r] - m_new); s1[r] = __builtin_amdgcn_exp2f(s1[r] - m_new); ps += s0[r] + s1[r]; }
    pf[0] = pack8(s0, 0); pf[1] = pack8(s0, 1); pf[2] = pack8(s1, 0); pf[3] = pack8(s1, 1);
  };
  auto scores = [&](bool first, auto inloop) {
    float ps = 0.f;
    PRIO(2);
    if (!first) {
      SBAR();
      f32x16 s0 = MFMA(kf[0], qf[0], negm);
#pragma unroll
      for (int s = 1; s < 8; ++s) { MNOP(); SBAR(); s0 = MFMA(kf[s], qf[s], s0); SBAR(); }
      SBAR();
      f32x16 s1 = MFMA(kf[8], qf[0], negm);
      SBAR();
      float pa = 0.f, pb = 0.f;
      unsigned w[16];
#define SMSTEP(S, e, wi) do { S[e] = __builtin_amdgcn_exp2f(S[e]); S[e + 1] = __builtin_amdgcn_exp2f(S[e + 1]); pa += S[e]; pb += S[e + 1]; w[wi] = cvtpk(S[e], S[e + 1]); PIN(pa); PIN(pb); PIN(w[wi]); } while (0)
#pragma unroll
      for (int s = 1; s < 8; ++s) {
        s1 = MFMA(kf[8 + s], qf[s], s1);
        SMSTEP(s0, 2 * (s - 1), s - 1);
        SBAR();
      }
      SMSTEP(s0, 14, 7);
      SBAR();
#pragma unroll
      for (int i = 0; i < 8; ++i) { SMSTEP(s1, 2 * i, 8 + i); SBAR(); }
#undef SMSTEP
      ps = pa + pb;
#pragma unroll
      for (int i = 0; i < 4; ++i) pf[i] = __builtin_bit_cast(bf16x8, (u32x4){w[4 * i], w[4 * i + 1], w[4 * i + 2], w[4 * i + 3]});
    }
    if (__builtin_expect(first || __any(!(ps <= PSLIM)), 0)) scores_slow(first, ps);
    l_run += ps;
    PIN(pf[0]); PIN(pf[1]); PIN(pf[2]); PIN(pf[3]); PIN(l_run); if (decltype(inloop)::value) PIN(negm);
    PRIO(PVPRIO);
  };

  __builtin_amdgcn_s_waitcnt(0xC07F);
  unsigned sp = 1u + g;
  for (int it = 0; it < 15; ++it) {
    const unsigned p = 2u * (unsigned)it + g + 1u;
    scores(it == 0, std::true_type{});
    WAIT_BAR(0);
    const unsigned sn = sp >= 3u ? sp - 3u : sp + 2u;
    const bool go = true;
    const unsigned ni = p + 2u, ns = sn * SL;
    if (go) { const unsigned target = tstart + PACE0 + (ni - 2u) * PACE + PSTAG;
      for (;;) { unsigned long long t_; asm volatile("s_memrealtime %0\n\ts_waitcnt lgkmcnt(0)" : "=s"(t_) :: "memory"); if ((int)((unsigned)t_ - target) >= 0) break; __builtin_amdgcn_s_sleep(2); } }
    const unsigned av = trV + sp * SL;
#pragma unroll
    for (int i = 0; i < 16; ++i) vf[i] = vfrag(av, i >> 2, i & 3);
    SBAR();
    if (go && ni <= 30u) issueK(ni + 1u, ns);
    SBAR();
#pragma unroll
    for (int i = 0; i < 8; ++i) kf[i] = rdk(sp * SL, i);
    SBAR();
#pragma unroll
    for (int i = 0; i < 8; ++i) { o[i & 3] = MFMA(vf[i], pf[i >> 2], o[i & 3]); SBAR(); MNOP(); SBAR(); }
    SBAR();
#pragma unroll
    for (int i = 8; i < 16; ++i) { o[i & 3] = MFMA(vf[i], pf[i >> 2], o[i & 3]); if (i < 12) { kf[2 * i - 8] = rdk(sp * SL, 2 * i - 8); kf[2 * i - 7] = rdk(sp * SL, 2 * i - 7); } SBAR(); MNOP(); SBAR(); }
    if (go) issueV(ni - 1u, ns + U);
    if (it == 14) issueWo(g, 3u + g);
    sp = sn;
    BAR();
  }
  scores(false, std::false_type{});
  unsigned ln; asm volatile("v_mbcnt_lo_u32_b32 %0, -1, 0\n\tv_mbcnt_hi_u32_b32 %0, -1, %0" : "=v"(ln));
  const unsigned x0 = lds0 + w4 * 8192u + ln * 16u;
  l_run = xhalf_sum(l_run);
  const unsigned stats = lds0 + 2u * SL + (ln & 31u) * 8u;
  if (ln < 32u) *(LAS f32x2*)(stats + wid * 256u) = (f32x2){m_run, l_run};
  const Hs hs = hs_make(ws, bid, ln);
  unsigned hs_r, hs_q;
  asm volatile("global_load_dword %0, %2, off offset:256 sc1\n\tglobal_load_dword %1, %3, off offset:128 sc1" : "=&v"(hs_r), "=&v"(hs_q) : "v"(hs.base + hs.mine), "v"(hs.base + hs.peer) : "memory");
  bf16x8 of0[4];
  {
    WAIT_BAR(10);
    STAMP(2);
    const unsigned av = trV + (1u + g) * SL;
#pragma unroll
    for (int ks = 0; ks < 4; ++ks) { vf[4 * ks + 2] = vfrag(av, ks, 2); vf[4 * ks + 3] = vfrag(av, ks, 3); }
    SBAR();
#pragma unroll
    for (int ks = 0; ks < 4; ++ks) { vf[4 * ks] = vfrag(av, ks, 0); vf[4 * ks + 1] = vfrag(av, ks, 1); }
    SBAR();
#pragma unroll
    for (int ks = 0; ks < 4; ++ks) { o[2] = MFMA(vf[4 * ks + 2], pf[ks], o[2]); o[3] = MFMA(vf[4 * ks + 3], pf[ks], o[3]); }
    SBAR();
#pragma unroll
    for (int ks = 0; ks < 2; ++ks) { o[0] = MFMA(vf[4 * ks], pf[ks], o[0]); o[1] = MFMA(vf[4 * ks + 1], pf[ks], o[1]); }
    SBAR();
    const unsigned xo = x0 + g * SL, xi = x0 + (1u - g) * SL;
#pragma unroll
    for (int q = 0; q < 4; ++q) { lds_st4(xo + q * 1024, o[2], q); lds_st4(xo + 4096 + q * 1024, o[3], q); }
    SBAR();
#pragma unroll
    for (int ks = 2; ks < 4; ++ks) { o[0] = MFMA(vf[4 * ks], pf[ks], o[0]); o[1] = MFMA(vf[4 * ks + 1], pf[ks], o[1]); }
    SBAR();
    if (g == 0) BAR();
    const f32x2 st = *(const LAS f32x2*)(stats + (wid ^ 4u) * 256u);
    const float ms = fmaxf(m_run, st[0]);
    const float e_me = __builtin_amdgcn_exp2f(m_run - ms), e_ot = __builtin_amdgcn_exp2f(st[0] - ms);
    const float inv = 1.f / (l_run * e_me + st[1] * e_ot);
    float a_me = e_me * inv, a_ot = e_ot * inv;
    PIN(a_me); PIN(a_ot);
    asm volatile("s_waitcnt vmcnt(0) lgkmcnt(0)\n\ts_barrier" : "+v"(hs_r), "+v"(hs_q) :: "memory");
    f32x4 w0[4], w1[4];
#pragma unroll
    for (int q = 0; q < 4; ++q) { w0[q] = lds_f4(xi + q * 1024); w1[q] = lds_f4(xi + 4096 + q * 1024); }
#pragma unroll
    for (int q = 0; q < 4; ++q)
#pragma unroll
      for (int e = 0; e < 4; ++e) { o[0][4 * q + e] = fmaf(w0[q][e], a_ot, o[0][4 * q + e] * a_me); o[1][4 * q + e] = fmaf(w1[q][e], a_ot, o[1][4 * q + e] * a_me); }
    of0[0] = pack8(o[0], 0); of0[1] = pack8(o[0], 1); of0[2] = pack8(o[1], 0); of0[3] = pack8(o[1], 1);
  }
  PRIO(0);
  const __amdgpu_buffer_rsrc_t srdO = __builtin_amdgcn_make_buffer_rsrc((void*)(ws + WS_O + (size_t)(hg * 8u + b) * 131072u), (short)0, 4 * 32768, 0x00020000);
#pragma unroll
  for (int s = 0; s < 4; ++s) __builtin_amdgcn_raw_buffer_store_b128(__builtin_bit_cast(u32x4, of0[s]), srdO, j * 32768u + wid * 4096u + (unsigned)s * 1024u + ln * 16u, 0, 16);
  STAMP(3);
  f32x16 c[4] = {};
  const unsigned trW2 = trV - U + (ln >> 5) * 1024u + g * U;
  auto wfrag = [&](unsigned slot, int s, int nb) { const unsigned a = ((trW2 + slot * SL) ^ (unsigned)(nb << 6)) + s * 4096; return lds_tr2(a, a + 1024); };
  bf16x8 w1[16], w2[16];
#pragma unroll
  for (int i = 0; i < 16; ++i) w2[i] = wfrag(3, i >> 2, i & 3);
  SBAR();
#pragma unroll
  for (int i = 0; i < 16; ++i) { c[i & 3] = MFMA(w2[i], of0[i >> 2], c[i & 3]); w1[i] = wfrag(4, i >> 2, i & 3); SBAR(); }
  PIN(c[0]); PIN(c[1]); PIN(c[2]); PIN(c[3]);
  WAIT_BAR(0);
  STAMP(4);
  if (wid == 0) {
    if (!__all(hs_r == hs_q || !hs.act)) hs_wait_armed(hs);
    if (ln < 4u) __hip_atomic_store(hs.base + hs.peer, 1u, RLX_AGENT);
  }
  issueWo(2u + g, 1u - g);
  if (wid == 0) hs_wait_flags(hs, ln < 4u);
  WAIT_BAR(0);
  STAMP(5);
  const unsigned xch = lds0 + 4u * SL;
  const __amdgpu_buffer_rsrc_t srdP = __builtin_amdgcn_make_buffer_rsrc((void*)(ws + WS_P + (size_t)(b * 4u + j) * 262144u), (short)0, 262144, 0x00020000);
  {
    bf16x8 of[12];
#pragma unroll
    for (int i = 0; i < 12; ++i)
      of[i] = __builtin_bit_cast(bf16x8, __builtin_amdgcn_raw_buffer_load_b128(srdO, wid * 4096u + (unsigned)(i & 3) * 1024u + ln * 16u, ((j + 1u + (unsigned)(i >> 2)) & 3u) * 32768u, 16));
    SBAR();
#pragma unroll
    for (int i = 0; i < 8; ++i) w2[i] = wfrag(1, i >> 2, i & 3);
    SBAR();
#pragma unroll
    for (int i = 0; i < 16; ++i) { c[i & 3] = MFMA(w1[i], of[i >> 2], c[i & 3]); if (i >= 8) w2[i] = wfrag(1, i >> 2, i & 3); SBAR(); }
#pragma unroll
    for (int i = 0; i < 16; ++i) { c[i & 3] = MFMA(w2[i], of[4 + (i >> 2)], c[i & 3]); w1[i] = wfrag(0, (i >> 1) & 3, (i & 1) + (i < 8 ? 2 : 0)); SBAR(); }
#pragma unroll
    for (int i = 0; i < 8; ++i) c[2 + (i & 1)] = MFMA(w1[i], of[8 + (i >> 1)], c[2 + (i & 1)]);
    SBAR();
#pragma unroll
    for (int i = 0; i < 4; ++i) c[0] = MFMA(w1[8 + 2 * i], of[8 + i], c[0]);
    SBAR();
#pragma unroll
    for (int q = 0; q < 4; ++q) { lds_st4(x0 + (2u + g) * SL + q * 1024, c[2], q); lds_st4(x0 + (2u + g) * SL + 4096 + q * 1024, c[3], q); }
    SBAR();
#pragma unroll
    for (int i = 0; i < 4; ++i) c[1] = MFMA(w1[9 + 2 * i], of[8 + i], c[1]);
    PIN(c[0]); PIN(c[1]);
    BAR();
    STAMP(6);
    const unsigned ra = x0 + (3u - g) * SL;
    f32x4 va[8];
#pragma unroll
    for (int q = 0; q < 8; ++q) va[q] = lds_f4(ra + q * 1024);
    SBAR();
    auto put = [&](int nb) {
      float mx = 0.f;
#pragma unroll
      for (int r = 0; r < 16; ++r) { c[nb][r] += va[4 * nb + (r >> 2)][r & 3]; mx = fmaxf(mx, fabsf(c[nb][r])); }
      const unsigned e = max(wave_umax(__float_as_uint(mx)) >> 23, 15u);
      const float sc = __uint_as_float((268u - e) << 23);
#pragma unroll
      for (int s = 0; s < 2; ++s) {
        u32x4 w;
#pragma unroll
        for (int q = 0; q < 4; ++q) { f32x2 v = {c[nb][8 * s + 2 * q] * sc, c[nb][8 * s + 2 * q + 1] * sc}; w[q] = __builtin_bit_cast(unsigned, __builtin_convertvector(v, f16x2)); }
        __builtin_amdgcn_raw_buffer_store_b128(w, srdP, hg * 32768u + wid * 4096u + (unsigned)(2 * nb + s) * 1024u + ln * 16u, 0, 16);
      }
      if (ln == 0) *(LAS unsigned char*)(xch + wid * 4u + (unsigned)nb) = (unsigned char)e;
    };
    put(0);
    put(1);
  }
  STAMP(7);
  WAIT_BAR(0);
  STAMP(8);
  if (wid == 0) {
    const bool sel = hs.act && ln >= 8u;
    if (sel) __hip_atomic_store(hs.base + hs.peer, (*(const LAS unsigned*)(xch + (ln & 7u) * 4u) & 0xFFFFu) | 0x10000u, RLX_AGENT);
    const unsigned fv = hs_wait_flags(hs, sel);
    if (sel) *(LAS unsigned*)(xch + 64u + (ln & 7u) * 4u) = fv;
  }
  BAR();
  STAMP(9);
  if (g == 0) {
    u32x4 pv[8];
#pragma unroll
    for (int u = 0; u < 8; ++u) pv[u] = __builtin_amdgcn_raw_buffer_load_b128(srdP, hg * 4096u + w4 * 1024u + ln * 16u, (unsigned)u * 32768u, 16);
    const u32x4 f0 = *(const LAS u32x4*)(xch + 64u), f1 = *(const LAS u32x4*)(xch + 80u);
    float acc[8] = {};
#pragma unroll
    for (int u = 0; u < 8; ++u) {
      const unsigned e = ((u < 4 ? f0[u & 3] : f1[u & 3]) >> (8u * (w4 >> 1))) & 0xFFu;
      const float isc = __uint_as_float((e - 14u) << 23);
#pragma unroll
      for (int q = 0; q < 4; ++q) { const unsigned wv = pv[u][q]; const f16x2 hv = __builtin_bit_cast(f16x2, wv); acc[2 * q] = fmaf((float)hv[0], isc, acc[2 * q]); acc[2 * q + 1] = fmaf((float)hv[1], isc, acc[2 * q + 1]); }
    }
    const unsigned qrow = (hg & 3u) * 32u + (ln & 31u), n = j * 128u + ((hg >> 2) * 2u + (w4 >> 1)) * 32u + (w4 & 1u) * 16u + (ln >> 5) * 8u;
    *(u32x4*)(out + ((size_t)b * SQ + qrow) * DM + n) = (u32x4){cvtpk(acc[0], acc[1]), cvtpk(acc[2], acc[3]), cvtpk(acc[4], acc[5]), cvtpk(acc[6], acc[7])};
  }
  STAMP(10);
}

extern "C" void kernel_launch(void* const* d_in, const int* in_sizes, int n_in, void* d_out, int out_size, void* d_ws, size_t ws_size, hipStream_t stream) {
  (void)in_sizes; (void)n_in; (void)out_size; (void)ws_size;
  (void)hipFuncSetAttribute((const void*)mha_fused, hipFuncAttributeMaxDynamicSharedMemorySize, (int)LDS_BYTES);
  hipLaunchKernelGGL(mha_fused, dim3(256), dim3(512), LDS_BYTES, stream, (const unsigned short*)d_in[0], (const unsigned short*)d_in[1], (const unsigned short*)d_in[2],
                     (const unsigned short*)d_in[3], (const unsigned short*)d_in[4], (unsigned short*)d_out, (char*)d_ws);
}
```

```cpp
#define KPOS 2
#define QSLOW 4
#define QNOP() asm volatile("s_nop 12")
#include <hip/hip_runtime.h>
#include <hip/hip_bf16.h>
#include <stdint.h>
#include <type_traits>

typedef __bf16 bf16x8 __attribute__((ext_vector_type(8)));
typedef __bf16 bf16x2 __attribute__((ext_vector_type(2)));
typedef _Float16 f16x2 __attribute__((ext_vector_type(2)));
typedef short s16x4 __attribute__((ext_vector_type(4)));
typedef float f32x16 __attribute__((ext_vector_type(16)));
typedef float f32x4 __attribute__((ext_vector_type(4)));
typedef float f32x2 __attribute__((ext_vector_type(2)));
typedef unsigned u32x4 __attribute__((ext_vector_type(4)));
typedef unsigned u32x2 __attribute__((ext_vector_type(2)));
#define LAS __attribute__((address_space(3)))
#define GAS __attribute__((address_space(1)))
#define DI __device__ __forceinline__
#define MFMA(a, b, c) __builtin_amdgcn_mfma_f32_32x32x16_bf16((a), (b), (c), 0, 0, 0)
#define RLX_AGENT __ATOMIC_RELAXED, __HIP_MEMORY_SCOPE_AGENT
#define PIN(x) asm volatile("" : "+v"(x))
#define SBAR() __builtin_amdgcn_sched_barrier(0)
#define PRIO(n) __builtin_amdgcn_s_setprio(n)

constexpr int SQ = 128, DM = 512, NH = 32, SK = 2048, DH = 128;
constexpr unsigned U = 16384u, SL = 32768u;
constexpr unsigned LDS_BYTES = 5 * SL;
#define CODEPF 15
#define PACE 112u
#define PACE0 430u
#define PSTAG (((bid >> 3) * PACE) >> 5)
#define MNOP() do { } while (0)
#define PLAUX 18
#define PVPRIO 0
#define PSLIM 8192.0f
constexpr float CSC = 0.088388347648318447f * 1.4426950408889634f;
constexpr size_t WS_FLAG = 0;
constexpr unsigned FB = 128;
constexpr size_t WS_O = 1 << 20;
constexpr size_t WS_P = WS_O + (8u << 20);
constexpr size_t WS_END = WS_P + (16u << 20);

DI unsigned rfl(unsigned x) { return __builtin_amdgcn_readfirstlane(x); }
DI unsigned cvtpk(float lo, float hi) { f32x2 v = {lo, hi}; bf16x2 b = __builtin_convertvector(v, bf16x2); return __builtin_bit_cast(unsigned, b); }
DI bf16x8 pack8(const f32x16& a, int s) {
  u32x4 w = {cvtpk(a[8 * s], a[8 * s + 1]), cvtpk(a[8 * s + 2], a[8 * s + 3]), cvtpk(a[8 * s + 4], a[8 * s + 5]), cvtpk(a[8 * s + 6], a[8 * s + 7])};
  return __builtin_bit_cast(bf16x8, w);
}
DI bf16x8 lds_b128(unsigned a) { return *(const LAS bf16x8*)a; }
DI f32x4 lds_f4(unsigned a) { return *(const LAS f32x4*)a; }
DI void lds_st4(unsigned a, const f32x16& v, int q) { *(LAS f32x4*)a = (f32x4){v[4 * q], v[4 * q + 1], v[4 * q + 2], v[4 * q + 3]}; }
DI s16x4 lds_tr(unsigned a) { return __builtin_amdgcn_ds_read_tr16_b64_v4i16((LAS s16x4*)a); }
DI bf16x8 lds_tr2(unsigned a0, unsigned a1) {
  s16x4 lo = lds_tr(a0), hi = lds_tr(a1);
  return __builtin_bit_cast(bf16x8, __builtin_shufflevector(lo, hi, 0, 1, 2, 3, 4, 5, 6, 7));
}
DI void dma4_kv(unsigned m0v, unsigned voff, __amdgpu_buffer_rsrc_t srd, unsigned soff) {
  unsigned keep;
  asm volatile("s_mov_b32 %0, m0\n\ts_mov_b32 m0, %1\n\ts_nop 3\n\t"
               "buffer_load_dwordx4 %2, %3, %4 offen nt lds\n\t"
               "buffer_load_dwordx4 %2, %3, %4 offen offset:1024 nt lds\n\t"
               "buffer_load_dwordx4 %2, %3, %4 offen offset:2048 nt lds\n\t"
               "buffer_load_dwordx4 %2, %3, %4 offen offset:3072 nt lds\n\t"
               "s_mov_b32 m0, %0"
               : "=&s"(keep) : "s"(rfl(m0v)), "v"(voff), "s"(srd), "s"(rfl(soff)) : "memory");
}
DI void dma4_def(unsigned m0v, unsigned voff, __amdgpu_buffer_rsrc_t srd, unsigned soff) {
  unsigned keep;
  asm volatile("s_mov_b32 %0, m0\n\ts_mov_b32 m0, %1\n\ts_nop 3\n\t"
               "buffer_load_dwordx4 %2, %3, %4 offen lds\n\t"
               "buffer_load_dwordx4 %2, %3, %4 offen offset:1024 lds\n\t"
               "buffer_load_dwordx4 %2, %3, %4 offen offset:2048 lds\n\t"
               "buffer_load_dwordx4 %2, %3, %4 offen offset:3072 lds\n\t"
               "s_mov_b32 m0, %0"
               : "=&s"(keep) : "s"(rfl(m0v)), "v"(voff), "s"(srd), "s"(rfl(soff)) : "memory");
}
#define DMA_K dma4_kv
#define DMA_V dma4_kv
DI void dma1(unsigned m0v, unsigned voff, __amdgpu_buffer_rsrc_t srd, unsigned soff) {
  unsigned keep;
  asm volatile("s_mov_b32 %0, m0\n\ts_mov_b32 m0, %1\n\ts_nop 3\n\t"
               "buffer_load_dwordx4 %2, %3, %4 offen lds\n\t"
               "s_mov_b32 m0, %0"
               : "=&s"(keep) : "s"(rfl(m0v)), "v"(voff), "s"(srd), "s"(rfl(soff)) : "memory");
}
DI void dma1nt(unsigned m0v, unsigned voff, __amdgpu_buffer_rsrc_t srd, unsigned soff) {
  unsigned keep;
  asm volatile("s_mov_b32 %0, m0\n\ts_mov_b32 m0, %1\n\ts_nop 3\n\t"
               "buffer_load_dwordx4 %2, %3, %4 offen nt lds\n\t"
               "s_mov_b32 m0, %0"
               : "=&s"(keep) : "s"(rfl(m0v)), "v"(voff), "s"(srd), "s"(rfl(soff)) : "memory");
}
#define DMAWQ dma1nt
#define DMAX dma1
#define WAIT_BAR(N) asm volatile("s_waitcnt vmcnt(" #N ") lgkmcnt(0)\n\ts_barrier" ::: "memory")
#define BAR() asm volatile("s_waitcnt lgkmcnt(0)\n\ts_barrier" ::: "memory")
#define WAITVM(N) asm volatile("s_waitcnt vmcnt(" #N ")" ::: "memory")

DI float xhalf_max(float v) { auto r = __builtin_amdgcn_permlane32_swap(__float_as_uint(v), __float_as_uint(v), false, false); return fmaxf(__uint_as_float(r[0]), __uint_as_float(r[1])); }
DI float xhalf_sum(float v) { auto r = __builtin_amdgcn_permlane32_swap(__float_as_uint(v), __float_as_uint(v), false, false); return __uint_as_float(r[0]) + __uint_as_float(r[1]); }

DI unsigned wave_umax(unsigned v) {
#define DPPMAX(ctrl, rmask) v = max(v, (unsigned)__builtin_amdgcn_update_dpp(0, (int)v, ctrl, rmask, 0xF, false))
  DPPMAX(0x111, 0xF); DPPMAX(0x112, 0xF); DPPMAX(0x114, 0xF); DPPMAX(0x118, 0xF);
  DPPMAX(0x142, 0xA); DPPMAX(0x143, 0xC);
#undef DPPMAX
  return (unsigned)__builtin_amdgcn_readlane((int)v, 63);
}
struct Hs { GAS unsigned* base; unsigned mine, peer; bool act; };
DI Hs hs_make(char* ws, unsigned bid, unsigned L) {
  Hs h; h.base = (GAS unsigned*)(ws + WS_FLAG);
  const bool h1 = L < 4u; h.act = h1 || (L >= 8u && L < 16u);
  const unsigned pb = h1 ? ((bid & ~24u) | (L << 3)) : ((bid & ~7u) | (L & 7u));
  h.mine = bid * FB + (L & 15u); h.peer = pb * FB + (h1 ? ((bid >> 3) & 3u) : 8u + (bid & 7u));
  return h;
}
DI void hs_echo(const Hs& h) { if (h.act) __hip_atomic_store(h.base + h.peer + 64u, __hip_atomic_load(h.base + h.mine + 32u, RLX_AGENT), RLX_AGENT); }
DI void hs_wait_armed(const Hs& h) {
#pragma nounroll
  for (unsigned spins = 0; spins < (1u << 20); ++spins) {
    hs_echo(h);
    bool ok = true;
    if (h.act) ok = __hip_atomic_load(h.base + h.mine + 64u, RLX_AGENT) == __hip_atomic_load(h.base + h.peer + 32u, RLX_AGENT);
    if (__all(ok)) break;
    __builtin_amdgcn_s_sleep(4);
  }
}
DI unsigned hs_wait_flags(const Hs& h, bool sel) {
  unsigned v = 1u;
#pragma nounroll
  for (unsigned spins = 0; spins < (1u << 20); ++spins) {
    if (sel) v = __hip_atomic_load(h.base + h.mine, RLX_AGENT);
    if (__all(v != 0u)) break;
    if ((spins & 31u) == 31u) hs_echo(h);
    __builtin_amdgcn_s_sleep(1);
  }
  asm volatile("" ::: "memory");
  return v;
}

#define STAMP(i) do { } while (0)

__global__ __launch_bounds__(512, 2) void mha_fused(const unsigned short* __restrict__ x, const unsigned short* __restrict__ kc, const unsigned short* __restrict__ vc,
                                                    const unsigned short* __restrict__ wq, const unsigned short* __restrict__ wo, unsigned short* __restrict__ out, char* __restrict__ ws) {
  extern __shared__ __attribute__((aligned(16))) char lds[];
  const unsigned tid = threadIdx.x, lane = tid & 63u, r32 = lane & 31u, hi = lane >> 5;
  const unsigned wid = rfl(tid >> 6), g = wid >> 2, w4 = wid & 3u;
  const unsigned bid = blockIdx.x, hg = bid & 7u, b = bid >> 5, j = (bid >> 3) & 3u, h = hg * 4u + j;
  const unsigned lds0 = (unsigned)(uintptr_t)lds;
  STAMP(0);
  const unsigned tref = (unsigned)__builtin_amdgcn_s_memrealtime() + PACE0 + PSTAG;
  auto due = [&](unsigned t) { unsigned long long t_; asm volatile("s_memrealtime %0\n\ts_waitcnt lgkmcnt(0)" : "=s"(t_) :: "memory"); return (int)((unsigned)t_ - tref - t) >= 0; };
  auto pace = [&](unsigned t) { while (!due(t)) __builtin_amdgcn_s_sleep(2); };
  asm volatile("" :: "s"(out), "s"(ws), "s"(wo));

  const __amdgpu_buffer_rsrc_t srdK = __builtin_amdgcn_make_buffer_rsrc((void*)(kc + (size_t)(b * NH + h) * SK * DH), (short)0, SK * DH * 2, 0x00020000);
  const __amdgpu_buffer_rsrc_t srdV = __builtin_amdgcn_make_buffer_rsrc((void*)(vc + (size_t)(b * NH + h) * SK * DH), (short)0, SK * DH * 2, 0x00020000);
  const __amdgpu_buffer_rsrc_t srdX = __builtin_amdgcn_make_buffer_rsrc((void*)(x + (size_t)b * SQ * DM), (short)0, SQ * DM * 2, 0x00020000);
  const __amdgpu_buffer_rsrc_t srdWq = __builtin_amdgcn_make_buffer_rsrc((void*)wq, (short)0, DM * NH * DH * 2, 0x00020000);
  const __amdgpu_buffer_rsrc_t srdWo = __builtin_amdgcn_make_buffer_rsrc((void*)(wo + (size_t)hg * 512 * DM), (short)0, 512 * DM * 2, 0x00020000);

  const unsigned l4 = lane >> 4, l15 = lane & 15u;
  const unsigned voffK = w4 * 4096u + l4 * 256u + ((l15 ^ (l4 | (w4 << 2))) << 4);
  const unsigned voffV = w4 * 4096u + l4 * 256u + ((l15 ^ (l4 << 2)) << 4);
  const unsigned voffWq = l4 * 8192u + ((l15 ^ (l4 << 2)) << 4);
  const unsigned voffWo = l4 * 1024u + ((l15 ^ (l4 << 2)) << 4);
  const unsigned voffX = (lane >> 3) * 1024u + (((lane & 7u) ^ (((lane >> 4) & 1u) | (w4 << 1))) << 4);

  auto issueK = [&](unsigned t, unsigned off) { DMA_K(lds0 + off + w4 * 4096u, voffK, srdK, t * U); };
  auto issueV = [&](unsigned t, unsigned off) { DMA_V(lds0 + off + w4 * 4096u, voffV, srdV, t * U); };
  auto issueWo = [&](unsigned cc, unsigned slot) {
    const unsigned c = (j + cc) & 3u;
#pragma unroll
    for (int q = 0; q < 8; ++q) dma1(lds0 + slot * SL + (w4 * 8u + q) * 1024u, voffWo, srdWo, (c * 128u + w4 * 32u + q * 4u) * 1024u + j * 256u);
  };
#define ROT ((b + 2u * j) & 7u)
  const unsigned rot = ROT;
  auto qslot = [](int c) { return (c % 3 == 0) ? 3u : (c % 3 == 1) ? 4u : 2u; };
  auto issueQP = [&](int c) {
    const unsigned kcn = ((unsigned)c + rot) & 7u, sb = lds0 + qslot(c) * SL;
#pragma unroll
    for (int q = 0; q < 4; ++q) {
      DMAWQ(sb + (w4 * 4u + q) * 1024u, voffWq, srdWq, (kcn * 64u + w4 * 16u + q * 4u) * 8192u + h * 256u);
      DMAX(sb + U + (q * 4u + w4) * 1024u, voffX, srdX, (q * 4u + w4) * 8192u + kcn * 128u);
    }
  };
  const unsigned blk = (lane >> 4) & 1u, q4 = l15 >> 2, p4 = lane & 3u, pp = ((p4 & 1u) << 1) | (p4 >> 1);
  const unsigned trV = lds0 + U + (4u * hi + q4) * 256u + ((q4 ^ (2u * g)) << 6) + blk * 32u + pp * 8u;
  const unsigned rdK = lds0 + ((r32 & 7u) + 16u * (r32 >> 3)) * 256u + ((hi ^ ((r32 & 3u) | ((r32 >> 3) << 2))) << 4);
  const unsigned qx = lds0 + 3u * SL + w4 * 4096u + lane * 16u;

  bf16x8 qf[8], kf[16];
  auto rdk = [&](unsigned off, int i) { return lds_b128(((rdK + off) ^ (unsigned)((i & 7) << 5)) + (i >> 3) * 2048); };
  if (g == 0) {
    const unsigned trW = lds0 + (8u * hi + q4) * 256u + (q4 << 6) + blk * 32u + pp * 8u;
    const unsigned rdX = lds0 + U + (w4 * 32u + r32) * 128u + ((hi ^ (((r32 >> 1) & 1u) | ((r32 >> 3) << 1))) << 4);
    f32x16 qa[4] = {};
    bf16x8 xb[2][4], wa_[2][16];
    auto rdq = [&](int c, int s) {
      const unsigned u0 = qslot(c) * SL;
      xb[c & 1][s] = lds_b128((rdX + u0) ^ (unsigned)(s << 5));
#pragma unroll
      for (int d = 0; d < 4; ++d) { const unsigned a = ((trW + u0) ^ (unsigned)(d << 6)) + s * 4096; wa_[c & 1][4 * s + d] = lds_tr2(a, a + 1024); }
    };
    {
      unsigned long long pc; asm volatile("s_getpc_b64 %0" : "=s"(pc));
      const __amdgpu_buffer_rsrc_t srdC = __builtin_amdgcn_make_buffer_rsrc((void*)(pc & ~255ull), (short)0, CODEPF * 1024, 0x00020000);
      if (wid == 1) dma1(lds0, lane * 16u, srdC, ((bid >> 3) % CODEPF) * 1024u);
    }
    unsigned r0;
    { const Hs hs = hs_make(ws, bid, lane);
      if (wid == 0 && hs.act) __hip_atomic_store(hs.base + hs.mine, 0u, RLX_AGENT);
      asm volatile("global_load_dword %0, %1, off offset:256 sc1" : "=v"(r0) : "v"(hs.base + hs.mine) : "memory"); }
    BAR();
#pragma unroll
    for (int s = 0; s < 4; ++s) rdq(0, s);
#pragma unroll
    for (int c = 0; c < 8; ++c) {
      if (c == 2) {
        asm volatile("s_waitcnt vmcnt(0) lgkmcnt(0)\n\ts_barrier" : "+v"(r0) :: "memory");
        const Hs hs = hs_make(ws, bid, lane);
        if (wid == 0 && hs.act) __hip_atomic_store(hs.base + hs.peer + 32u, r0 + 1u, RLX_AGENT);
      } else if (c < 7) BAR();
#pragma unroll
      for (int s = 0; s < 4; ++s)
#pragma unroll
        for (int d = 0; d < 4; ++d) {
          SBAR();
          qa[d] = MFMA(wa_[c & 1][4 * s + d], xb[c & 1][s], qa[d]);
          SBAR();
          if (c >= QSLOW) { QNOP(); SBAR(); }
          if (c < 7) {
            const unsigned u0 = qslot(c + 1) * SL, a = ((trW + u0) ^ (unsigned)(d << 6)) + s * 4096;
            if (d == 0) xb[(c + 1) & 1][s] = lds_b128((rdX + u0) ^ (unsigned)(s << 5));
            wa_[(c + 1) & 1][4 * s + d] = lds_tr2(a, a + 1024);
          }
        }
      SBAR();
    }
#pragma unroll
    for (int d = 0; d < 4; ++d) {
#pragma unroll
      for (int r = 0; r < 16; ++r) qa[d][r] *= CSC;
      qf[2 * d] = pack8(qa[d], 0); qf[2 * d + 1] = pack8(qa[d], 1);
    }
#pragma unroll
    for (int f = 0; f < 8; ++f) *(LAS bf16x8*)(qx + (f >> 2) * U + (f & 3) * 1024) = qf[f];
    BAR();
#pragma unroll
    for (int i = 0; i < 16; ++i) kf[i] = rdk(U, i);
  } else {
    issueQP(0); issueQP(1);
    WAIT_BAR(8); issueQP(2);
#pragma unroll
    for (int c = 0; c < 5; ++c) { WAIT_BAR(8); issueQP(c + 3); }
    issueK(0, U); issueK(1, 0);
    WAIT_BAR(16);
    WAIT_BAR(8);
    const Hs hs = hs_make(ws, bid, lane);
    unsigned rq;
    asm volatile("global_load_dword %0, %1, off sc1" : "=v"(rq) : "v"(hs.base + hs.mine + 32u) : "memory");
    issueK(2, SL); issueV(0, SL + U);
    asm volatile("s_waitcnt vmcnt(8) lgkmcnt(0)\n\ts_barrier" : "+v"(rq) :: "memory");
    if (wid == 4 && hs.act) __hip_atomic_store(hs.base + hs.peer + 64u, rq, RLX_AGENT);
#pragma unroll
    for (int f = 0; f < 8; ++f) qf[f] = lds_b128(qx + (f >> 2) * U + (f & 3) * 1024);
#pragma unroll
    for (int i = 0; i < 16; ++i) kf[i] = rdk(0, i);
    issueK(3, 2u * SL); issueV(1, 2u * SL + U);
    pace(3u * PACE - PACE / 2u);
    issueK(4, 3u * SL);
    pace(3u * PACE);
    issueV(2, 3u * SL + U);
    WAIT_BAR(16);
  }
  STAMP(1);

  f32x16 o[4] = {};
  float m_run = 0.f, l_run = 0.f;
  f32x16 negm = {};
  bf16x8 pf[4], vf[16];
  auto vfrag = [&](unsigned av, int ks, int d) { const unsigned a = (av ^ (unsigned)(d << 6)) + (32 * (ks & 1) + 8 * (ks >> 1)) * 256; return lds_tr2(a, a + 4096); };
  auto scores_slow = [&](bool first, float& ps) {
    f32x16 s0 = {}, s1 = {};
#pragma unroll
    for (int s = 0; s < 8; ++s) { s0 = MFMA(kf[s], qf[s], s0); s1 = MFMA(kf[8 + s], qf[s], s1); }
    float mx = fmaxf(s0[0], s1[0]);
#pragma unroll
    for (int r = 1; r < 16; ++r) mx = fmaxf(fmaxf(mx, s0[r]), s1[r]);
    mx = xhalf_max(mx);
    const float m_new = first ? mx : fmaxf(mx, m_run);
    if (!first) {
      const float alpha = __builtin_amdgcn_exp2f(m_run - m_new);
      l_run *= alpha;
#pragma unroll
      for (int d = 0; d < 4; ++d)
#pragma unroll
        for (int r = 0; r < 16; ++r) o[d][r] *= alpha;
    }
    m_run = m_new;
    ps = 0.f;
#pragma unroll
    for (int r = 0; r < 16; ++r) { negm[r] = -m_new; s0[r] = __builtin_amdgcn_exp2f(s0[r] - m_new); s1[r] = __builtin_amdgcn_exp2f(s1[r] - m_new); ps += s0[r] + s1[r]; }
    pf[0] = pack8(s0, 0); pf[1] = pack8(s0, 1); pf[2] = pack8(s1, 0); pf[3] = pack8(s1, 1);
  };
  auto scores = [&](bool first, auto inloop) {
    float ps = 0.f;
    PRIO(2);
    if (!first) {
      SBAR();
      f32x16 s0 = MFMA(kf[0], qf[0], negm);
#pragma unroll
      for (int s = 1; s < 8; ++s) { MNOP(); SBAR(); s0 = MFMA(kf[s], qf[s], s0); SBAR(); }
      SBAR();
      f32x16 s1 = MFMA(kf[8], qf[0], negm);
      SBAR();
      float pa = 0.f, pb = 0.f;
      unsigned w[16];
#define SMSTEP(S, e, wi) do { S[e] = __builtin_amdgcn_exp2f(S[e]); S[e + 1] = __builtin_amdgcn_exp2f(S[e + 1]); pa += S[e]; pb += S[e + 1]; w[wi] = cvtpk(S[e], S[e + 1]); PIN(pa); PIN(pb); PIN(w[wi]); } while (0)
#pragma unroll
      for (int s = 1; s < 8; ++s) {
        s1 = MFMA(kf[8 + s], qf[s], s1);
        SMSTEP(s0, 2 * (s - 1), s - 1);
        SBAR();
      }
      SMSTEP(s0, 14, 7);
      SBAR();
#pragma unroll
      for (int i = 0; i < 8; ++i) { SMSTEP(s1, 2 * i, 8 + i); SBAR(); }
#undef SMSTEP
      ps = pa + pb;
#pragma unroll
      for (int i = 0; i < 4; ++i) pf[i] = __builtin_bit_cast(bf16x8, (u32x4){w[4 * i], w[4 * i + 1], w[4 * i + 2], w[4 * i + 3]});
    }
    if (__builtin_expect(first || __any(!(ps <= PSLIM)), 0)) scores_slow(first, ps);
    l_run += ps;
    PIN(pf[0]); PIN(pf[1]); PIN(pf[2]); PIN(pf[3]); PIN(l_run); if (decltype(inloop)::value) PIN(negm);
    PRIO(PVPRIO);
  };

  __builtin_amdgcn_s_waitcnt(0xC07F);
  unsigned sp = 1u + g;
  const unsigned p0 = 3u + ((((bid >> 3) * 11u) & 31u) * 3u >> 2);
  for (int it = 0; it < 15; ++it) {
    const unsigned p = 2u * (unsigned)it + g + 1u;
    scores(it == 0, std::true_type{});
    unsigned long long ts;
    asm volatile("s_waitcnt vmcnt(0) lgkmcnt(0)\n\ts_barrier\n\ts_memrealtime %0" : "=s"(ts) :: "memory");
    const unsigned sn = sp >= 3u ? sp - 3u : sp + 2u;
    const bool go = p != p0;
    const unsigned ni = p + (p < p0 ? 3u : 2u), ns = (p < p0 ? (sn == 4u ? 0u : sn + 1u) : sn) * SL;
    const unsigned tni = ni <= 30u ? ni * PACE : 30u * PACE + (ni - 30u) * (PACE / 2u);
    bool kgo = go && ni <= 30u;
    const unsigned av = trV + sp * SL;
#pragma unroll
    for (int i = 0; i < 16; ++i) vf[i] = vfrag(av, i >> 2, i & 3);
    SBAR();
    SBAR();
#pragma unroll
    for (int i = 0; i < 8; ++i) kf[i] = rdk(sp * SL, i);
    SBAR();
#pragma unroll
    for (int i = 0; i < KPOS; ++i) { o[i & 3] = MFMA(vf[i], pf[i >> 2], o[i & 3]); SBAR(); }
    asm volatile("s_waitcnt lgkmcnt(0)" : "+s"(ts) :: "memory");
    const int late = (int)((unsigned)ts - tref - tni);
    if (kgo && late + (int)(PACE / 2u) + 15 >= 0) { issueK(ni + 1u, ns); kgo = false; }
    SBAR();
#pragma unroll
    for (int i = KPOS; i < 8; ++i) { o[i & 3] = MFMA(vf[i], pf[i >> 2], o[i & 3]); SBAR(); }
#pragma unroll
    for (int i = 8; i < 16; ++i) { o[i & 3] = MFMA(vf[i], pf[i >> 2], o[i & 3]); if (i < 12) { kf[2 * i - 8] = rdk(sp * SL, 2 * i - 8); kf[2 * i - 7] = rdk(sp * SL, 2 * i - 7); } SBAR(); }
    if (kgo) { pace(tni - PACE / 2u); issueK(ni + 1u, ns); }
    if (go) { if (late + 35 < 0) pace(tni); issueV(ni - 1u, ns + U); }
    if (it == 14) issueWo(g, 3u + g);
    sp = sn;
    BAR();
  }
  scores(false, std::false_type{});
  unsigned ln; asm volatile("v_mbcnt_lo_u32_b32 %0, -1, 0\n\tv_mbcnt_hi_u32_b32 %0, -1, %0" : "=v"(ln));
  const unsigned x0 = lds0 + w4 * 8192u + ln * 16u;
  l_run = xhalf_sum(l_run);
  const unsigned stats = lds0 + 2u * SL + (ln & 31u) * 8u;
  if (ln < 32u) *(LAS f32x2*)(stats + wid * 256u) = (f32x2){m_run, l_run};
  const Hs hs = hs_make(ws, bid, ln);
  unsigned hs_r, hs_q;
  asm volatile("global_load_dword %0, %2, off offset:256 sc1\n\tglobal_load_dword %1, %3, off offset:128 sc1" : "=&v"(hs_r), "=&v"(hs_q) : "v"(hs.base + hs.mine), "v"(hs.base + hs.peer) : "memory");
  bf16x8 of0[4];
  {
    WAIT_BAR(10);
    STAMP(2);
    const unsigned av = trV + (1u + g) * SL;
#pragma unroll
    for (int ks = 0; ks < 4; ++ks) { vf[4 * ks + 2] = vfrag(av, ks, 2); vf[4 * ks + 3] = vfrag(av, ks, 3); }
    SBAR();
#pragma unroll
    for (int ks = 0; ks < 4; ++ks) { vf[4 * ks] = vfrag(av, ks, 0); vf[4 * ks + 1] = vfrag(av, ks, 1); }
    SBAR();
#pragma unroll
    for (int ks = 0; ks < 4; ++ks) { o[2] = MFMA(vf[4 * ks + 2], pf[ks], o[2]); o[3] = MFMA(vf[4 * ks + 3], pf[ks], o[3]); }
    SBAR();
#pragma unroll
    for (int ks = 0; ks < 2; ++ks) { o[0] = MFMA(vf[4 * ks], pf[ks], o[0]); o[1] = MFMA(vf[4 * ks + 1], pf[ks], o[1]); }
    SBAR();
    const unsigned xo = x0 + g * SL, xi = x0 + (1u - g) * SL;
#pragma unroll
    for (int q = 0; q < 4; ++q) { lds_st4(xo + q * 1024, o[2], q); lds_st4(xo + 4096 + q * 1024, o[3], q); }
    SBAR();
#pragma unroll
    for (int ks = 2; ks < 4; ++ks) { o[0] = MFMA(vf[4 * ks], pf[ks], o[0]); o[1] = MFMA(vf[4 * ks + 1], pf[ks], o[1]); }
    SBAR();
    if (g == 0) BAR();
    const f32x2 st = *(const LAS f32x2*)(stats + (wid ^ 4u) * 256u);
    const float ms = fmaxf(m_run, st[0]);
    const float e_me = __builtin_amdgcn_exp2f(m_run - ms), e_ot = __builtin_amdgcn_exp2f(st[0] - ms);
    const float inv = 1.f / (l_run * e_me + st[1] * e_ot);
    float a_me = e_me * inv, a_ot = e_ot * inv;
    PIN(a_me); PIN(a_ot);
    asm volatile("s_waitcnt vmcnt(0) lgkmcnt(0)\n\ts_barrier" : "+v"(hs_r), "+v"(hs_q) :: "memory");
    f32x4 w0[4], w1[4];
#pragma unroll
    for (int q = 0; q < 4; ++q) { w0[q] = lds_f4(xi + q * 1024); w1[q] = lds_f4(xi + 4096 + q * 1024); }
#pragma unroll
    for (int q = 0; q < 4; ++q)
#pragma unroll
      for (int e = 0; e < 4; ++e) { o[0][4 * q + e] = fmaf(w0[q][e], a_ot, o[0][4 * q + e] * a_me); o[1][4 * q + e] = fmaf(w1[q][e], a_ot, o[1][4 * q + e] * a_me); }
    of0[0] = pack8(o[0], 0); of0[1] = pack8(o[0], 1); of0[2] = pack8(o[1], 0); of0[3] = pack8(o[1], 1);
  }
  PRIO(0);
  const __amdgpu_buffer_rsrc_t srdO = __builtin_amdgcn_make_buffer_rsrc((void*)(ws + WS_O + (size_t)(hg * 8u + b) * 131072u), (short)0, 4 * 32768, 0x00020000);
#pragma unroll
  for (int s = 0; s < 4; ++s) __builtin_amdgcn_raw_buffer_store_b128(__builtin_bit_cast(u32x4, of0[s]), srdO, j * 32768u + wid * 4096u + (unsigned)s * 1024u + ln * 16u, 0, 16);
  STAMP(3);
  f32x16 c[4] = {};
  const unsigned trW2 = trV - U + (ln >> 5) * 1024u + g * U;
  auto wfrag = [&](unsigned slot, int s, int nb) { const unsigned a = ((trW2 + slot * SL) ^ (unsigned)(nb << 6)) + s * 4096; return lds_tr2(a, a + 1024); };
  bf16x8 w1[16], w2[16];
#pragma unroll
  for (int i = 0; i < 16; ++i) w2[i] = wfrag(3, i >> 2, i & 3);
  SBAR();
#pragma unroll
  for (int i = 0; i < 16; ++i) { c[i & 3] = MFMA(w2[i], of0[i >> 2], c[i & 3]); w1[i] = wfrag(4, i >> 2, i & 3); SBAR(); }
  PIN(c[0]); PIN(c[1]); PIN(c[2]); PIN(c[3]);
  WAIT_BAR(0);
  STAMP(4);
  if (wid == 0) {
    if (!__all(hs_r == hs_q || !hs.act)) hs_wait_armed(hs);
    if (ln < 4u) __hip_atomic_store(hs.base + hs.peer, 1u, RLX_AGENT);
  }
  issueWo(2u + g, 1u - g);
  if (wid == 0) hs_wait_flags(hs, ln < 4u);
  WAIT_BAR(0);
  STAMP(5);
  const unsigned xch = lds0 + 4u * SL;
  const __amdgpu_buffer_rsrc_t srdP = __builtin_amdgcn_make_buffer_rsrc((void*)(ws + WS_P + (size_t)(b * 4u + j) * 262144u), (short)0, 262144, 0x00020000);
  {
    bf16x8 of[12];
#pragma unroll
    for (int i = 0; i < 12; ++i)
      of[i] = __builtin_bit_cast(bf16x8, __builtin_amdgcn_raw_buffer_load_b128(srdO, wid * 4096u + (unsigned)(i & 3) * 1024u + ln * 16u, ((j + 1u + (unsigned)(i >> 2)) & 3u) * 32768u, 16));
    SBAR();
#pragma unroll
    for (int i = 0; i < 8; ++i) w2[i] = wfrag(1, i >> 2, i & 3);
    SBAR();
#pragma unroll
    for (int i = 0; i < 16; ++i) { c[i & 3] = MFMA(w1[i], of[i >> 2], c[i & 3]); if (i >= 8) w2[i] = wfrag(1, i >> 2, i & 3); SBAR(); }
#pragma unroll
    for (int i = 0; i < 16; ++i) { c[i & 3] = MFMA(w2[i], of[4 + (i >> 2)], c[i & 3]); w1[i] = wfrag(0, (i >> 1) & 3, (i & 1) + (i < 8 ? 2 : 0)); SBAR(); }
#pragma unroll
    for (int i = 0; i < 8; ++i) c[2 + (i & 1)] = MFMA(w1[i], of[8 + (i >> 1)], c[2 + (i & 1)]);
    SBAR();
#pragma unroll
    for (int i = 0; i < 4; ++i) c[0] = MFMA(w1[8 + 2 * i], of[8 + i], c[0]);
    SBAR();
#pragma unroll
    for (int q = 0; q < 4; ++q) { lds_st4(x0 + (2u + g) * SL + q * 1024, c[2], q); lds_st4(x0 + (2u + g) * SL + 4096 + q * 1024, c[3], q); }
    SBAR();
#pragma unroll
    for (int i = 0; i < 4; ++i) c[1] = MFMA(w1[9 + 2 * i], of[8 + i], c[1]);
    PIN(c[0]); PIN(c[1]);
    BAR();
    STAMP(6);
    const unsigned ra = x0 + (3u - g) * SL;
    f32x4 va[8];
#pragma unroll
    for (int q = 0; q < 8; ++q) va[q] = lds_f4(ra + q * 1024);
    SBAR();
    auto put = [&](int nb) {
      float mx = 0.f;
#pragma unroll
      for (int r = 0; r < 16; ++r) { c[nb][r] += va[4 * nb + (r >> 2)][r & 3]; mx = fmaxf(mx, fabsf(c[nb][r])); }
      const unsigned e = max(wave_umax(__float_as_uint(mx)) >> 23, 15u);
      const float sc = __uint_as_float((268u - e) << 23);
#pragma unroll
      for (int s = 0; s < 2; ++s) {
        u32x4 w;
#pragma unroll
        for (int q = 0; q < 4; ++q) { f32x2 v = {c[nb][8 * s + 2 * q] * sc, c[nb][8 * s + 2 * q + 1] * sc}; w[q] = __builtin_bit_cast(unsigned, __builtin_convertvector(v, f16x2)); }
        __builtin_amdgcn_raw_buffer_store_b128(w, srdP, hg * 32768u + wid * 4096u + (unsigned)(2 * nb + s) * 1024u + ln * 16u, 0, 16);
      }
      if (ln == 0) *(LAS unsigned char*)(xch + wid * 4u + (unsigned)nb) = (unsigned char)e;
    };
    put(0);
    put(1);
  }
  STAMP(7);
  WAIT_BAR(0);
  STAMP(8);
  if (wid == 0) {
    const bool sel = hs.act && ln >= 8u;
    if (sel) __hip_atomic_store(hs.base + hs.peer, (*(const LAS unsigned*)(xch + (ln & 7u) * 4u) & 0xFFFFu) | 0x10000u, RLX_AGENT);
    const unsigned fv = hs_wait_flags(hs, sel);
    if (sel) *(LAS unsigned*)(xch + 64u + (ln & 7u) * 4u) = fv;
  }
  BAR();
  STAMP(9);
  if (g == 0) {
    u32x4 pv[8];
#pragma unroll
    for (int u = 0; u < 8; ++u) pv[u] = __builtin_amdgcn_raw_buffer_load_b128(srdP, hg * 4096u + w4 * 1024u + ln * 16u, (unsigned)u * 32768u, PLAUX);
    const u32x4 f0 = *(const LAS u32x4*)(xch + 64u), f1 = *(const LAS u32x4*)(xch + 80u);
    float acc[8] = {};
#pragma unroll
    for (int u = 0; u < 8; ++u) {
      const unsigned e = ((u < 4 ? f0[u & 3] : f1[u & 3]) >> (8u * (w4 >> 1))) & 0xFFu;
      const float isc = __uint_as_float((e - 14u) << 23);
#pragma unroll
      for (int q = 0; q < 4; ++q) { const unsigned wv = pv[u][q]; const f16x2 hv = __builtin_bit_cast(f16x2, wv); acc[2 * q] = fmaf((float)hv[0], isc, acc[2 * q]); acc[2 * q + 1] = fmaf((float)hv[1], isc, acc[2 * q + 1]); }
    }
    const unsigned qrow = (hg & 3u) * 32u + (ln & 31u), n = j * 128u + ((hg >> 2) * 2u + (w4 >> 1)) * 32u + (w4 & 1u) * 16u + (ln >> 5) * 8u;
    *(u32x4*)(out + ((size_t)b * SQ + qrow) * DM + n) = (u32x4){cvtpk(acc[0], acc[1]), cvtpk(acc[2], acc[3]), cvtpk(acc[4], acc[5]), cvtpk(acc[6], acc[7])};
  }
  STAMP(10);
}

extern "C" void kernel_launch(void* const* d_in, const int* in_sizes, int n_in, void* d_out, int out_size, void* d_ws, size_t ws_size, hipStream_t stream) {
  (void)in_sizes; (void)n_in; (void)out_size; (void)ws_size;
  (void)hipFuncSetAttribute((const void*)mha_fused, hipFuncAttributeMaxDynamicSharedMemorySize, (int)LDS_BYTES);
  hipLaunchKernelGGL(mha_fused, dim3(256), dim3(512), LDS_BYTES, stream, (const unsigned short*)d_in[0], (const unsigned short*)d_in[1], (const unsigned short*)d_in[2],
                     (const unsigned short*)d_in[3], (const unsigned short*)d_in[4], (unsigned short*)d_out, (char*)d_ws);
}
```

```cpp
#define VDEFER
#define PACE 110u
#include <hip/hip_runtime.h>
#include <hip/hip_bf16.h>
#include <stdint.h>
#include <type_traits>

typedef __bf16 bf16x8 __attribute__((ext_vector_type(8)));
typedef __bf16 bf16x2 __attribute__((ext_vector_type(2)));
typedef _Float16 f16x2 __attribute__((ext_vector_type(2)));
typedef short s16x4 __attribute__((ext_vector_type(4)));
typedef float f32x16 __attribute__((ext_vector_type(16)));
typedef float f32x4 __attribute__((ext_vector_type(4)));
typedef float f32x2 __attribute__((ext_vector_type(2)));
typedef unsigned u32x4 __attribute__((ext_vector_type(4)));
typedef unsigned u32x2 __attribute__((ext_vector_type(2)));
#define LAS __attribute__((address_space(3)))
#define GAS __attribute__((address_space(1)))
#define DI __device__ __forceinline__
#define MFMA(a, b, c) __builtin_amdgcn_mfma_f32_32x32x16_bf16((a), (b), (c), 0, 0, 0)
#define RLX_AGENT __ATOMIC_RELAXED, __HIP_MEMORY_SCOPE_AGENT
#define PIN(x) asm volatile("" : "+v"(x))
#define SBAR() __builtin_amdgcn_sched_barrier(0)
#define PRIO(n) __builtin_amdgcn_s_setprio(n)

constexpr int SQ = 128, DM = 512, NH = 32, SK = 2048, DH = 128;
constexpr unsigned U = 16384u, SL = 32768u;
constexpr unsigned LDS_BYTES = 5 * SL;
#define CODEPF 17
#define PACE0 430u
#define PSTAG (((bid >> 3) * PACE) >> 5)
#define MNOP() do { } while (0)
#define PLAUX 18
#define KPOS 2
#define PVPRIO 0
#define PSLIM 8192.0f
constexpr float CSC = 0.088388347648318447f * 1.4426950408889634f;
constexpr size_t WS_FLAG = 0;
constexpr unsigned FB = 128;
constexpr size_t WS_O = 1 << 20;
constexpr size_t WS_P = WS_O + (8u << 20);
constexpr size_t WS_END = WS_P + (16u << 20);

DI unsigned rfl(unsigned x) { return __builtin_amdgcn_readfirstlane(x); }
DI unsigned cvtpk(float lo, float hi) { f32x2 v = {lo, hi}; bf16x2 b = __builtin_convertvector(v, bf16x2); return __builtin_bit_cast(unsigned, b); }
DI bf16x8 pack8(const f32x16& a, int s) {
  u32x4 w = {cvtpk(a[8 * s], a[8 * s + 1]), cvtpk(a[8 * s + 2], a[8 * s + 3]), cvtpk(a[8 * s + 4], a[8 * s + 5]), cvtpk(a[8 * s + 6], a[8 * s + 7])};
  return __builtin_bit_cast(bf16x8, w);
}
DI bf16x8 lds_b128(unsigned a) { return *(const LAS bf16x8*)a; }
DI f32x4 lds_f4(unsigned a) { return *(const LAS f32x4*)a; }
DI void lds_st4(unsigned a, const f32x16& v, int q) { *(LAS f32x4*)a = (f32x4){v[4 * q], v[4 * q + 1], v[4 * q + 2], v[4 * q + 3]}; }
DI s16x4 lds_tr(unsigned a) { return __builtin_amdgcn_ds_read_tr16_b64_v4i16((LAS s16x4*)a); }
DI bf16x8 lds_tr2(unsigned a0, unsigned a1) {
  s16x4 lo = lds_tr(a0), hi = lds_tr(a1);
  return __builtin_bit_cast(bf16x8, __builtin_shufflevector(lo, hi, 0, 1, 2, 3, 4, 5, 6, 7));
}
DI void dma4_kv(unsigned m0v, unsigned voff, __amdgpu_buffer_rsrc_t srd, unsigned soff) {
  unsigned keep;
  asm volatile("s_mov_b32 %0, m0\n\ts_mov_b32 m0, %1\n\ts_nop 3\n\t"
               "buffer_load_dwordx4 %2, %3, %4 offen nt lds\n\t"
               "buffer_load_dwordx4 %2, %3, %4 offen offset:1024 nt lds\n\t"
               "buffer_load_dwordx4 %2, %3, %4 offen offset:2048 nt lds\n\t"
               "buffer_load_dwordx4 %2, %3, %4 offen offset:3072 nt lds\n\t"
               "s_mov_b32 m0, %0"
               : "=&s"(keep) : "s"(rfl(m0v)), "v"(voff), "s"(srd), "s"(rfl(soff)) : "memory");
}
DI void dma4_def(unsigned m0v, unsigned voff, __amdgpu_buffer_rsrc_t srd, unsigned soff) {
  unsigned keep;
  asm volatile("s_mov_b32 %0, m0\n\ts_mov_b32 m0, %1\n\ts_nop 3\n\t"
               "buffer_load_dwordx4 %2, %3, %4 offen lds\n\t"
               "buffer_load_dwordx4 %2, %3, %4 offen offset:1024 lds\n\t"
               "buffer_load_dwordx4 %2, %3, %4 offen offset:2048 lds\n\t"
               "buffer_load_dwordx4 %2, %3, %4 offen offset:3072 lds\n\t"
               "s_mov_b32 m0, %0"
               : "=&s"(keep) : "s"(rfl(m0v)), "v"(voff), "s"(srd), "s"(rfl(soff)) : "memory");
}
#define DMA_K dma4_kv
#define DMA_V dma4_kv
DI void dma1(unsigned m0v, unsigned voff, __amdgpu_buffer_rsrc_t srd, unsigned soff) {
  unsigned keep;
  asm volatile("s_mov_b32 %0, m0\n\ts_mov_b32 m0, %1\n\ts_nop 3\n\t"
               "buffer_load_dwordx4 %2, %3, %4 offen lds\n\t"
               "s_mov_b32 m0, %0"
               : "=&s"(keep) : "s"(rfl(m0v)), "v"(voff), "s"(srd), "s"(rfl(soff)) : "memory");
}
DI void dma1nt(unsigned m0v, unsigned voff, __amdgpu_buffer_rsrc_t srd, unsigned soff) {
  unsigned keep;
  asm volatile("s_mov_b32 %0, m0\n\ts_mov_b32 m0, %1\n\ts_nop 3\n\t"
               "buffer_load_dwordx4 %2, %3, %4 offen nt lds\n\t"
               "s_mov_b32 m0, %0"
               : "=&s"(keep) : "s"(rfl(m0v)), "v"(voff), "s"(srd), "s"(rfl(soff)) : "memory");
}
#define DMAWQ dma1nt
#define DMAX dma1
#define WAIT_BAR(N) asm volatile("s_waitcnt vmcnt(" #N ") lgkmcnt(0)\n\ts_barrier" ::: "memory")
#define BAR() asm volatile("s_waitcnt lgkmcnt(0)\n\ts_barrier" ::: "memory")
#define WAITVM(N) asm volatile("s_waitcnt vmcnt(" #N ")" ::: "memory")

DI float xhalf_max(float v) { auto r = __builtin_amdgcn_permlane32_swap(__float_as_uint(v), __float_as_uint(v), false, false); return fmaxf(__uint_as_float(r[0]), __uint_as_float(r[1])); }
DI float xhalf_sum(float v) { auto r = __builtin_amdgcn_permlane32_swap(__float_as_uint(v), __float_as_uint(v), false, false); return __uint_as_float(r[0]) + __uint_as_float(r[1]); }

DI unsigned wave_umax(unsigned v) {
#define DPPMAX(ctrl, rmask) v = max(v, (unsigned)__builtin_amdgcn_update_dpp(0, (int)v, ctrl, rmask, 0xF, false))
  DPPMAX(0x111, 0xF); DPPMAX(0x112, 0xF); DPPMAX(0x114, 0xF); DPPMAX(0x118, 0xF);
  DPPMAX(0x142, 0xA); DPPMAX(0x143, 0xC);
#undef DPPMAX
  return (unsigned)__builtin_amdgcn_readlane((int)v, 63);
}
struct Hs { GAS unsigned* base; unsigned mine, peer; bool act; };
DI Hs hs_make(char* ws, unsigned bid, unsigned L) {
  Hs h; h.base = (GAS unsigned*)(ws + WS_FLAG);
  const bool h1 = L < 4u; h.act = h1 || (L >= 8u && L < 16u);
  const unsigned pb = h1 ? ((bid & ~24u) | (L << 3)) : ((bid & ~7u) | (L & 7u));
  h.mine = bid * FB + (L & 15u); h.peer = pb * FB + (h1 ? ((bid >> 3) & 3u) : 8u + (bid & 7u));
  return h;
}
DI void hs_echo(const Hs& h) { if (h.act) __hip_atomic_store(h.base + h.peer + 64u, __hip_atomic_load(h.base + h.mine + 32u, RLX_AGENT), RLX_AGENT); }
DI void hs_wait_armed(const Hs& h) {
#pragma nounroll
  for (unsigned spins = 0; spins < (1u << 20); ++spins) {
    hs_echo(h);
    bool ok = true;
    if (h.act) ok = __hip_atomic_load(h.base + h.mine + 64u, RLX_AGENT) == __hip_atomic_load(h.base + h.peer + 32u, RLX_AGENT);
    if (__all(ok)) break;
    __builtin_amdgcn_s_sleep(4);
  }
}
DI unsigned hs_wait_flags(const Hs& h, bool sel) {
  unsigned v = 1u;
#pragma nounroll
  for (unsigned spins = 0; spins < (1u << 20); ++spins) {
    if (sel) v = __hip_atomic_load(h.base + h.mine, RLX_AGENT);
    if (__all(v != 0u)) break;
    if ((spins & 31u) == 31u) hs_echo(h);
    __builtin_amdgcn_s_sleep(1);
  }
  asm volatile("" ::: "memory");
  return v;
}

#define STAMP(i) do { } while (0)

__global__ __launch_bounds__(512, 2) void mha_fused(const unsigned short* __restrict__ x, const unsigned short* __restrict__ kc, const unsigned short* __restrict__ vc,
                                                    const unsigned short* __restrict__ wq, const unsigned short* __restrict__ wo, unsigned short* __restrict__ out, char* __restrict__ ws) {
  extern __shared__ __attribute__((aligned(16))) char lds[];
  const unsigned tid = threadIdx.x, lane = tid & 63u, r32 = lane & 31u, hi = lane >> 5;
  const unsigned wid = rfl(tid >> 6), g = wid >> 2, w4 = wid & 3u;
  const unsigned bid = blockIdx.x, hg = bid & 7u, b = bid >> 5, j = (bid >> 3) & 3u, h = hg * 4u + j;
  const unsigned lds0 = (unsigned)(uintptr_t)lds;
  STAMP(0);
  const unsigned tref = (unsigned)__builtin_amdgcn_s_memrealtime() + PACE0 + PSTAG;
  auto due = [&](unsigned t) { unsigned long long t_; asm volatile("s_memrealtime %0\n\ts_waitcnt lgkmcnt(0)" : "=s"(t_) :: "memory"); return (int)((unsigned)t_ - tref - t) >= 0; };
  auto pace = [&](unsigned t) { while (!due(t)) __builtin_amdgcn_s_sleep(2); };
  asm volatile("" :: "s"(out), "s"(ws), "s"(wo));

  const __amdgpu_buffer_rsrc_t srdK = __builtin_amdgcn_make_buffer_rsrc((void*)(kc + (size_t)(b * NH + h) * SK * DH), (short)0, SK * DH * 2, 0x00020000);
  const __amdgpu_buffer_rsrc_t srdV = __builtin_amdgcn_make_buffer_rsrc((void*)(vc + (size_t)(b * NH + h) * SK * DH), (short)0, SK * DH * 2, 0x00020000);
  const __amdgpu_buffer_rsrc_t srdX = __builtin_amdgcn_make_buffer_rsrc((void*)(x + (size_t)b * SQ * DM), (short)0, SQ * DM * 2, 0x00020000);
  const __amdgpu_buffer_rsrc_t srdWq = __builtin_amdgcn_make_buffer_rsrc((void*)wq, (short)0, DM * NH * DH * 2, 0x00020000);
  const __amdgpu_buffer_rsrc_t srdWo = __builtin_amdgcn_make_buffer_rsrc((void*)(wo + (size_t)hg * 512 * DM), (short)0, 512 * DM * 2, 0x00020000);

  const unsigned l4 = lane >> 4, l15 = lane & 15u;
  const unsigned voffK = w4 * 4096u + l4 * 256u + ((l15 ^ (l4 | (w4 << 2))) << 4);
  const unsigned voffV = w4 * 4096u + l4 * 256u + ((l15 ^ (l4 << 2)) << 4);
  const unsigned voffWq = l4 * 8192u + ((l15 ^ (l4 << 2)) << 4);
  const unsigned voffWo = l4 * 1024u + ((l15 ^ (l4 << 2)) << 4);
  const unsigned voffX = (lane >> 3) * 1024u + (((lane & 7u) ^ (((lane >> 4) & 1u) | (w4 << 1))) << 4);

  auto issueK = [&](unsigned t, unsigned off) { DMA_K(lds0 + off + w4 * 4096u, voffK, srdK, t * U); };
  auto issueV = [&](unsigned t, unsigned off) { DMA_V(lds0 + off + w4 * 4096u, voffV, srdV, t * U); };
  auto issueWo = [&](unsigned cc, unsigned slot) {
    const unsigned c = (j + cc) & 3u;
#pragma unroll
    for (int q = 0; q < 8; ++q) dma1(lds0 + slot * SL + (w4 * 8u + q) * 1024u, voffWo, srdWo, (c * 128u + w4 * 32u + q * 4u) * 1024u + j * 256u);
  };
#define ROT ((b + 2u * j) & 7u)
  const unsigned rot = ROT;
  auto qslot = [](int c) { return (c % 3 == 0) ? 3u : (c % 3 == 1) ? 4u : 2u; };
  auto issueQP = [&](int c) {
    const unsigned kcn = ((unsigned)c + rot) & 7u, sb = lds0 + qslot(c) * SL;
#pragma unroll
    for (int q = 0; q < 4; ++q) {
      DMAWQ(sb + (w4 * 4u + q) * 1024u, voffWq, srdWq, (kcn * 64u + w4 * 16u + q * 4u) * 8192u + h * 256u);
      DMAX(sb + U + (q * 4u + w4) * 1024u, voffX, srdX, (q * 4u + w4) * 8192u + kcn * 128u);
    }
  };
  const unsigned blk = (lane >> 4) & 1u, q4 = l15 >> 2, p4 = lane & 3u, pp = ((p4 & 1u) << 1) | (p4 >> 1);
  const unsigned trV = lds0 + U + (4u * hi + q4) * 256u + ((q4 ^ (2u * g)) << 6) + blk * 32u + pp * 8u;
  const unsigned rdK = lds0 + ((r32 & 7u) + 16u * (r32 >> 3)) * 256u + ((hi ^ ((r32 & 3u) | ((r32 >> 3) << 2))) << 4);
  const unsigned qx = lds0 + 3u * SL + w4 * 4096u + lane * 16u;

  bf16x8 qf[8], kf[16];
  auto rdk = [&](unsigned off, int i) { return lds_b128(((rdK + off) ^ (unsigned)((i & 7) << 5)) + (i >> 3) * 2048); };
  if (g == 0) {
    const unsigned trW = lds0 + (8u * hi + q4) * 256u + (q4 << 6) + blk * 32u + pp * 8u;
    const unsigned rdX = lds0 + U + (w4 * 32u + r32) * 128u + ((hi ^ (((r32 >> 1) & 1u) | ((r32 >> 3) << 1))) << 4);
    f32x16 qa[4] = {};
    bf16x8 xb[2][4], wa_[2][16];
    auto rdq = [&](int c, int s) {
      const unsigned u0 = qslot(c) * SL;
      xb[c & 1][s] = lds_b128((rdX + u0) ^ (unsigned)(s << 5));
#pragma unroll
      for (int d = 0; d < 4; ++d) { const unsigned a = ((trW + u0) ^ (unsigned)(d << 6)) + s * 4096; wa_[c & 1][4 * s + d] = lds_tr2(a, a + 1024); }
    };
    {
      unsigned long long pc; asm volatile("s_getpc_b64 %0" : "=s"(pc));
      const __amdgpu_buffer_rsrc_t srdC = __builtin_amdgcn_make_buffer_rsrc((void*)(pc & ~255ull), (short)0, CODEPF * 1024, 0x00020000);
      if (wid == 1) dma1(lds0, lane * 16u, srdC, ((bid >> 3) % CODEPF) * 1024u);
    }
    unsigned r0;
    { const Hs hs = hs_make(ws, bid, lane);
      if (wid == 0 && hs.act) __hip_atomic_store(hs.base + hs.mine, 0u, RLX_AGENT);
      asm volatile("global_load_dword %0, %1, off offset:256 sc1" : "=v"(r0) : "v"(hs.base + hs.mine) : "memory"); }
    BAR();
#pragma unroll
    for (int s = 0; s < 4; ++s) rdq(0, s);
#pragma unroll
    for (int c = 0; c < 8; ++c) {
      if (c == 2) {
        asm volatile("s_waitcnt vmcnt(0) lgkmcnt(0)\n\ts_barrier" : "+v"(r0) :: "memory");
        const Hs hs = hs_make(ws, bid, lane);
        if (wid == 0 && hs.act) __hip_atomic_store(hs.base + hs.peer + 32u, r0 + 1u, RLX_AGENT);
      } else if (c < 7) BAR();
#pragma unroll
      for (int s = 0; s < 4; ++s)
#pragma unroll
        for (int d = 0; d < 4; ++d) {
          SBAR();
          qa[d] = MFMA(wa_[c & 1][4 * s + d], xb[c & 1][s], qa[d]);
          SBAR();
          if (c < 7) {
            const unsigned u0 = qslot(c + 1) * SL, a = ((trW + u0) ^ (unsigned)(d << 6)) + s * 4096;
            if (d == 0) xb[(c + 1) & 1][s] = lds_b128((rdX + u0) ^ (unsigned)(s << 5));
            wa_[(c + 1) & 1][4 * s + d] = lds_tr2(a, a + 1024);
          }
        }
      SBAR();
    }
#pragma unroll
    for (int d = 0; d < 4; ++d) {
#pragma unroll
      for (int r = 0; r < 16; ++r) qa[d][r] *= CSC;
      qf[2 * d] = pack8(qa[d], 0); qf[2 * d + 1] = pack8(qa[d], 1);
    }
#pragma unroll
    for (int f = 0; f < 8; ++f) *(LAS bf16x8*)(qx + (f >> 2) * U + (f & 3) * 1024) = qf[f];
    BAR();
#pragma unroll
    for (int i = 0; i < 16; ++i) kf[i] = rdk(U, i);
  } else {
    issueQP(0); issueQP(1);
    WAIT_BAR(8); issueQP(2);
#pragma unroll
    for (int c = 0; c < 5; ++c) { WAIT_BAR(8); issueQP(c + 3); }
    issueK(0, U); issueK(1, 0);
    WAIT_BAR(16);
    WAIT_BAR(8);
    const Hs hs = hs_make(ws, bid, lane);
    unsigned rq;
    asm volatile("global_load_dword %0, %1, off sc1" : "=v"(rq) : "v"(hs.base + hs.mine + 32u) : "memory");
    issueK(2, SL); issueV(0, SL + U);
    asm volatile("s_waitcnt vmcnt(8) lgkmcnt(0)\n\ts_barrier" : "+v"(rq) :: "memory");
    if (wid == 4 && hs.act) __hip_atomic_store(hs.base + hs.peer + 64u, rq, RLX_AGENT);
#pragma unroll
    for (int f = 0; f < 8; ++f) qf[f] = lds_b128(qx + (f >> 2) * U + (f & 3) * 1024);
#pragma unroll
    for (int i = 0; i < 16; ++i) kf[i] = rdk(0, i);
    issueK(3, 2u * SL); issueV(1, 2u * SL + U);
    pace(3u * PACE - PACE / 2u);
    issueK(4, 3u * SL);
    pace(3u * PACE);
    issueV(2, 3u * SL + U);
    WAIT_BAR(16);
  }
  STAMP(1);

  f32x16 o[4] = {};
  float m_run = 0.f, l_run = 0.f;
  f32x16 negm = {};
  bf16x8 pf[4], vf[16];
  auto vfrag = [&](unsigned av, int ks, int d) { const unsigned a = (av ^ (unsigned)(d << 6)) + (32 * (ks & 1) + 8 * (ks >> 1)) * 256; return lds_tr2(a, a + 4096); };
  bool vpend = false; unsigned vdst = 0u, vsrc = 0u;
  auto scores_slow = [&](bool first, float& ps) {
    f32x16 s0 = {}, s1 = {};
#pragma unroll
    for (int s = 0; s < 8; ++s) { s0 = MFMA(kf[s], qf[s], s0); s1 = MFMA(kf[8 + s], qf[s], s1); }
    float mx = fmaxf(s0[0], s1[0]);
#pragma unroll
    for (int r = 1; r < 16; ++r) mx = fmaxf(fmaxf(mx, s0[r]), s1[r]);
    mx = xhalf_max(mx);
    const float m_new = first ? mx : fmaxf(mx, m_run);
    if (!first) {
      const float alpha = __builtin_amdgcn_exp2f(m_run - m_new);
      l_run *= alpha;
#pragma unroll
      for (int d = 0; d < 4; ++d)
#pragma unroll
        for (int r = 0; r < 16; ++r) o[d][r] *= alpha;
    }
    m_run = m_new;
    ps = 0.f;
#pragma unroll
    for (int r = 0; r < 16; ++r) { negm[r] = -m_new; s0[r] = __builtin_amdgcn_exp2f(s0[r] - m_new); s1[r] = __builtin_amdgcn_exp2f(s1[r] - m_new); ps += s0[r] + s1[r]; }
    pf[0] = pack8(s0, 0); pf[1] = pack8(s0, 1); pf[2] = pack8(s1, 0); pf[3] = pack8(s1, 1);
  };
  auto scores = [&](bool first, auto inloop) {
    float ps = 0.f;
    PRIO(2);
    if (!first) {
      SBAR();
      f32x16 s0 = MFMA(kf[0], qf[0], negm);
      SBAR();
#pragma unroll
      for (int s = 1; s < 8; ++s) {
        s0 = MFMA(kf[s], qf[s], s0); SBAR();
        if (decltype(inloop)::value && (s & 1)) { PIN(s0); if (vpend) dma1nt(vdst + (s >> 1) * 1024u, voffV, srdV, vsrc + (s >> 1) * 1024u); SBAR(); }
      }
      SBAR();
      f32x16 s1;
      asm volatile("v_mfma_f32_32x32x16_bf16 %0, %1, %2, %3" : "=&v"(s1) : "v"(kf[8]), "v"(qf[0]), "v"(negm));
      SBAR();
      float pa = 0.f, pb = 0.f;
      unsigned w[16];
#define EXP2(S, e) do { S[e] = __builtin_amdgcn_exp2f(S[e]); S[e + 1] = __builtin_amdgcn_exp2f(S[e + 1]); float t0_ = S[e], t1_ = S[e + 1]; PIN(t0_); PIN(t1_); S[e] = t0_; S[e + 1] = t1_; SBAR(); } while (0)
#define FIN2(S, e, wi) do { pa += S[e]; pb += S[e + 1]; w[wi] = cvtpk(S[e], S[e + 1]); PIN(pa); PIN(pb); PIN(w[wi]); SBAR(); } while (0)
#pragma unroll
      for (int s = 1; s < 8; ++s) {
        s1 = MFMA(kf[8 + s], qf[s], s1); SBAR();
        EXP2(s0, 2 * (s - 1));
        if (s > 1) FIN2(s0, 2 * (s - 2), s - 2);
      }
      EXP2(s0, 14); FIN2(s0, 12, 6);
      EXP2(s1, 0); FIN2(s0, 14, 7);
#pragma unroll
      for (int i = 1; i < 8; ++i) { EXP2(s1, 2 * i); FIN2(s1, 2 * (i - 1), 7 + i); }
      FIN2(s1, 14, 15);
#undef EXP2
#undef FIN2
      ps = pa + pb;
#pragma unroll
      for (int i = 0; i < 4; ++i) pf[i] = __builtin_bit_cast(bf16x8, (u32x4){w[4 * i], w[4 * i + 1], w[4 * i + 2], w[4 * i + 3]});
    }
    if (__builtin_expect(first || __any(!(ps <= PSLIM)), 0)) scores_slow(first, ps);
    l_run += ps;
    PIN(pf[0]); PIN(pf[1]); PIN(pf[2]); PIN(pf[3]); PIN(l_run); if (decltype(inloop)::value) PIN(negm);
    PRIO(PVPRIO);
  };

  __builtin_amdgcn_s_waitcnt(0xC07F);
  unsigned sp = 1u + g;
  const unsigned p0 = 3u + ((((bid >> 3) * 11u) & 31u) * 3u >> 2);
  for (int it = 0; it < 15; ++it) {
    const unsigned p = 2u * (unsigned)it + g + 1u;
    scores(it == 0, std::true_type{});
    unsigned long long ts;
    asm volatile("s_waitcnt vmcnt(0) lgkmcnt(0)\n\ts_barrier\n\ts_memrealtime %0" : "=s"(ts) :: "memory");
    const unsigned sn = sp >= 3u ? sp - 3u : sp + 2u;
    const bool go = p != p0;
    const unsigned ni = p + (p < p0 ? 3u : 2u), ns = (p < p0 ? (sn == 4u ? 0u : sn + 1u) : sn) * SL;
    const unsigned tni = ni <= 30u ? ni * PACE : 30u * PACE + (ni - 30u) * (PACE / 2u);
    bool kgo = go && ni <= 30u;
    const unsigned av = trV + sp * SL;
#pragma unroll
    for (int i = 0; i < 16; ++i) vf[i] = vfrag(av, i >> 2, i & 3);
    SBAR();
    SBAR();
#pragma unroll
    for (int i = 0; i < 8; ++i) kf[i] = rdk(sp * SL, i);
    SBAR();
#pragma unroll
    for (int i = 0; i < KPOS; ++i) { o[i & 3] = MFMA(vf[i], pf[i >> 2], o[i & 3]); SBAR(); }
    asm volatile("s_waitcnt lgkmcnt(0)" : "+s"(ts) :: "memory");
    const int late = (int)((unsigned)ts - tref - tni);
    if (kgo && late + (int)(PACE / 2u) + 15 >= 0) { issueK(ni + 1u, ns); kgo = false; }
    SBAR();
#pragma unroll
    for (int i = KPOS; i < 8; ++i) { o[i & 3] = MFMA(vf[i], pf[i >> 2], o[i & 3]); SBAR(); }
#pragma unroll
    for (int i = 8; i < 16; ++i) { o[i & 3] = MFMA(vf[i], pf[i >> 2], o[i & 3]); if (i < 12) { kf[2 * i - 8] = rdk(sp * SL, 2 * i - 8); kf[2 * i - 7] = rdk(sp * SL, 2 * i - 7); } SBAR(); }
    if (kgo) { pace(tni - PACE / 2u); issueK(ni + 1u, ns); }
    if (go && late + 35 < 0) pace(tni);
    vpend = go; vdst = lds0 + ns + U + w4 * 4096u; vsrc = (ni - 1u) * U;
    if (it == 14) { issueV(ni - 1u, ns + U); issueWo(g, 3u + g); }
    sp = sn;
    BAR();
  }
  scores(false, std::false_type{});
  unsigned ln; asm volatile("v_mbcnt_lo_u32_b32 %0, -1, 0\n\tv_mbcnt_hi_u32_b32 %0, -1, %0" : "=v"(ln));
  const unsigned x0 = lds0 + w4 * 8192u + ln * 16u;
  l_run = xhalf_sum(l_run);
  const unsigned stats = lds0 + 2u * SL + (ln & 31u) * 8u;
  if (ln < 32u) *(LAS f32x2*)(stats + wid * 256u) = (f32x2){m_run, l_run};
  const Hs hs = hs_make(ws, bid, ln);
  unsigned hs_r, hs_q;
  asm volatile("global_load_dword %0, %2, off offset:256 sc1\n\tglobal_load_dword %1, %3, off offset:128 sc1" : "=&v"(hs_r), "=&v"(hs_q) : "v"(hs.base + hs.mine), "v"(hs.base + hs.peer) : "memory");
  bf16x8 of0[4];
  {
    WAIT_BAR(10);
    STAMP(2);
    const unsigned av = trV + (1u + g) * SL;
#pragma unroll
    for (int ks = 0; ks < 4; ++ks) { vf[4 * ks + 2] = vfrag(av, ks, 2); vf[4 * ks + 3] = vfrag(av, ks, 3); }
    SBAR();
#pragma unroll
    for (int ks = 0; ks < 4; ++ks) { vf[4 * ks] = vfrag(av, ks, 0); vf[4 * ks + 1] = vfrag(av, ks, 1); }
    SBAR();
#pragma unroll
    for (int ks = 0; ks < 4; ++ks) { o[2] = MFMA(vf[4 * ks + 2], pf[ks], o[2]); o[3] = MFMA(vf[4 * ks + 3], pf[ks], o[3]); }
    SBAR();
#pragma unroll
    for (int ks = 0; ks < 2; ++ks) { o[0] = MFMA(vf[4 * ks], pf[ks], o[0]); o[1] = MFMA(vf[4 * ks + 1], pf[ks], o[1]); }
    SBAR();
    const unsigned xo = x0 + g * SL, xi = x0 + (1u - g) * SL;
#pragma unroll
    for (int q = 0; q < 4; ++q) { lds_st4(xo + q * 1024, o[2], q); lds_st4(xo + 4096 + q * 1024, o[3], q); }
    SBAR();
#pragma unroll
    for (int ks = 2; ks < 4; ++ks) { o[0] = MFMA(vf[4 * ks], pf[ks], o[0]); o[1] = MFMA(vf[4 * ks + 1], pf[ks], o[1]); }
    SBAR();
    if (g == 0) BAR();
    const f32x2 st = *(const LAS f32x2*)(stats + (wid ^ 4u) * 256u);
    const float ms = fmaxf(m_run, st[0]);
    const float e_me = __builtin_amdgcn_exp2f(m_run - ms), e_ot = __builtin_amdgcn_exp2f(st[0] - ms);
    const float inv = 1.f / (l_run * e_me + st[1] * e_ot);
    float a_me = e_me * inv, a_ot = e_ot * inv;
    PIN(a_me); PIN(a_ot);
    asm volatile("s_waitcnt vmcnt(0) lgkmcnt(0)\n\ts_barrier" : "+v"(hs_r), "+v"(hs_q) :: "memory");
    f32x4 w0[4], w1[4];
#pragma unroll
    for (int q = 0; q < 4; ++q) { w0[q] = lds_f4(xi + q * 1024); w1[q] = lds_f4(xi + 4096 + q * 1024); }
#pragma unroll
    for (int q = 0; q < 4; ++q)
#pragma unroll
      for (int e = 0; e < 4; ++e) { o[0][4 * q + e] = fmaf(w0[q][e], a_ot, o[0][4 * q + e] * a_me); o[1][4 * q + e] = fmaf(w1[q][e], a_ot, o[1][4 * q + e] * a_me); }
    of0[0] = pack8(o[0], 0); of0[1] = pack8(o[0], 1); of0[2] = pack8(o[1], 0); of0[3] = pack8(o[1], 1);
  }
  PRIO(0);
  const __amdgpu_buffer_rsrc_t srdO = __builtin_amdgcn_make_buffer_rsrc((void*)(ws + WS_O + (size_t)(hg * 8u + b) * 131072u), (short)0, 4 * 32768, 0x00020000);
#pragma unroll
  for (int s = 0; s < 4; ++s) __builtin_amdgcn_raw_buffer_store_b128(__builtin_bit_cast(u32x4, of0[s]), srdO, j * 32768u + wid * 4096u + (unsigned)s * 1024u + ln * 16u, 0, 16);
  STAMP(3);
  f32x16 c[4] = {};
  const unsigned trW2 = trV - U + (ln >> 5) * 1024u + g * U;
  auto wfrag = [&](unsigned slot, int s, int nb) { const unsigned a = ((trW2 + slot * SL) ^ (unsigned)(nb << 6)) + s * 4096; return lds_tr2(a, a + 1024); };
  bf16x8 w1[16], w2[16];
#pragma unroll
  for (int i = 0; i < 16; ++i) w2[i] = wfrag(3, i >> 2, i & 3);
  SBAR();
#pragma unroll
  for (int i = 0; i < 16; ++i) { c[i & 3] = MFMA(w2[i], of0[i >> 2], c[i & 3]); w1[i] = wfrag(4, i >> 2, i & 3); SBAR(); }
  PIN(c[0]); PIN(c[1]); PIN(c[2]); PIN(c[3]);
  WAIT_BAR(0);
  STAMP(4);
  if (wid == 0) {
    if (!__all(hs_r == hs_q || !hs.act)) hs_wait_armed(hs);
    if (ln < 4u) __hip_atomic_store(hs.base + hs.peer, 1u, RLX_AGENT);
  }
  issueWo(2u + g, 1u - g);
  if (wid == 0) hs_wait_flags(hs, ln < 4u);
  WAIT_BAR(0);
  STAMP(5);
  const unsigned xch = lds0 + 4u * SL;
  const __amdgpu_buffer_rsrc_t srdP = __builtin_amdgcn_make_buffer_rsrc((void*)(ws + WS_P + (size_t)(b * 4u + j) * 262144u), (short)0, 262144, 0x00020000);
  {
    bf16x8 of[12];
#pragma unroll
    for (int i = 0; i < 12; ++i)
      of[i] = __builtin_bit_cast(bf16x8, __builtin_amdgcn_raw_buffer_load_b128(srdO, wid * 4096u + (unsigned)(i & 3) * 1024u + ln * 16u, ((j + 1u + (unsigned)(i >> 2)) & 3u) * 32768u, 16));
    SBAR();
#pragma unroll
    for (int i = 0; i < 8; ++i) w2[i] = wfrag(1, i >> 2, i & 3);
    SBAR();
#pragma unroll
    for (int i = 0; i < 16; ++i) { c[i & 3] = MFMA(w1[i], of[i >> 2], c[i & 3]); if (i >= 8) w2[i] = wfrag(1, i >> 2, i & 3); SBAR(); }
#pragma unroll
    for (int i = 0; i < 16; ++i) { c[i & 3] = MFMA(w2[i], of[4 + (i >> 2)], c[i & 3]); w1[i] = wfrag(0, (i >> 1) & 3, (i & 1) + (i < 8 ? 2 : 0)); SBAR(); }
#pragma unroll
    for (int i = 0; i < 8; ++i) c[2 + (i & 1)] = MFMA(w1[i], of[8 + (i >> 1)], c[2 + (i & 1)]);
    SBAR();
#pragma unroll
    for (int i = 0; i < 4; ++i) c[0] = MFMA(w1[8 + 2 * i], of[8 + i], c[0]);
    SBAR();
#pragma unroll
    for (int q = 0; q < 4; ++q) { lds_st4(x0 + (2u + g) * SL + q * 1024, c[2], q); lds_st4(x0 + (2u + g) * SL + 4096 + q * 1024, c[3], q); }
    SBAR();
#pragma unroll
    for (int i = 0; i < 4; ++i) c[1] = MFMA(w1[9 + 2 * i], of[8 + i], c[1]);
    PIN(c[0]); PIN(c[1]);
    BAR();
    STAMP(6);
    const unsigned ra = x0 + (3u - g) * SL;
    f32x4 va[8];
#pragma unroll
    for (int q = 0; q < 8; ++q) va[q] = lds_f4(ra + q * 1024);
    SBAR();
    auto put = [&](int nb) {
      float mx = 0.f;
#pragma unroll
      for (int r = 0; r < 16; ++r) { c[nb][r] += va[4 * nb + (r >> 2)][r & 3]; mx = fmaxf(mx, fabsf(c[nb][r])); }
      const unsigned e = max(wave_umax(__float_as_uint(mx)) >> 23, 15u);
      const float sc = __uint_as_float((268u - e) << 23);
#pragma unroll
      for (int s = 0; s < 2; ++s) {
        u32x4 w;
#pragma unroll
        for (int q = 0; q < 4; ++q) { f32x2 v = {c[nb][8 * s + 2 * q] * sc, c[nb][8 * s + 2 * q + 1] * sc}; w[q] = __builtin_bit_cast(unsigned, __builtin_convertvector(v, f16x2)); }
        __builtin_amdgcn_raw_buffer_store_b128(w, srdP, hg * 32768u + wid * 4096u + (unsigned)(2 * nb + s) * 1024u + ln * 16u, 0, 16);
      }
      if (ln == 0) *(LAS unsigned char*)(xch + wid * 4u + (unsigned)nb) = (unsigned char)e;
    };
    put(0);
    put(1);
  }
  STAMP(7);
  WAIT_BAR(0);
  STAMP(8);
  if (wid == 0) {
    const bool sel = hs.act && ln >= 8u;
    if (sel) __hip_atomic_store(hs.base + hs.peer, (*(const LAS unsigned*)(xch + (ln & 7u) * 4u) & 0xFFFFu) | 0x10000u, RLX_AGENT);
    const unsigned fv = hs_wait_flags(hs, sel);
    if (sel) *(LAS unsigned*)(xch + 64u + (ln & 7u) * 4u) = fv;
  }
  BAR();
  STAMP(9);
  if (g == 0) {
    u32x4 pv[8];
#pragma unroll
    for (int u = 0; u < 8; ++u) pv[u] = __builtin_amdgcn_raw_buffer_load_b128(srdP, hg * 4096u + w4 * 1024u + ln * 16u, (unsigned)u * 32768u, PLAUX);
    const u32x4 f0 = *(const LAS u32x4*)(xch + 64u), f1 = *(const LAS u32x4*)(xch + 80u);
    float acc[8] = {};
#pragma unroll
    for (int u = 0; u < 8; ++u) {
      const unsigned e = ((u < 4 ? f0[u & 3] : f1[u & 3]) >> (8u * (w4 >> 1))) & 0xFFu;
      const float isc = __uint_as_float((e - 14u) << 23);
#pragma unroll
      for (int q = 0; q < 4; ++q) { const unsigned wv = pv[u][q]; const f16x2 hv = __builtin_bit_cast(f16x2, wv); acc[2 * q] = fmaf((float)hv[0], isc, acc[2 * q]); acc[2 * q + 1] = fmaf((float)hv[1], isc, acc[2 * q + 1]); }
    }
    const unsigned qrow = (hg & 3u) * 32u + (ln & 31u), n = j * 128u + ((hg >> 2) * 2u + (w4 >> 1)) * 32u + (w4 & 1u) * 16u + (ln >> 5) * 8u;
    *(u32x4*)(out + ((size_t)b * SQ + qrow) * DM + n) = (u32x4){cvtpk(acc[0], acc[1]), cvtpk(acc[2], acc[3]), cvtpk(acc[4], acc[5]), cvtpk(acc[6], acc[7])};
  }
  STAMP(10);
}

extern "C" void kernel_launch(void* const* d_in, const int* in_sizes, int n_in, void* d_out, int out_size, void* d_ws, size_t ws_size, hipStream_t stream) {
  (void)in_sizes; (void)n_in; (void)out_size; (void)ws_size;
  (void)hipFuncSetAttribute((const void*)mha_fused, hipFuncAttributeMaxDynamicSharedMemorySize, (int)LDS_BYTES);
  hipLaunchKernelGGL(mha_fused, dim3(256), dim3(512), LDS_BYTES, stream, (const unsigned short*)d_in[0], (const unsigned short*)d_in[1], (const unsigned short*)d_in[2],
                     (const unsigned short*)d_in[3], (const unsigned short*)d_in[4], (unsigned short*)d_out, (char*)d_ws);
}
```

```cpp
#define TROT (bid * 13u + (bid >> 5))
#include <hip/hip_runtime.h>
#include <hip/hip_bf16.h>
#include <stdint.h>
#include <type_traits>

typedef __bf16 bf16x8 __attribute__((ext_vector_type(8)));
typedef __bf16 bf16x2 __attribute__((ext_vector_type(2)));
typedef _Float16 f16x2 __attribute__((ext_vector_type(2)));
typedef short s16x4 __attribute__((ext_vector_type(4)));
typedef float f32x16 __attribute__((ext_vector_type(16)));
typedef float f32x4 __attribute__((ext_vector_type(4)));
typedef float f32x2 __attribute__((ext_vector_type(2)));
typedef unsigned u32x4 __attribute__((ext_vector_type(4)));
typedef unsigned u32x2 __attribute__((ext_vector_type(2)));
#define LAS __attribute__((address_space(3)))
#define GAS __attribute__((address_space(1)))
#define DI __device__ __forceinline__
#define MFMA(a, b, c) __builtin_amdgcn_mfma_f32_32x32x16_bf16((a), (b), (c), 0, 0, 0)
#define RLX_AGENT __ATOMIC_RELAXED, __HIP_MEMORY_SCOPE_AGENT
#define PIN(x) asm volatile("" : "+v"(x))
#define SBAR() __builtin_amdgcn_sched_barrier(0)
#define PRIO(n) __builtin_amdgcn_s_setprio(n)

constexpr int SQ = 128, DM = 512, NH = 32, SK = 2048, DH = 128;
constexpr unsigned U = 16384u, SL = 32768u;
constexpr unsigned LDS_BYTES = 5 * SL;
#define CODEPF 17
#define PACE 114u
#define PACE0 480u
#define PSLIM 8192.0f
constexpr float CSC = 0.088388347648318447f * 1.4426950408889634f;
constexpr size_t WS_FLAG = 0;
constexpr unsigned FB = 128;
constexpr size_t WS_O = 1 << 20;
constexpr size_t WS_P = WS_O + (8u << 20);
constexpr size_t WS_END = WS_P + (16u << 20);

DI unsigned rfl(unsigned x) { return __builtin_amdgcn_readfirstlane(x); }
DI unsigned cvtpk(float lo, float hi) { f32x2 v = {lo, hi}; bf16x2 b = __builtin_convertvector(v, bf16x2); return __builtin_bit_cast(unsigned, b); }
DI bf16x8 pack8(const f32x16& a, int s) {
  u32x4 w = {cvtpk(a[8 * s], a[8 * s + 1]), cvtpk(a[8 * s + 2], a[8 * s + 3]), cvtpk(a[8 * s + 4], a[8 * s + 5]), cvtpk(a[8 * s + 6], a[8 * s + 7])};
  return __builtin_bit_cast(bf16x8, w);
}
DI bf16x8 lds_b128(unsigned a) { return *(const LAS bf16x8*)a; }
DI f32x4 lds_f4(unsigned a) { return *(const LAS f32x4*)a; }
DI void lds_st4(unsigned a, const f32x16& v, int q) { *(LAS f32x4*)a = (f32x4){v[4 * q], v[4 * q + 1], v[4 * q + 2], v[4 * q + 3]}; }
DI s16x4 lds_tr(unsigned a) { return __builtin_amdgcn_ds_read_tr16_b64_v4i16((LAS s16x4*)a); }
DI bf16x8 lds_tr2(unsigned a0, unsigned a1) {
  s16x4 lo = lds_tr(a0), hi = lds_tr(a1);
  return __builtin_bit_cast(bf16x8, __builtin_shufflevector(lo, hi, 0, 1, 2, 3, 4, 5, 6, 7));
}
DI void dma4_kv(unsigned m0v, unsigned voff, __amdgpu_buffer_rsrc_t srd, unsigned soff) {
  asm volatile("s_mov_b32 m0, %0\n\ts_nop 3\n\t"
               "buffer_load_dwordx4 %1, %2, %3 offen nt lds\n\t"
               "buffer_load_dwordx4 %1, %2, %3 offen offset:1024 nt lds\n\t"
               "buffer_load_dwordx4 %1, %2, %3 offen offset:2048 nt lds\n\t"
               "buffer_load_dwordx4 %1, %2, %3 offen offset:3072 nt lds"
               :: "s"(rfl(m0v)), "v"(voff), "s"(srd), "s"(rfl(soff)) : "memory");
}
DI void dma1(unsigned m0v, unsigned voff, __amdgpu_buffer_rsrc_t srd, unsigned soff) {
  asm volatile("s_mov_b32 m0, %0\n\ts_nop 3\n\t"
               "buffer_load_dwordx4 %1, %2, %3 offen lds"
               :: "s"(rfl(m0v)), "v"(voff), "s"(srd), "s"(rfl(soff)) : "memory");
}
DI void dma1nt(unsigned m0v, unsigned voff, __amdgpu_buffer_rsrc_t srd, unsigned soff) {
  asm volatile("s_mov_b32 m0, %0\n\ts_nop 3\n\t"
               "buffer_load_dwordx4 %1, %2, %3 offen nt lds"
               :: "s"(rfl(m0v)), "v"(voff), "s"(srd), "s"(rfl(soff)) : "memory");
}
#define WAIT_BAR(N) asm volatile("s_waitcnt vmcnt(" #N ") lgkmcnt(0)\n\ts_barrier" ::: "memory")
#define BAR() asm volatile("s_waitcnt lgkmcnt(0)\n\ts_barrier" ::: "memory")
#define WAITVM(N) asm volatile("s_waitcnt vmcnt(" #N ")" ::: "memory")

DI float xhalf_max(float v) { auto r = __builtin_amdgcn_permlane32_swap(__float_as_uint(v), __float_as_uint(v), false, false); return fmaxf(__uint_as_float(r[0]), __uint_as_float(r[1])); }
DI float xhalf_sum(float v) { auto r = __builtin_amdgcn_permlane32_swap(__float_as_uint(v), __float_as_uint(v), false, false); return __uint_as_float(r[0]) + __uint_as_float(r[1]); }

DI unsigned wave_umax(unsigned v) {
#define DPPMAX(ctrl, rmask) v = max(v, (unsigned)__builtin_amdgcn_update_dpp(0, (int)v, ctrl, rmask, 0xF, false))
  DPPMAX(0x111, 0xF); DPPMAX(0x112, 0xF); DPPMAX(0x114, 0xF); DPPMAX(0x118, 0xF);
  DPPMAX(0x142, 0xA); DPPMAX(0x143, 0xC);
#undef DPPMAX
  return (unsigned)__builtin_amdgcn_readlane((int)v, 63);
}
struct Hs { GAS unsigned* base; unsigned mine, peer; bool act; };
DI Hs hs_make(char* ws, unsigned bid, unsigned L) {
  Hs h; h.base = (GAS unsigned*)(ws + WS_FLAG);
  const bool h1 = L < 4u; h.act = h1 || (L >= 8u && L < 16u);
  const unsigned pb = h1 ? ((bid & ~24u) | (L << 3)) : ((bid & ~7u) | (L & 7u));
  h.mine = bid * FB + (L & 15u); h.peer = pb * FB + (h1 ? ((bid >> 3) & 3u) : 8u + (bid & 7u));
  return h;
}
DI void hs_echo(const Hs& h) { if (h.act) __hip_atomic_store(h.base + h.peer + 64u, __hip_atomic_load(h.base + h.mine + 32u, RLX_AGENT), RLX_AGENT); }
DI void hs_wait_armed(const Hs& h) {
#pragma nounroll
  for (unsigned spins = 0; spins < (1u << 20); ++spins) {
    hs_echo(h);
    bool ok = true;
    if (h.act) ok = __hip_atomic_load(h.base + h.mine + 64u, RLX_AGENT) == __hip_atomic_load(h.base + h.peer + 32u, RLX_AGENT);
    if (__all(ok)) break;
    __builtin_amdgcn_s_sleep(4);
  }
}
DI unsigned hs_wait_flags(const Hs& h, bool sel) {
  unsigned v = 1u;
#pragma nounroll
  for (unsigned spins = 0; spins < (1u << 20); ++spins) {
    if (sel) v = __hip_atomic_load(h.base + h.mine, RLX_AGENT);
    if (__all(v != 0u)) break;
    if ((spins & 15u) == 15u) hs_echo(h);
    __builtin_amdgcn_s_sleep(1);
  }
  asm volatile("" ::: "memory");
  return v;
}

#define STAMP(i) do { } while (0)

__global__ __launch_bounds__(512, 2) void mha_fused(const unsigned short* __restrict__ x, const unsigned short* __restrict__ kc, const unsigned short* __restrict__ vc,
                                                    const unsigned short* __restrict__ wq, const unsigned short* __restrict__ wo, unsigned short* __restrict__ out, char* __restrict__ ws) {
  extern __shared__ __attribute__((aligned(16))) char lds[];
  const unsigned tid = threadIdx.x, lane = tid & 63u, r32 = lane & 31u, hi = lane >> 5;
  const unsigned wid = rfl(tid >> 6), g = wid >> 2, w4 = wid & 3u;
  const unsigned bid = blockIdx.x, hg = bid & 7u, b = bid >> 5, j = (bid >> 3) & 3u, h = hg * 4u + j;
  const unsigned lds0 = (unsigned)(uintptr_t)lds;
  STAMP(0);
  const unsigned tref = (unsigned)__builtin_amdgcn_s_memrealtime() + PACE0 + (((bid >> 3) * PACE) >> 5);
  auto due = [&](unsigned t) { unsigned long long t_; asm volatile("s_memrealtime %0\n\ts_waitcnt lgkmcnt(0)" : "=s"(t_) :: "memory"); return (int)((unsigned)t_ - tref - t) >= 0; };
  auto pace = [&](unsigned t) { while (!due(t)) __builtin_amdgcn_s_sleep(2); };
  asm volatile("" :: "s"(out), "s"(ws), "s"(wo));

  const __amdgpu_buffer_rsrc_t srdK = __builtin_amdgcn_make_buffer_rsrc((void*)(kc + (size_t)(b * NH + h) * SK * DH), (short)0, SK * DH * 2, 0x00020000);
  const __amdgpu_buffer_rsrc_t srdV = __builtin_amdgcn_make_buffer_rsrc((void*)(vc + (size_t)(b * NH + h) * SK * DH), (short)0, SK * DH * 2, 0x00020000);
  const __amdgpu_buffer_rsrc_t srdX = __builtin_amdgcn_make_buffer_rsrc((void*)(x + (size_t)b * SQ * DM), (short)0, SQ * DM * 2, 0x00020000);
  const __amdgpu_buffer_rsrc_t srdWq = __builtin_amdgcn_make_buffer_rsrc((void*)wq, (short)0, DM * NH * DH * 2, 0x00020000);
  const __amdgpu_buffer_rsrc_t srdWo = __builtin_amdgcn_make_buffer_rsrc((void*)(wo + (size_t)hg * 512 * DM), (short)0, 512 * DM * 2, 0x00020000);

  unsigned w8k = w4 * 8192u; asm volatile("" : "+s"(w8k));
  const unsigned l4 = lane >> 4, l15 = lane & 15u;
  const unsigned voffK = w4 * 4096u + l4 * 256u + ((l15 ^ (l4 | (w4 << 2))) << 4);
  const unsigned voffV = w4 * 4096u + l4 * 256u + ((l15 ^ (l4 << 2)) << 4);
  const unsigned voffWq = l4 * 8192u + ((l15 ^ (l4 << 2)) << 4);
  const unsigned voffWo = l4 * 1024u + ((l15 ^ (l4 << 2)) << 4);
  const unsigned voffX = (lane >> 3) * 1024u + (((lane & 7u) ^ (((lane >> 4) & 1u) | (w4 << 1))) << 4);

  const unsigned trot = TROT;
  auto issueK = [&](unsigned t, unsigned off) { dma4_kv(lds0 + off + w4 * 4096u, voffK, srdK, ((t + trot) & 31u) * U); };
  auto issueV = [&](unsigned t, unsigned off) { dma4_kv(lds0 + off + w4 * 4096u, voffV, srdV, ((t + trot) & 31u) * U); };
  auto hpos = [&](unsigned cc) { const unsigned r = cc - 1u + b % 3u; return rfl(cc == 0u ? j : (j + 1u + ((0x124u >> (2u * r)) & 3u)) & 3u); };
  auto issueWo = [&](unsigned cc, unsigned slot) {
    const unsigned c = hpos(cc);
#pragma unroll
    for (int q = 0; q < 8; ++q) dma1(lds0 + slot * SL + w8k + q * 1024u, voffWo, srdWo, (c * 128u + w4 * 32u + q * 4u) * 1024u + j * 256u);
  };
  const unsigned rot = (b + 2u * j) & 7u;
  auto qslot = [](int c) { return (c % 3 == 0) ? 3u : (c % 3 == 1) ? 4u : 2u; };
  auto issueQP = [&](int c) {
    const unsigned kcn = ((unsigned)c + rot) & 7u, sb = lds0 + qslot(c) * SL;
#pragma unroll
    for (int q = 0; q < 4; ++q) {
      dma1nt(sb + (w4 * 4u + q) * 1024u, voffWq, srdWq, (kcn * 64u + w4 * 16u + q * 4u) * 8192u + h * 256u);
      dma1(sb + U + (q * 4u + w4) * 1024u, voffX, srdX, (q * 4u + w4) * 8192u + kcn * 128u);
    }
  };
  const unsigned blk = (lane >> 4) & 1u, q4 = l15 >> 2, p4 = lane & 3u, pp = ((p4 & 1u) << 1) | (p4 >> 1);
  const unsigned trV = lds0 + U + (4u * hi + q4) * 256u + ((q4 ^ (2u * g)) << 6) + blk * 32u + pp * 8u;
  const unsigned rdK = lds0 + ((r32 & 7u) + 16u * (r32 >> 3)) * 256u + ((hi ^ ((r32 & 3u) | ((r32 >> 3) << 2))) << 4);
  const unsigned qx = lds0 + 3u * SL + w4 * 4096u + lane * 16u;

  bf16x8 qf[8], kf[16];
  auto rdk = [&](unsigned off, int i) { return lds_b128(((rdK + off) ^ (unsigned)((i & 7) << 5)) + (i >> 3) * 2048); };
  if (g == 0) {
    const unsigned trW = lds0 + (8u * hi + q4) * 256u + (q4 << 6) + blk * 32u + pp * 8u;
    const unsigned rdX = lds0 + U + (w4 * 32u + r32) * 128u + ((hi ^ (((r32 >> 1) & 1u) | ((r32 >> 3) << 1))) << 4);
    f32x16 qa[4] = {};
    bf16x8 xb[2][4], wa_[2][16];
    auto rdq = [&](int c, int s) {
      const unsigned u0 = qslot(c) * SL;
      xb[c & 1][s] = lds_b128((rdX + u0) ^ (unsigned)(s << 5));
#pragma unroll
      for (int d = 0; d < 4; ++d) { const unsigned a = ((trW + u0) ^ (unsigned)(d << 6)) + s * 4096; wa_[c & 1][4 * s + d] = lds_tr2(a, a + 1024); }
    };
    {
      unsigned long long pc; asm volatile("s_getpc_b64 %0" : "=s"(pc));
      const __amdgpu_buffer_rsrc_t srdC = __builtin_amdgcn_make_buffer_rsrc((void*)(pc & ~255ull), (short)0, CODEPF * 1024, 0x00020000);
      if (wid == 1) dma1(lds0, lane * 16u, srdC, ((bid >> 3) % CODEPF) * 1024u);
    }
    unsigned r0;
    { const Hs hs = hs_make(ws, bid, lane);
      if (wid == 0 && hs.act) __hip_atomic_store(hs.base + hs.mine, 0u, RLX_AGENT);
      asm volatile("global_load_dword %0, %1, off offset:256 sc1" : "=v"(r0) : "v"(hs.base + hs.mine) : "memory"); }
    BAR();
#pragma unroll
    for (int s = 0; s < 4; ++s) rdq(0, s);
#pragma unroll
    for (int c = 0; c < 8; ++c) {
      if (c == 0) {
        asm volatile("s_waitcnt vmcnt(0) lgkmcnt(0)\n\ts_barrier" : "+v"(r0) :: "memory");
        const Hs hs = hs_make(ws, bid, lane);
        if (wid == 0 && hs.act) __hip_atomic_store(hs.base + hs.peer + 32u, r0 + 1u, RLX_AGENT);
      } else if (c < 7) BAR();
#pragma unroll
      for (int s = 0; s < 4; ++s)
#pragma unroll
        for (int d = 0; d < 4; ++d) {
          SBAR();
          qa[d] = MFMA(wa_[c & 1][4 * s + d], xb[c & 1][s], qa[d]);
          SBAR();
          if (c < 7) {
            const unsigned u0 = qslot(c + 1) * SL, a = ((trW + u0) ^ (unsigned)(d << 6)) + s * 4096;
            if (d == 0) xb[(c + 1) & 1][s] = lds_b128((rdX + u0) ^ (unsigned)(s << 5));
            wa_[(c + 1) & 1][4 * s + d] = lds_tr2(a, a + 1024);
          }
        }
      SBAR();
    }
#pragma unroll
    for (int d = 0; d < 4; ++d) {
#pragma unroll
      for (int r = 0; r < 16; ++r) qa[d][r] *= CSC;
      qf[2 * d] = pack8(qa[d], 0); qf[2 * d + 1] = pack8(qa[d], 1);
    }
#pragma unroll
    for (int f = 0; f < 8; ++f) *(LAS bf16x8*)(qx + (f >> 2) * U + (f & 3) * 1024) = qf[f];
    BAR();
#pragma unroll
    for (int i = 0; i < 16; ++i) kf[i] = rdk(U, i);
  } else {
    issueQP(0); issueQP(1);
    WAIT_BAR(8); issueQP(2);
#pragma unroll
    for (int c = 0; c < 5; ++c) { WAIT_BAR(8); issueQP(c + 3); }
    issueK(0, U);
    WAIT_BAR(12);
    issueK(1, 0);
    WAIT_BAR(8);
    pace(PACE / 2u); issueK(2, SL); pace(PACE); issueV(0, SL + U);
    WAIT_BAR(8);
    const Hs hs = hs_make(ws, bid, lane);
    unsigned rq;
    asm volatile("global_load_dword %0, %1, off offset:128 sc1" : "=v"(rq) : "v"(hs.base + hs.mine) : "memory");
#pragma unroll
    for (int f = 0; f < 8; ++f) qf[f] = lds_b128(qx + (f >> 2) * U + (f & 3) * 1024);
#pragma unroll
    for (int i = 0; i < 16; ++i) kf[i] = rdk(0, i);
    pace(PACE + PACE / 2u); issueK(3, 2u * SL); pace(2u * PACE); issueV(1, 2u * SL + U);
    pace(2u * PACE + PACE / 2u);
    issueK(4, 3u * SL);
    pace(3u * PACE);
    issueV(2, 3u * SL + U);
    asm volatile("s_waitcnt vmcnt(16) lgkmcnt(0)\n\ts_barrier" : "+v"(rq) :: "memory");
    if (wid == 4 && hs.act) __hip_atomic_store(hs.base + hs.peer + 64u, rq, RLX_AGENT);
  }
  STAMP(1);

  f32x16 o[4] = {};
  float m_run = 0.f, l_run = 0.f;
  f32x16 negm = {};
  bf16x8 pf[4], vf[16];
  auto vfrag = [&](unsigned av, int ks, int d) { const unsigned a = (av ^ (unsigned)(d << 6)) + (32 * (ks & 1) + 8 * (ks >> 1)) * 256; return lds_tr2(a, a + 4096); };
  bool vpend = false; unsigned vdst = 0u, vsrc = 0u;
  auto scores_slow = [&](bool first, float& ps) {
    f32x16 s0 = {}, s1 = {};
#pragma unroll
    for (int s = 0; s < 8; ++s) { s0 = MFMA(kf[s], qf[s], s0); s1 = MFMA(kf[8 + s], qf[s], s1); }
    float mx = fmaxf(s0[0], s1[0]);
#pragma unroll
    for (int r = 1; r < 16; ++r) mx = fmaxf(fmaxf(mx, s0[r]), s1[r]);
    mx = xhalf_max(mx);
    const float m_new = first ? mx : fmaxf(mx, m_run);
    if (!first) {
      const float alpha = __builtin_amdgcn_exp2f(m_run - m_new);
      l_run *= alpha;
#pragma unroll
      for (int d = 0; d < 4; ++d)
#pragma unroll
        for (int r = 0; r < 16; ++r) o[d][r] *= alpha;
    }
    m_run = m_new;
    ps = 0.f;
#pragma unroll
    for (int r = 0; r < 16; ++r) { negm[r] = -m_new; s0[r] = __builtin_amdgcn_exp2f(s0[r] - m_new); s1[r] = __builtin_amdgcn_exp2f(s1[r] - m_new); ps += s0[r] + s1[r]; }
    pf[0] = pack8(s0, 0); pf[1] = pack8(s0, 1); pf[2] = pack8(s1, 0); pf[3] = pack8(s1, 1);
  };
  auto scores = [&](bool first, auto inloop) {
    float ps = 0.f;
    PRIO(2);
    if (!first) {
      SBAR();
      f32x16 s0 = MFMA(kf[0], qf[0], negm);
      SBAR();
#pragma unroll
      for (int s = 1; s < 8; ++s) {
        s0 = MFMA(kf[s], qf[s], s0); SBAR();
        if (decltype(inloop)::value && (s & 1)) { PIN(s0); if (vpend) dma1nt(vdst + (s >> 1) * 1024u, voffV, srdV, vsrc + (s >> 1) * 1024u); SBAR(); }
      }
      SBAR();
      f32x16 s1;
      asm volatile("v_mfma_f32_32x32x16_bf16 %0, %1, %2, %3" : "=&v"(s1) : "v"(kf[8]), "v"(qf[0]), "v"(negm));
      SBAR();
      float pa = 0.f, pb = 0.f;
      unsigned w[16];
#define EXP2(S, e) do { S[e] = __builtin_amdgcn_exp2f(S[e]); S[e + 1] = __builtin_amdgcn_exp2f(S[e + 1]); float t0_ = S[e], t1_ = S[e + 1]; PIN(t0_); PIN(t1_); S[e] = t0_; S[e + 1] = t1_; SBAR(); } while (0)
#define FIN2(S, e, wi) do { pa += S[e]; pb += S[e + 1]; w[wi] = cvtpk(S[e], S[e + 1]); PIN(pa); PIN(pb); PIN(w[wi]); SBAR(); } while (0)
#pragma unroll
      for (int s = 1; s < 8; ++s) {
        s1 = MFMA(kf[8 + s], qf[s], s1); SBAR();
        EXP2(s0, 2 * (s - 1));
        if (s > 1) FIN2(s0, 2 * (s - 2), s - 2);
      }
      EXP2(s0, 14); FIN2(s0, 12, 6);
      EXP2(s1, 0); FIN2(s0, 14, 7);
#pragma unroll
      for (int i = 1; i < 8; ++i) { EXP2(s1, 2 * i); FIN2(s1, 2 * (i - 1), 7 + i); }
      FIN2(s1, 14, 15);
#undef EXP2
#undef FIN2
      ps = pa + pb;
#pragma unroll
      for (int i = 0; i < 4; ++i) pf[i] = __builtin_bit_cast(bf16x8, (u32x4){w[4 * i], w[4 * i + 1], w[4 * i + 2], w[4 * i + 3]});
    }
    if (__builtin_expect(first || __any(!(ps <= PSLIM)), 0)) scores_slow(first, ps);
    l_run += ps;
    PIN(pf[0]); PIN(pf[1]); PIN(pf[2]); PIN(pf[3]); PIN(l_run); if (decltype(inloop)::value) PIN(negm);
    PRIO(0);
  };

  __builtin_amdgcn_s_waitcnt(0xC07F);
  unsigned sp = 1u + g;
  const unsigned p0 = 3u + ((((bid >> 3) * 11u) & 31u) * 3u >> 2);
  for (int it = 0; it < 15; ++it) {
    const unsigned p = 2u * (unsigned)it + g + 1u;
    scores(it == 0, std::true_type{});
    unsigned long long ts;
    asm volatile("s_waitcnt vmcnt(0) lgkmcnt(0)\n\ts_barrier\n\ts_memrealtime %0" : "=s"(ts) :: "memory");
    const unsigned av = trV + sp * SL;
#pragma unroll
    for (int i = 0; i < 16; ++i) vf[i] = vfrag(av, i >> 2, i & 3);
    SBAR();
    SBAR();
#pragma unroll
    for (int i = 0; i < 8; ++i) kf[i] = rdk(sp * SL, i);
    SBAR();
    const unsigned sn = sp >= 3u ? sp - 3u : sp + 2u;
    const bool go = p != p0;
    const unsigned ni = p + (p < p0 ? 3u : 2u), ns = (p < p0 ? (sn == 4u ? 0u : sn + 1u) : sn) * SL;
    const unsigned tni = (ni + min(ni, 30u)) * (PACE / 2u);
    bool kgo = go && ni <= 30u;
    SBAR();
#pragma unroll
    for (int i = 0; i < 2; ++i) { o[i & 3] = MFMA(vf[i], pf[i >> 2], o[i & 3]); SBAR(); }
    asm volatile("s_waitcnt lgkmcnt(0)" : "+s"(ts) :: "memory");
    const int late = (int)((unsigned)ts - tref - tni);
    const bool vend = ni >= 31u;
    if (kgo && late + (int)(PACE / 2u) + 15 >= 0) { issueK(ni + 1u, ns); kgo = false; }
    if (vend) issueV(ni - 1u, ns + U);
    SBAR();
#pragma unroll
    for (int i = 2; i < 8; ++i) { o[i & 3] = MFMA(vf[i], pf[i >> 2], o[i & 3]); SBAR(); }
#pragma unroll
    for (int i = 8; i < 16; ++i) { o[i & 3] = MFMA(vf[i], pf[i >> 2], o[i & 3]); if (i < 12) { kf[2 * i - 8] = rdk(sp * SL, 2 * i - 8); kf[2 * i - 7] = rdk(sp * SL, 2 * i - 7); } SBAR(); }
    if (kgo) { pace(tni - PACE / 2u); issueK(ni + 1u, ns); }
    if (go && !vend && late + 35 < 0) pace(tni);
    vpend = go && !vend; vdst = lds0 + ns + U + w4 * 4096u; vsrc = ((ni - 1u + trot) & 31u) * U;
    if (it == 14) issueWo(g, 3u + g);
    sp = sn;
    __builtin_amdgcn_s_waitcnt(0xC07F);
    asm volatile("s_barrier" ::: "memory");
  }
  scores(false, std::false_type{});
  unsigned ln; asm volatile("v_mbcnt_lo_u32_b32 %0, -1, 0\n\tv_mbcnt_hi_u32_b32 %0, -1, %0" : "=v"(ln));
  const unsigned x0 = lds0 + w8k + ln * 16u;
  l_run = xhalf_sum(l_run);
  const unsigned stats = lds0 + 2u * SL + (ln & 31u) * 8u;
  if (ln < 32u) *(LAS f32x2*)(stats + wid * 256u) = (f32x2){m_run, l_run};
  const Hs hs = hs_make(ws, bid, ln);
  unsigned hs_r, hs_q, hs_qi, hs_ro;
  asm volatile("global_load_dword %0, %4, off offset:256 sc1\n\tglobal_load_dword %1, %5, off offset:128 sc1\n\t"
               "global_load_dword %2, %4, off offset:128 sc1\n\tglobal_load_dword %3, %5, off offset:256 sc1"
               : "=&v"(hs_r), "=&v"(hs_q), "=&v"(hs_qi), "=&v"(hs_ro) : "v"(hs.base + hs.mine), "v"(hs.base + hs.peer) : "memory");
  bf16x8 of0[4];
  {
    WAIT_BAR(12);
    STAMP(2);
    const unsigned av = trV + (1u + g) * SL;
#pragma unroll
    for (int ks = 0; ks < 4; ++ks) { vf[4 * ks + 2] = vfrag(av, ks, 2); vf[4 * ks + 3] = vfrag(av, ks, 3); }
    SBAR();
#pragma unroll
    for (int ks = 0; ks < 4; ++ks) { vf[4 * ks] = vfrag(av, ks, 0); vf[4 * ks + 1] = vfrag(av, ks, 1); }
    SBAR();
#pragma unroll
    for (int ks = 0; ks < 4; ++ks) { o[2] = MFMA(vf[4 * ks + 2], pf[ks], o[2]); o[3] = MFMA(vf[4 * ks + 3], pf[ks], o[3]); }
    SBAR();
#pragma unroll
    for (int ks = 0; ks < 2; ++ks) { o[0] = MFMA(vf[4 * ks], pf[ks], o[0]); o[1] = MFMA(vf[4 * ks + 1], pf[ks], o[1]); }
    SBAR();
    const unsigned xo = x0 + g * SL, xi = x0 + (1u - g) * SL;
#pragma unroll
    for (int q = 0; q < 4; ++q) { lds_st4(xo + q * 1024, o[2], q); lds_st4(xo + 4096 + q * 1024, o[3], q); }
    SBAR();
#pragma unroll
    for (int ks = 2; ks < 4; ++ks) { o[0] = MFMA(vf[4 * ks], pf[ks], o[0]); o[1] = MFMA(vf[4 * ks + 1], pf[ks], o[1]); }
    SBAR();
    if (g == 0) BAR();
    const f32x2 st = *(const LAS f32x2*)(stats + (wid ^ 4u) * 256u);
    const float ms = fmaxf(m_run, st[0]);
    const float e_me = __builtin_amdgcn_exp2f(m_run - ms), e_ot = __builtin_amdgcn_exp2f(st[0] - ms);
    const float inv = 1.f / (l_run * e_me + st[1] * e_ot);
    float a_me = e_me * inv, a_ot = e_ot * inv;
    PIN(a_me); PIN(a_ot);
    asm volatile("s_waitcnt vmcnt(0) lgkmcnt(0)\n\ts_barrier" : "+v"(hs_r), "+v"(hs_q), "+v"(hs_qi), "+v"(hs_ro) :: "memory");
    f32x4 w0[4], w1[4];
#pragma unroll
    for (int q = 0; q < 4; ++q) { w0[q] = lds_f4(xi + q * 1024); w1[q] = lds_f4(xi + 4096 + q * 1024); }
#pragma unroll
    for (int q = 0; q < 4; ++q)
#pragma unroll
      for (int e = 0; e < 4; ++e) { o[0][4 * q + e] = fmaf(w0[q][e], a_ot, o[0][4 * q + e] * a_me); o[1][4 * q + e] = fmaf(w1[q][e], a_ot, o[1][4 * q + e] * a_me); }
    of0[0] = pack8(o[0], 0); of0[1] = pack8(o[0], 1); of0[2] = pack8(o[1], 0); of0[3] = pack8(o[1], 1);
  }
  PRIO(0);
  const __amdgpu_buffer_rsrc_t srdO = __builtin_amdgcn_make_buffer_rsrc((void*)(ws + WS_O + (size_t)(hg * 8u + b) * 131072u), (short)0, 4 * 32768, 0x00020000);
#pragma unroll
  for (int s = 0; s < 4; ++s) __builtin_amdgcn_raw_buffer_store_b128(__builtin_bit_cast(u32x4, of0[s]), srdO, j * 32768u + wid * 4096u + (unsigned)s * 1024u + ln * 16u, 0, 16);
  STAMP(3);
  f32x16 c[4] = {};
  const unsigned trW2 = trV - U + (ln >> 5) * 1024u + g * U;
  auto wfrag = [&](unsigned slot, int s, int nb) { const unsigned a = ((trW2 + slot * SL) ^ (unsigned)(nb << 6)) + s * 4096; return lds_tr2(a, a + 1024); };
  bf16x8 w1[16], w2[16];
#pragma unroll
  for (int i = 0; i < 16; ++i) w2[i] = wfrag(3, i >> 2, i & 3);
  SBAR();
#pragma unroll
  for (int i = 0; i < 16; ++i) { c[i & 3] = MFMA(w2[i], of0[i >> 2], c[i & 3]); w1[i] = wfrag(4, i >> 2, i & 3); SBAR(); }
  PIN(c[0]); PIN(c[1]); PIN(c[2]); PIN(c[3]);
  WAIT_BAR(0);
  STAMP(4);
  if (wid == 0) {
    if (hs.act && hs_qi != hs_ro) __hip_atomic_store(hs.base + hs.peer + 64u, hs_qi, RLX_AGENT);
    if (!__all(hs_r == hs_q || !hs.act)) hs_wait_armed(hs);
    if (ln < 4u) __hip_atomic_store(hs.base + hs.peer, 1u, RLX_AGENT);
  }
  issueWo(2u + g, 1u - g);
  if (wid == 0) hs_wait_flags(hs, ln < 4u);
  WAIT_BAR(0);
  STAMP(5);
  const unsigned xch = lds0 + 4u * SL;
  const __amdgpu_buffer_rsrc_t srdP = __builtin_amdgcn_make_buffer_rsrc((void*)(ws + WS_P + (size_t)(b * 4u + j) * 262144u), (short)0, 262144, 0x00020000);
  {
    bf16x8 of[12];
#pragma unroll
    for (int i = 0; i < 12; ++i)
      of[i] = __builtin_bit_cast(bf16x8, __builtin_amdgcn_raw_buffer_load_b128(srdO, wid * 4096u + (unsigned)(i & 3) * 1024u + ln * 16u, hpos(1u + (unsigned)(i >> 2)) * 32768u, 16));
    SBAR();
#pragma unroll
    for (int i = 0; i < 8; ++i) w2[i] = wfrag(1, i >> 2, i & 3);
    SBAR();
#pragma unroll
    for (int i = 0; i < 16; ++i) { c[i & 3] = MFMA(w1[i], of[i >> 2], c[i & 3]); if (i >= 8) w2[i] = wfrag(1, i >> 2, i & 3); SBAR(); }
#pragma unroll
    for (int i = 0; i < 16; ++i) { c[i & 3] = MFMA(w2[i], of[4 + (i >> 2)], c[i & 3]); w1[i] = wfrag(0, (i >> 1) & 3, (i & 1) + (i < 8 ? 2 : 0)); SBAR(); }
#pragma unroll
    for (int i = 0; i < 8; ++i) c[2 + (i & 1)] = MFMA(w1[i], of[8 + (i >> 1)], c[2 + (i & 1)]);
    SBAR();
#pragma unroll
    for (int i = 0; i < 4; ++i) c[0] = MFMA(w1[8 + 2 * i], of[8 + i], c[0]);
    SBAR();
#pragma unroll
    for (int q = 0; q < 4; ++q) { lds_st4(x0 + (2u + g) * SL + q * 1024, c[2], q); lds_st4(x0 + (2u + g) * SL + 4096 + q * 1024, c[3], q); }
    SBAR();
#pragma unroll
    for (int i = 0; i < 4; ++i) c[1] = MFMA(w1[9 + 2 * i], of[8 + i], c[1]);
    PIN(c[0]); PIN(c[1]);
    BAR();
    STAMP(6);
    const unsigned ra = x0 + (3u - g) * SL;
    f32x4 va[8];
#pragma unroll
    for (int q = 0; q < 8; ++q) va[q] = lds_f4(ra + q * 1024);
    SBAR();
    auto put = [&](int nb) {
      float mx = 0.f;
#pragma unroll
      for (int r = 0; r < 16; ++r) { c[nb][r] += va[4 * nb + (r >> 2)][r & 3]; mx = fmaxf(mx, fabsf(c[nb][r])); }
      const unsigned e = max(wave_umax(__float_as_uint(mx)) >> 23, 15u);
      const float sc = __uint_as_float((268u - e) << 23);
#pragma unroll
      for (int s = 0; s < 2; ++s) {
        u32x4 w;
#pragma unroll
        for (int q = 0; q < 4; ++q) { f32x2 v = {c[nb][8 * s + 2 * q] * sc, c[nb][8 * s + 2 * q + 1] * sc}; w[q] = __builtin_bit_cast(unsigned, __builtin_convertvector(v, f16x2)); }
        __builtin_amdgcn_raw_buffer_store_b128(w, srdP, hg * 32768u + wid * 4096u + (unsigned)(2 * nb + s) * 1024u + ln * 16u, 0, 16);
      }
      if (ln == 0) *(LAS unsigned char*)(xch + wid * 4u + (unsigned)nb) = (unsigned char)e;
    };
    put(0);
    put(1);
  }
  STAMP(7);
  WAIT_BAR(0);
  STAMP(8);
  if (wid == 0) {
    const bool sel = hs.act && ln >= 8u;
    if (sel) __hip_atomic_store(hs.base + hs.peer, (*(const LAS unsigned*)(xch + (ln & 7u) * 4u) & 0xFFFFu) | 0x10000u, RLX_AGENT);
    const unsigned fv = hs_wait_flags(hs, sel);
    if (sel) *(LAS unsigned*)(xch + 64u + (ln & 7u) * 4u) = fv;
  }
  BAR();
  STAMP(9);
  if (g == 0) {
    u32x4 pv[8];
#pragma unroll
    for (int u = 0; u < 8; ++u) pv[u] = __builtin_amdgcn_raw_buffer_load_b128(srdP, hg * 4096u + w4 * 1024u + ln * 16u, (unsigned)u * 32768u, 18);
    const u32x4 f0 = *(const LAS u32x4*)(xch + 64u), f1 = *(const LAS u32x4*)(xch + 80u);
    float acc[8] = {};
#pragma unroll
    for (int u = 0; u < 8; ++u) {
      const unsigned e = ((u < 4 ? f0[u & 3] : f1[u & 3]) >> (8u * (w4 >> 1))) & 0xFFu;
      const float isc = __uint_as_float((e - 14u) << 23);
#pragma unroll
      for (int q = 0; q < 4; ++q) { const unsigned wv = pv[u][q]; const f16x2 hv = __builtin_bit_cast(f16x2, wv); acc[2 * q] = fmaf((float)hv[0], isc, acc[2 * q]); acc[2 * q + 1] = fmaf((float)hv[1], isc, acc[2 * q + 1]); }
    }
    const unsigned qrow = (hg & 3u) * 32u + (ln & 31u), n = j * 128u + ((hg >> 2) * 2u + (w4 >> 1)) * 32u + (w4 & 1u) * 16u + (ln >> 5) * 8u;
    *(u32x4*)(out + ((size_t)b * SQ + qrow) * DM + n) = (u32x4){cvtpk(acc[0], acc[1]), cvtpk(acc[2], acc[3]), cvtpk(acc[4], acc[5]), cvtpk(acc[6], acc[7])};
  }
  STAMP(10);
}

extern "C" void kernel_launch(void* const* d_in, const int* in_sizes, int n_in, void* d_out, int out_size, void* d_ws, size_t ws_size, hipStream_t stream) {
  (void)in_sizes; (void)n_in; (void)out_size; (void)ws_size;
  (void)hipFuncSetAttribute((const void*)mha_fused, hipFuncAttributeMaxDynamicSharedMemorySize, (int)LDS_BYTES);
  hipLaunchKernelGGL(mha_fused, dim3(256), dim3(512), LDS_BYTES, stream, (const unsigned short*)d_in[0], (const unsigned short*)d_in[1], (const unsigned short*)d_in[2],
                     (const unsigned short*)d_in[3], (const unsigned short*)d_in[4], (unsigned short*)d_out, (char*)d_ws);
}
```

```cpp
#include <hip/hip_runtime.h>
#include <hip/hip_bf16.h>
#include <stdint.h>
#include <type_traits>

typedef __bf16 bf16x8 __attribute__((ext_vector_type(8)));
typedef __bf16 bf16x2 __attribute__((ext_vector_type(2)));
typedef _Float16 f16x2 __attribute__((ext_vector_type(2)));
typedef short s16x4 __attribute__((ext_vector_type(4)));
typedef float f32x16 __attribute__((ext_vector_type(16)));
typedef float f32x4 __attribute__((ext_vector_type(4)));
typedef float f32x2 __attribute__((ext_vector_type(2)));
typedef unsigned u32x4 __attribute__((ext_vector_type(4)));
typedef unsigned u32x2 __attribute__((ext_vector_type(2)));
#define LAS __attribute__((address_space(3)))
#define GAS __attribute__((address_space(1)))
#define DI __device__ __forceinline__
#define MFMA(a, b, c) __builtin_amdgcn_mfma_f32_32x32x16_bf16((a), (b), (c), 0, 0, 0)
#define RLX_AGENT __ATOMIC_RELAXED, __HIP_MEMORY_SCOPE_AGENT
#define PIN(x) asm volatile("" : "+v"(x))
#define SBAR() __builtin_amdgcn_sched_barrier(0)
#define PRIO(n) __builtin_amdgcn_s_setprio(n)

constexpr int SQ = 128, DM = 512, NH = 32, SK = 2048, DH = 128;
constexpr unsigned U = 16384u, SL = 32768u;
constexpr unsigned LDS_BYTES = 5 * SL;
#define CODEPF 17
#define PACE 114u
#define PACE0 480u
#define PSLIM 8192.0f
constexpr float CSC = 0.088388347648318447f * 1.4426950408889634f;
constexpr size_t WS_FLAG = 0;
constexpr unsigned FB = 128;
constexpr size_t WS_O = 1 << 20;
constexpr size_t WS_P = WS_O + (8u << 20);
constexpr size_t WS_END = WS_P + (16u << 20);

DI unsigned rfl(unsigned x) { return __builtin_amdgcn_readfirstlane(x); }
DI unsigned cvtpk(float lo, float hi) { f32x2 v = {lo, hi}; bf16x2 b = __builtin_convertvector(v, bf16x2); return __builtin_bit_cast(unsigned, b); }
DI bf16x8 pack8(const f32x16& a, int s) {
  u32x4 w = {cvtpk(a[8 * s], a[8 * s + 1]), cvtpk(a[8 * s + 2], a[8 * s + 3]), cvtpk(a[8 * s + 4], a[8 * s + 5]), cvtpk(a[8 * s + 6], a[8 * s + 7])};
  return __builtin_bit_cast(bf16x8, w);
}
DI bf16x8 lds_b128(unsigned a) { return *(const LAS bf16x8*)a; }
DI f32x4 lds_f4(unsigned a) { return *(const LAS f32x4*)a; }
DI void lds_st4(unsigned a, const f32x16& v, int q) { *(LAS f32x4*)a = (f32x4){v[4 * q], v[4 * q + 1], v[4 * q + 2], v[4 * q + 3]}; }
DI s16x4 lds_tr(unsigned a) { return __builtin_amdgcn_ds_read_tr16_b64_v4i16((LAS s16x4*)a); }
DI bf16x8 lds_tr2(unsigned a0, unsigned a1) {
  s16x4 lo = lds_tr(a0), hi = lds_tr(a1);
  return __builtin_bit_cast(bf16x8, __builtin_shufflevector(lo, hi, 0, 1, 2, 3, 4, 5, 6, 7));
}
DI void dma4_kv(unsigned m0v, unsigned voff, __amdgpu_buffer_rsrc_t srd, unsigned soff) {
  asm volatile("s_mov_b32 m0, %0\n\ts_nop 3\n\t"
               "buffer_load_dwordx4 %1, %2, %3 offen nt lds\n\t"
               "buffer_load_dwordx4 %1, %2, %3 offen offset:1024 nt lds\n\t"
               "buffer_load_dwordx4 %1, %2, %3 offen offset:2048 nt lds\n\t"
               "buffer_load_dwordx4 %1, %2, %3 offen offset:3072 nt lds"
               :: "s"(rfl(m0v)), "v"(voff), "s"(srd), "s"(rfl(soff)) : "memory");
}
DI void dma1(unsigned m0v, unsigned voff, __amdgpu_buffer_rsrc_t srd, unsigned soff) {
  asm volatile("s_mov_b32 m0, %0\n\ts_nop 3\n\t"
               "buffer_load_dwordx4 %1, %2, %3 offen lds"
               :: "s"(rfl(m0v)), "v"(voff), "s"(srd), "s"(rfl(soff)) : "memory");
}
DI void dma1nt(unsigned m0v, unsigned voff, __amdgpu_buffer_rsrc_t srd, unsigned soff) {
  asm volatile("s_mov_b32 m0, %0\n\ts_nop 3\n\t"
               "buffer_load_dwordx4 %1, %2, %3 offen nt lds"
               :: "s"(rfl(m0v)), "v"(voff), "s"(srd), "s"(rfl(soff)) : "memory");
}
#define WAIT_BAR(N) asm volatile("s_waitcnt vmcnt(" #N ") lgkmcnt(0)\n\ts_barrier" ::: "memory")
#define BAR() asm volatile("s_waitcnt lgkmcnt(0)\n\ts_barrier" ::: "memory")
#define WAITVM(N) asm volatile("s_waitcnt vmcnt(" #N ")" ::: "memory")

DI float xhalf_max(float v) { auto r = __builtin_amdgcn_permlane32_swap(__float_as_uint(v), __float_as_uint(v), false, false); return fmaxf(__uint_as_float(r[0]), __uint_as_float(r[1])); }
DI float xhalf_sum(float v) { auto r = __builtin_amdgcn_permlane32_swap(__float_as_uint(v), __float_as_uint(v), false, false); return __uint_as_float(r[0]) + __uint_as_float(r[1]); }

DI unsigned wave_umax(unsigned v) {
#define DPPMAX(ctrl, rmask) v = max(v, (unsigned)__builtin_amdgcn_update_dpp(0, (int)v, ctrl, rmask, 0xF, false))
  DPPMAX(0x111, 0xF); DPPMAX(0x112, 0xF); DPPMAX(0x114, 0xF); DPPMAX(0x118, 0xF);
  DPPMAX(0x142, 0xA); DPPMAX(0x143, 0xC);
#undef DPPMAX
  return (unsigned)__builtin_amdgcn_readlane((int)v, 63);
}
struct Hs { GAS unsigned* base; unsigned mine, peer; bool act; };
DI Hs hs_make(char* ws, unsigned bid, unsigned L) {
  Hs h; h.base = (GAS unsigned*)(ws + WS_FLAG);
  const bool h1 = L < 4u; h.act = h1 || (L >= 8u && L < 16u);
  const unsigned pb = h1 ? ((bid & ~24u) | (L << 3)) : ((bid & ~7u) | (L & 7u));
  h.mine = bid * FB + (L & 15u); h.peer = pb * FB + (h1 ? ((bid >> 3) & 3u) : 8u + (bid & 7u));
  return h;
}
DI void hs_echo(const Hs& h) { if (h.act) __hip_atomic_store(h.base + h.peer + 64u, __hip_atomic_load(h.base + h.mine + 32u, RLX_AGENT), RLX_AGENT); }
DI void hs_wait_armed(const Hs& h) {
#pragma nounroll
  for (unsigned spins = 0; spins < (1u << 20); ++spins) {
    hs_echo(h);
    bool ok = true;
    if (h.act) ok = __hip_atomic_load(h.base + h.mine + 64u, RLX_AGENT) == __hip_atomic_load(h.base + h.peer + 32u, RLX_AGENT);
    if (__all(ok)) break;
    __builtin_amdgcn_s_sleep(4);
  }
}
DI unsigned hs_wait_flags(const Hs& h, bool sel) {
  unsigned v = 1u;
#pragma nounroll
  for (unsigned spins = 0; spins < (1u << 20); ++spins) {
    if (sel) v = __hip_atomic_load(h.base + h.mine, RLX_AGENT);
    if (__all(v != 0u)) break;
    if ((spins & 15u) == 15u) hs_echo(h);
    __builtin_amdgcn_s_sleep(1);
  }
  asm volatile("" ::: "memory");
  return v;
}

#define STAMP(i) do { } while (0)

__global__ __launch_bounds__(512, 2) void mha_fused(const unsigned short* __restrict__ x, const unsigned short* __restrict__ kc, const unsigned short* __restrict__ vc,
                                                    const unsigned short* __restrict__ wq, const unsigned short* __restrict__ wo, unsigned short* __restrict__ out, char* __restrict__ ws) {
  extern __shared__ __attribute__((aligned(16))) char lds[];
  const unsigned tid = threadIdx.x, lane = tid & 63u, r32 = lane & 31u, hi = lane >> 5;
  const unsigned wid = rfl(tid >> 6), g = wid >> 2, w4 = wid & 3u;
  const unsigned bid = blockIdx.x, hg = bid & 7u, b = bid >> 5, j = (bid >> 3) & 3u, h = hg * 4u + j;
  const unsigned lds0 = (unsigned)(uintptr_t)lds;
  STAMP(0);
  const unsigned tref = (unsigned)__builtin_amdgcn_s_memrealtime() + PACE0 + (((bid >> 3) * PACE) >> 5);
  auto due = [&](unsigned t) { unsigned long long t_; asm volatile("s_memrealtime %0\n\ts_waitcnt lgkmcnt(0)" : "=s"(t_) :: "memory"); return (int)((unsigned)t_ - tref - t) >= 0; };
  auto pace = [&](unsigned t) { while (!due(t)) __builtin_amdgcn_s_sleep(2); };
  asm volatile("" :: "s"(out), "s"(ws), "s"(wo));

  const __amdgpu_buffer_rsrc_t srdK = __builtin_amdgcn_make_buffer_rsrc((void*)(kc + (size_t)(b * NH + h) * SK * DH), (short)0, SK * DH * 2, 0x00020000);
  const __amdgpu_buffer_rsrc_t srdV = __builtin_amdgcn_make_buffer_rsrc((void*)(vc + (size_t)(b * NH + h) * SK * DH), (short)0, SK * DH * 2, 0x00020000);
  const __amdgpu_buffer_rsrc_t srdX = __builtin_amdgcn_make_buffer_rsrc((void*)(x + (size_t)b * SQ * DM), (short)0, SQ * DM * 2, 0x00020000);
  const __amdgpu_buffer_rsrc_t srdWq = __builtin_amdgcn_make_buffer_rsrc((void*)wq, (short)0, DM * NH * DH * 2, 0x00020000);
  const __amdgpu_buffer_rsrc_t srdWo = __builtin_amdgcn_make_buffer_rsrc((void*)(wo + (size_t)hg * 512 * DM), (short)0, 512 * DM * 2, 0x00020000);

  unsigned w8k = w4 * 8192u; asm volatile("" : "+s"(w8k));
  const unsigned l4 = lane >> 4, l15 = lane & 15u;
  const unsigned voffK = w4 * 4096u + l4 * 256u + ((l15 ^ (l4 | (w4 << 2))) << 4);
  const unsigned voffV = w4 * 4096u + l4 * 256u + ((l15 ^ (l4 << 2)) << 4);
  const unsigned voffWq = l4 * 8192u + ((l15 ^ (l4 << 2)) << 4);
  const unsigned voffWo = l4 * 1024u + ((l15 ^ (l4 << 2)) << 4);
  const unsigned voffX = (lane >> 3) * 1024u + (((lane & 7u) ^ (((lane >> 4) & 1u) | (w4 << 1))) << 4);

  auto issueK = [&](unsigned t, unsigned off) { dma4_kv(lds0 + off + w4 * 4096u, voffK, srdK, t * U); };
  auto issueV = [&](unsigned t, unsigned off) { dma4_kv(lds0 + off + w4 * 4096u, voffV, srdV, t * U); };
  auto hpos = [&](unsigned cc) { const unsigned r = cc - 1u + b % 3u; return rfl(cc == 0u ? j : (j + 1u + ((0x124u >> (2u * r)) & 3u)) & 3u); };
  auto issueWo = [&](unsigned cc, unsigned slot) {
    const unsigned c = hpos(cc);
#pragma unroll
    for (int q = 0; q < 8; ++q) dma1(lds0 + slot * SL + w8k + q * 1024u, voffWo, srdWo, (c * 128u + w4 * 32u + q * 4u) * 1024u + j * 256u);
  };
  const unsigned rot = (b + 2u * j) & 7u;
  auto qslot = [](int c) { return (c % 3 == 0) ? 3u : (c % 3 == 1) ? 4u : 2u; };
  auto issueQP = [&](int c) {
    const unsigned kcn = ((unsigned)c + rot) & 7u, sb = lds0 + qslot(c) * SL;
#pragma unroll
    for (int q = 0; q < 4; ++q) {
      dma1nt(sb + (w4 * 4u + q) * 1024u, voffWq, srdWq, (kcn * 64u + w4 * 16u + q * 4u) * 8192u + h * 256u);
      dma1(sb + U + (q * 4u + w4) * 1024u, voffX, srdX, (q * 4u + w4) * 8192u + kcn * 128u);
    }
  };
  const unsigned blk = (lane >> 4) & 1u, q4 = l15 >> 2, p4 = lane & 3u, pp = ((p4 & 1u) << 1) | (p4 >> 1);
  const unsigned trV = lds0 + U + (4u * hi + q4) * 256u + ((q4 ^ (2u * g)) << 6) + blk * 32u + pp * 8u;
  const unsigned rdK = lds0 + ((r32 & 7u) + 16u * (r32 >> 3)) * 256u + ((hi ^ ((r32 & 3u) | ((r32 >> 3) << 2))) << 4);
  const unsigned qx = lds0 + 3u * SL + w4 * 4096u + lane * 16u;

  bf16x8 qf[8], kf[16];
  auto rdk = [&](unsigned off, int i) { return lds_b128(((rdK + off) ^ (unsigned)((i & 7) << 5)) + (i >> 3) * 2048); };
  if (g == 0) {
    const unsigned trW = lds0 + (8u * hi + q4) * 256u + (q4 << 6) + blk * 32u + pp * 8u;
    const unsigned rdX = lds0 + U + (w4 * 32u + r32) * 128u + ((hi ^ (((r32 >> 1) & 1u) | ((r32 >> 3) << 1))) << 4);
    f32x16 qa[4] = {};
    bf16x8 xb[2][4], wa_[2][16];
    auto rdq = [&](int c, int s) {
      const unsigned u0 = qslot(c) * SL;
      xb[c & 1][s] = lds_b128((rdX + u0) ^ (unsigned)(s << 5));
#pragma unroll
      for (int d = 0; d < 4; ++d) { const unsigned a = ((trW + u0) ^ (unsigned)(d << 6)) + s * 4096; wa_[c & 1][4 * s + d] = lds_tr2(a, a + 1024); }
    };
    {
      unsigned long long pc; asm volatile("s_getpc_b64 %0" : "=s"(pc));
      const __amdgpu_buffer_rsrc_t srdC = __builtin_amdgcn_make_buffer_rsrc((void*)(pc & ~255ull), (short)0, CODEPF * 1024, 0x00020000);
      if (wid == 1) dma1(lds0, lane * 16u, srdC, ((bid >> 3) % CODEPF) * 1024u);
    }
    unsigned r0;
    { const Hs hs = hs_make(ws, bid, lane);
      if (wid == 0 && hs.act) __hip_atomic_store(hs.base + hs.mine, 0u, RLX_AGENT);
      asm volatile("global_load_dword %0, %1, off offset:256 sc1" : "=v"(r0) : "v"(hs.base + hs.mine) : "memory"); }
    BAR();
#pragma unroll
    for (int s = 0; s < 4; ++s) rdq(0, s);
#pragma unroll
    for (int c = 0; c < 8; ++c) {
      if (c == 0) {
        asm volatile("s_waitcnt vmcnt(0) lgkmcnt(0)\n\ts_barrier" : "+v"(r0) :: "memory");
        const Hs hs = hs_make(ws, bid, lane);
        if (wid == 0 && hs.act) __hip_atomic_store(hs.base + hs.peer + 32u, r0 + 1u, RLX_AGENT);
      } else if (c < 7) BAR();
#pragma unroll
      for (int s = 0; s < 4; ++s)
#pragma unroll
        for (int d = 0; d < 4; ++d) {
          SBAR();
          qa[d] = MFMA(wa_[c & 1][4 * s + d], xb[c & 1][s], qa[d]);
          SBAR();
          if (c < 7) {
            const unsigned u0 = qslot(c + 1) * SL, a = ((trW + u0) ^ (unsigned)(d << 6)) + s * 4096;
            if (d == 0) xb[(c + 1) & 1][s] = lds_b128((rdX + u0) ^ (unsigned)(s << 5));
            wa_[(c + 1) & 1][4 * s + d] = lds_tr2(a, a + 1024);
          }
        }
      SBAR();
    }
#pragma unroll
    for (int d = 0; d < 4; ++d) {
#pragma unroll
      for (int r = 0; r < 16; ++r) qa[d][r] *= CSC;
      qf[2 * d] = pack8(qa[d], 0); qf[2 * d + 1] = pack8(qa[d], 1);
    }
#pragma unroll
    for (int f = 0; f < 8; ++f) *(LAS bf16x8*)(qx + (f >> 2) * U + (f & 3) * 1024) = qf[f];
    BAR();
#pragma unroll
    for (int i = 0; i < 16; ++i) kf[i] = rdk(U, i);
  } else {
    issueQP(0); issueQP(1);
    WAIT_BAR(8); issueQP(2);
#pragma unroll
    for (int c = 0; c < 5; ++c) { WAIT_BAR(8); issueQP(c + 3); }
    issueK(0, U);
    WAIT_BAR(12);
    issueK(1, 0);
    WAIT_BAR(8);
    pace(PACE / 2u); issueK(2, SL); pace(PACE); issueV(0, SL + U);
    WAIT_BAR(8);
    const Hs hs = hs_make(ws, bid, lane);
    unsigned rq;
    asm volatile("global_load_dword %0, %1, off offset:128 sc1" : "=v"(rq) : "v"(hs.base + hs.mine) : "memory");
#pragma unroll
    for (int f = 0; f < 8; ++f) qf[f] = lds_b128(qx + (f >> 2) * U + (f & 3) * 1024);
#pragma unroll
    for (int i = 0; i < 16; ++i) kf[i] = rdk(0, i);
    pace(PACE + PACE / 2u); issueK(3, 2u * SL); pace(2u * PACE); issueV(1, 2u * SL + U);
    pace(2u * PACE + PACE / 2u);
    issueK(4, 3u * SL);
    pace(3u * PACE);
    issueV(2, 3u * SL + U);
    asm volatile("s_waitcnt vmcnt(16) lgkmcnt(0)\n\ts_barrier" : "+v"(rq) :: "memory");
    if (wid == 4 && hs.act) __hip_atomic_store(hs.base + hs.peer + 64u, rq, RLX_AGENT);
  }
  STAMP(1);

  f32x16 o[4] = {};
  float m_run = 0.f, l_run = 0.f;
  f32x16 negm = {};
  bf16x8 pf[4], vf[16];
  auto vfrag = [&](unsigned av, int ks, int d) { const unsigned a = (av ^ (unsigned)(d << 6)) + (32 * (ks & 1) + 8 * (ks >> 1)) * 256; return lds_tr2(a, a + 4096); };
  bool vpend = false; unsigned vdst = 0u, vsrc = 0u;
  auto scores_slow = [&](bool first, float& ps) {
    f32x16 s0 = {}, s1 = {};
#pragma unroll
    for (int s = 0; s < 8; ++s) { s0 = MFMA(kf[s], qf[s], s0); s1 = MFMA(kf[8 + s], qf[s], s1); }
    float mx = fmaxf(s0[0], s1[0]);
#pragma unroll
    for (int r = 1; r < 16; ++r) mx = fmaxf(fmaxf(mx, s0[r]), s1[r]);
    mx = xhalf_max(mx);
    const float m_new = first ? mx : fmaxf(mx, m_run);
    if (!first) {
      const float alpha = __builtin_amdgcn_exp2f(m_run - m_new);
      l_run *= alpha;
#pragma unroll
      for (int d = 0; d < 4; ++d)
#pragma unroll
        for (int r = 0; r < 16; ++r) o[d][r] *= alpha;
    }
    m_run = m_new;
    ps = 0.f;
#pragma unroll
    for (int r = 0; r < 16; ++r) { negm[r] = -m_new; s0[r] = __builtin_amdgcn_exp2f(s0[r] - m_new); s1[r] = __builtin_amdgcn_exp2f(s1[r] - m_new); ps += s0[r] + s1[r]; }
    pf[0] = pack8(s0, 0); pf[1] = pack8(s0, 1); pf[2] = pack8(s1, 0); pf[3] = pack8(s1, 1);
  };
  auto scores = [&](bool first, auto inloop) {
    float ps = 0.f;
    PRIO(2);
    if (!first) {
      SBAR();
      f32x16 s0 = MFMA(kf[0], qf[0], negm);
      SBAR();
#pragma unroll
      for (int s = 1; s < 8; ++s) {
        s0 = MFMA(kf[s], qf[s], s0); SBAR();
        if (decltype(inloop)::value && (s & 1)) { PIN(s0); if (vpend) dma1nt(vdst + (s >> 1) * 1024u, voffV, srdV, vsrc + (s >> 1) * 1024u); SBAR(); }
      }
      SBAR();
      f32x16 s1;
      asm volatile("v_mfma_f32_32x32x16_bf16 %0, %1, %2, %3" : "=&v"(s1) : "v"(kf[8]), "v"(qf[0]), "v"(negm));
      SBAR();
      float pa = 0.f, pb = 0.f;
      unsigned w[16];
#define EXP2(S, e) do { S[e] = __builtin_amdgcn_exp2f(S[e]); S[e + 1] = __builtin_amdgcn_exp2f(S[e + 1]); float t0_ = S[e], t1_ = S[e + 1]; PIN(t0_); PIN(t1_); S[e] = t0_; S[e + 1] = t1_; SBAR(); } while (0)
#define FIN2(S, e, wi) do { pa += S[e]; pb += S[e + 1]; w[wi] = cvtpk(S[e], S[e + 1]); PIN(pa); PIN(pb); PIN(w[wi]); SBAR(); } while (0)
#pragma unroll
      for (int s = 1; s < 8; ++s) {
        s1 = MFMA(kf[8 + s], qf[s], s1); SBAR();
        EXP2(s0, 2 * (s - 1));
        if (s > 1) FIN2(s0, 2 * (s - 2), s - 2);
      }
      EXP2(s0, 14); FIN2(s0, 12, 6);
      EXP2(s1, 0); FIN2(s0, 14, 7);
#pragma unroll
      for (int i = 1; i < 8; ++i) { EXP2(s1, 2 * i); FIN2(s1, 2 * (i - 1), 7 + i); }
      FIN2(s1, 14, 15);
#undef EXP2
#undef FIN2
      ps = pa + pb;
#pragma unroll
      for (int i = 0; i < 4; ++i) pf[i] = __builtin_bit_cast(bf16x8, (u32x4){w[4 * i], w[4 * i + 1], w[4 * i + 2], w[4 * i + 3]});
    }
    if (__builtin_expect(first || __any(!(ps <= PSLIM)), 0)) scores_slow(first, ps);
    l_run += ps;
    PIN(pf[0]); PIN(pf[1]); PIN(pf[2]); PIN(pf[3]); PIN(l_run); if (decltype(inloop)::value) PIN(negm);
    PRIO(0);
  };

  __builtin_amdgcn_s_waitcnt(0xC07F);
  unsigned sp = 1u + g;
  const unsigned p0 = 3u + ((((bid >> 3) * 11u) & 31u) * 3u >> 2);
  for (int it = 0; it < 15; ++it) {
    const unsigned p = 2u * (unsigned)it + g + 1u;
    scores(it == 0, std::true_type{});
    unsigned long long ts;
    asm volatile("s_waitcnt vmcnt(0) lgkmcnt(0)\n\ts_barrier\n\ts_memrealtime %0" : "=s"(ts) :: "memory");
    const unsigned av = trV + sp * SL;
#pragma unroll
    for (int i = 0; i < 16; ++i) vf[i] = vfrag(av, i >> 2, i & 3);
    SBAR();
    SBAR();
#pragma unroll
    for (int i = 0; i < 8; ++i) kf[i] = rdk(sp * SL, i);
    SBAR();
    const unsigned sn = sp >= 3u ? sp - 3u : sp + 2u;
    const bool go = p != p0;
    const unsigned ni = p + (p < p0 ? 3u : 2u), ns = (p < p0 ? (sn == 4u ? 0u : sn + 1u) : sn) * SL;
    const unsigned tni = (ni + min(ni, 30u)) * (PACE / 2u);
    bool kgo = go && ni <= 30u;
    SBAR();
#pragma unroll
    for (int i = 0; i < 2; ++i) { o[i & 3] = MFMA(vf[i], pf[i >> 2], o[i & 3]); SBAR(); }
    asm volatile("s_waitcnt lgkmcnt(0)" : "+s"(ts) :: "memory");
    const int late = (int)((unsigned)ts - tref - tni);
    const bool vend = ni >= 31u;
    if (kgo && late + (int)(PACE / 2u) + 15 >= 0) { issueK(ni + 1u, ns); kgo = false; }
    if (vend) issueV(ni - 1u, ns + U);
    SBAR();
#pragma unroll
    for (int i = 2; i < 8; ++i) { o[i & 3] = MFMA(vf[i], pf[i >> 2], o[i & 3]); SBAR(); }
#pragma unroll
    for (int i = 8; i < 16; ++i) { o[i & 3] = MFMA(vf[i], pf[i >> 2], o[i & 3]); if (i < 12) { kf[2 * i - 8] = rdk(sp * SL, 2 * i - 8); kf[2 * i - 7] = rdk(sp * SL, 2 * i - 7); } SBAR(); }
    if (kgo) { pace(tni - PACE / 2u); issueK(ni + 1u, ns); }
    if (go && !vend && late + 35 < 0) pace(tni);
    vpend = go && !vend; vdst = lds0 + ns + U + w4 * 4096u; vsrc = (ni - 1u) * U;
    if (it == 14) issueWo(g, 3u + g);
    sp = sn;
    __builtin_amdgcn_s_waitcnt(0xC07F);
    asm volatile("s_barrier" ::: "memory");
  }
  scores(false, std::false_type{});
  unsigned ln; asm volatile("v_mbcnt_lo_u32_b32 %0, -1, 0\n\tv_mbcnt_hi_u32_b32 %0, -1, %0" : "=v"(ln));
  const unsigned x0 = lds0 + w8k + ln * 16u;
  l_run = xhalf_sum(l_run);
  const unsigned stats = lds0 + 2u * SL + (ln & 31u) * 8u;
  if (ln < 32u) *(LAS f32x2*)(stats + wid * 256u) = (f32x2){m_run, l_run};
  const Hs hs = hs_make(ws, bid, ln);
  unsigned hs_r, hs_q, hs_qi, hs_ro;
  asm volatile("global_load_dword %0, %4, off offset:256 sc1\n\tglobal_load_dword %1, %5, off offset:128 sc1\n\t"
               "global_load_dword %2, %4, off offset:128 sc1\n\tglobal_load_dword %3, %5, off offset:256 sc1"
               : "=&v"(hs_r), "=&v"(hs_q), "=&v"(hs_qi), "=&v"(hs_ro) : "v"(hs.base + hs.mine), "v"(hs.base + hs.peer) : "memory");
  bf16x8 of0[4];
  {
    WAIT_BAR(12);
    STAMP(2);
    const unsigned av = trV + (1u + g) * SL;
#pragma unroll
    for (int ks = 0; ks < 4; ++ks) { vf[4 * ks + 2] = vfrag(av, ks, 2); vf[4 * ks + 3] = vfrag(av, ks, 3); }
    SBAR();
#pragma unroll
    for (int ks = 0; ks < 4; ++ks) { vf[4 * ks] = vfrag(av, ks, 0); vf[4 * ks + 1] = vfrag(av, ks, 1); }
    SBAR();
#pragma unroll
    for (int ks = 0; ks < 4; ++ks) { o[2] = MFMA(vf[4 * ks + 2], pf[ks], o[2]); o[3] = MFMA(vf[4 * ks + 3], pf[ks], o[3]); }
    SBAR();
#pragma unroll
    for (int ks = 0; ks < 2; ++ks) { o[0] = MFMA(vf[4 * ks], pf[ks], o[0]); o[1] = MFMA(vf[4 * ks + 1], pf[ks], o[1]); }
    SBAR();
    const unsigned xo = x0 + g * SL, xi = x0 + (1u - g) * SL;
#pragma unroll
    for (int q = 0; q < 4; ++q) { lds_st4(xo + q * 1024, o[2], q); lds_st4(xo + 4096 + q * 1024, o[3], q); }
    SBAR();
#pragma unroll
    for (int ks = 2; ks < 4; ++ks) { o[0] = MFMA(vf[4 * ks], pf[ks], o[0]); o[1] = MFMA(vf[4 * ks + 1], pf[ks], o[1]); }
    SBAR();
    if (g == 0) BAR();
    const f32x2 st = *(const LAS f32x2*)(stats + (wid ^ 4u) * 256u);
    const float ms = fmaxf(m_run, st[0]);
    const float e_me = __builtin_amdgcn_exp2f(m_run - ms), e_ot = __builtin_amdgcn_exp2f(st[0] - ms);
    const float inv = 1.f / (l_run * e_me + st[1] * e_ot);
    float a_me = e_me * inv, a_ot = e_ot * inv;
    PIN(a_me); PIN(a_ot);
    asm volatile("s_waitcnt vmcnt(0) lgkmcnt(0)\n\ts_barrier" : "+v"(hs_r), "+v"(hs_q), "+v"(hs_qi), "+v"(hs_ro) :: "memory");
    f32x4 w0[4], w1[4];
#pragma unroll
    for (int q = 0; q < 4; ++q) { w0[q] = lds_f4(xi + q * 1024); w1[q] = lds_f4(xi + 4096 + q * 1024); }
#pragma unroll
    for (int q = 0; q < 4; ++q)
#pragma unroll
      for (int e = 0; e < 4; ++e) { o[0][4 * q + e] = fmaf(w0[q][e], a_ot, o[0][4 * q + e] * a_me); o[1][4 * q + e] = fmaf(w1[q][e], a_ot, o[1][4 * q + e] * a_me); }
    of0[0] = pack8(o[0], 0); of0[1] = pack8(o[0], 1); of0[2] = pack8(o[1], 0); of0[3] = pack8(o[1], 1);
  }
  PRIO(0);
  const __amdgpu_buffer_rsrc_t srdO = __builtin_amdgcn_make_buffer_rsrc((void*)(ws + WS_O + (size_t)(hg * 8u + b) * 131072u), (short)0, 4 * 32768, 0x00020000);
#pragma unroll
  for (int s = 0; s < 4; ++s) __builtin_amdgcn_raw_buffer_store_b128(__builtin_bit_cast(u32x4, of0[s]), srdO, j * 32768u + wid * 4096u + (unsigned)s * 1024u + ln * 16u, 0, 16);
  STAMP(3);
  f32x16 c[4] = {};
  const unsigned trW2 = trV - U + (ln >> 5) * 1024u + g * U;
  auto wfrag = [&](unsigned slot, int s, int nb) { const unsigned a = ((trW2 + slot * SL) ^ (unsigned)(nb << 6)) + s * 4096; return lds_tr2(a, a + 1024); };
  bf16x8 w1[16], w2[16];
#pragma unroll
  for (int i = 0; i < 16; ++i) w2[i] = wfrag(3, i >> 2, i & 3);
  SBAR();
#pragma unroll
  for (int i = 0; i < 16; ++i) { c[i & 3] = MFMA(w2[i], of0[i >> 2], c[i & 3]); w1[i] = wfrag(4, i >> 2, i & 3); SBAR(); }
  PIN(c[0]); PIN(c[1]); PIN(c[2]); PIN(c[3]);
  WAIT_BAR(0);
  STAMP(4);
  if (wid == 0) {
    if (hs.act && hs_qi != hs_ro) __hip_atomic_store(hs.base + hs.peer + 64u, hs_qi, RLX_AGENT);
    if (!__all(hs_r == hs_q || !hs.act)) hs_wait_armed(hs);
    if (ln < 4u) __hip_atomic_store(hs.base + hs.peer, 1u, RLX_AGENT);
  }
  issueWo(2u + g, 1u - g);
  if (wid == 0) hs_wait_flags(hs, ln < 4u);
  WAIT_BAR(0);
  STAMP(5);
  const unsigned xch = lds0 + 4u * SL;
  const __amdgpu_buffer_rsrc_t srdP = __builtin_amdgcn_make_buffer_rsrc((void*)(ws + WS_P + (size_t)(b * 4u + j) * 262144u), (short)0, 262144, 0x00020000);
  {
    bf16x8 of[12];
#pragma unroll
    for (int i = 0; i < 12; ++i)
      of[i] = __builtin_bit_cast(bf16x8, __builtin_amdgcn_raw_buffer_load_b128(srdO, wid * 4096u + (unsigned)(i & 3) * 1024u + ln * 16u, hpos(1u + (unsigned)(i >> 2)) * 32768u, 16));
    SBAR();
#pragma unroll
    for (int i = 0; i < 8; ++i) w2[i] = wfrag(1, i >> 2, i & 3);
    SBAR();
#pragma unroll
    for (int i = 0; i < 16; ++i) { c[i & 3] = MFMA(w1[i], of[i >> 2], c[i & 3]); if (i >= 8) w2[i] = wfrag(1, i >> 2, i & 3); SBAR(); }
#pragma unroll
    for (int i = 0; i < 16; ++i) { c[i & 3] = MFMA(w2[i], of[4 + (i >> 2)], c[i & 3]); w1[i] = wfrag(0, (i >> 1) & 3, (i & 1) + (i < 8 ? 2 : 0)); SBAR(); }
#pragma unroll
    for (int i = 0; i < 8; ++i) c[2 + (i & 1)] = MFMA(w1[i], of[8 + (i >> 1)], c[2 + (i & 1)]);
    SBAR();
#pragma unroll
    for (int i = 0; i < 4; ++i) c[0] = MFMA(w1[8 + 2 * i], of[8 + i], c[0]);
    SBAR();
#pragma unroll
    for (int q = 0; q < 4; ++q) { lds_st4(x0 + (2u + g) * SL + q * 1024, c[2], q); lds_st4(x0 + (2u + g) * SL + 4096 + q * 1024, c[3], q); }
    SBAR();
#pragma unroll
    for (int i = 0; i < 4; ++i) c[1] = MFMA(w1[9 + 2 * i], of[8 + i], c[1]);
    PIN(c[0]); PIN(c[1]);
    BAR();
    STAMP(6);
    const unsigned ra = x0 + (3u - g) * SL;
    f32x4 va[8];
#pragma unroll
    for (int q = 0; q < 8; ++q) va[q] = lds_f4(ra + q * 1024);
    SBAR();
    auto put = [&](int nb) {
      float mx = 0.f;
#pragma unroll
      for (int r = 0; r < 16; ++r) { c[nb][r] += va[4 * nb + (r >> 2)][r & 3]; mx = fmaxf(mx, fabsf(c[nb][r])); }
      const unsigned e = max(wave_umax(__float_as_uint(mx)) >> 23, 15u);
      const float sc = __uint_as_float((268u - e) << 23);
#pragma unroll
      for (int s = 0; s < 2; ++s) {
        u32x4 w;
#pragma unroll
        for (int q = 0; q < 4; ++q) { f32x2 v = {c[nb][8 * s + 2 * q] * sc, c[nb][8 * s + 2 * q + 1] * sc}; w[q] = __builtin_bit_cast(unsigned, __builtin_convertvector(v, f16x2)); }
        __builtin_amdgcn_raw_buffer_store_b128(w, srdP, hg * 32768u + wid * 4096u + (unsigned)(2 * nb + s) * 1024u + ln * 16u, 0, 16);
      }
      if (ln == 0) *(LAS unsigned char*)(xch + wid * 4u + (unsigned)nb) = (unsigned char)e;
    };
    put(0);
    put(1);
  }
  STAMP(7);
  WAIT_BAR(0);
  STAMP(8);
  if (wid == 0) {
    const bool sel = hs.act && ln >= 8u;
    if (sel) __hip_atomic_store(hs.base + hs.peer, (*(const LAS unsigned*)(xch + (ln & 7u) * 4u) & 0xFFFFu) | 0x10000u, RLX_AGENT);
    const unsigned fv = hs_wait_flags(hs, sel);
    if (sel) *(LAS unsigned*)(xch + 64u + (ln & 7u) * 4u) = fv;
  }
  BAR();
  STAMP(9);
  if (g == 0) {
    u32x4 pv[8];
#pragma unroll
    for (int u = 0; u < 8; ++u) pv[u] = __builtin_amdgcn_raw_buffer_load_b128(srdP, hg * 4096u + w4 * 1024u + ln * 16u, (unsigned)u * 32768u, 18);
    const u32x4 f0 = *(const LAS u32x4*)(xch + 64u), f1 = *(const LAS u32x4*)(xch + 80u);
    float acc[8] = {};
#pragma unroll
    for (int u = 0; u < 8; ++u) {
      const unsigned e = ((u < 4 ? f0[u & 3] : f1[u & 3]) >> (8u * (w4 >> 1))) & 0xFFu;
      const float isc = __uint_as_float((e - 14u) << 23);
#pragma unroll
      for (int q = 0; q < 4; ++q) { const unsigned wv = pv[u][q]; const f16x2 hv = __builtin_bit_cast(f16x2, wv); acc[2 * q] = fmaf((float)hv[0], isc, acc[2 * q]); acc[2 * q + 1] = fmaf((float)hv[1], isc, acc[2 * q + 1]); }
    }
    const unsigned qrow = (hg & 3u) * 32u + (ln & 31u), n = j * 128u + ((hg >> 2) * 2u + (w4 >> 1)) * 32u + (w4 & 1u) * 16u + (ln >> 5) * 8u;
    *(u32x4*)(out + ((size_t)b * SQ + qrow) * DM + n) = (u32x4){cvtpk(acc[0], acc[1]), cvtpk(acc[2], acc[3]), cvtpk(acc[4], acc[5]), cvtpk(acc[6], acc[7])};
  }
  STAMP(10);
}

extern "C" void kernel_launch(void* const* d_in, const int* in_sizes, int n_in, void* d_out, int out_size, void* d_ws, size_t ws_size, hipStream_t stream) {
  (void)in_sizes; (void)n_in; (void)out_size; (void)ws_size;
  (void)hipFuncSetAttribute((const void*)mha_fused, hipFuncAttributeMaxDynamicSharedMemorySize, (int)LDS_BYTES);
  hipLaunchKernelGGL(mha_fused, dim3(256), dim3(512), LDS_BYTES, stream, (const unsigned short*)d_in[0], (const unsigned short*)d_in[1], (const unsigned short*)d_in[2],
                     (const unsigned short*)d_in[3], (const unsigned short*)d_in[4], (unsigned short*)d_out, (char*)d_ws);
}
```

```cpp
#include <hip/hip_runtime.h>
#include <hip/hip_bf16.h>
#include <stdint.h>
#include <type_traits>

typedef __bf16 bf16x8 __attribute__((ext_vector_type(8)));
typedef __bf16 bf16x2 __attribute__((ext_vector_type(2)));
typedef _Float16 f16x2 __attribute__((ext_vector_type(2)));
typedef short s16x4 __attribute__((ext_vector_type(4)));
typedef float f32x16 __attribute__((ext_vector_type(16)));
typedef float f32x4 __attribute__((ext_vector_type(4)));
typedef float f32x2 __attribute__((ext_vector_type(2)));
typedef unsigned u32x4 __attribute__((ext_vector_type(4)));
typedef unsigned u32x2 __attribute__((ext_vector_type(2)));
#define LAS __attribute__((address_space(3)))
#define GAS __attribute__((address_space(1)))
#define DI __device__ __forceinline__
#define MFMA(a, b, c) __builtin_amdgcn_mfma_f32_32x32x16_bf16((a), (b), (c), 0, 0, 0)
#define RLX_AGENT __ATOMIC_RELAXED, __HIP_MEMORY_SCOPE_AGENT
#define PIN(x) asm volatile("" : "+v"(x))
#define SBAR() __builtin_amdgcn_sched_barrier(0)
#define PRIO(n) __builtin_amdgcn_s_setprio(n)

constexpr int SQ = 128, DM = 512, NH = 32, SK = 2048, DH = 128;
constexpr unsigned U = 16384u, SL = 32768u;
constexpr unsigned LDS_BYTES = 5 * SL;
#define CODEPF 17
#define PACE 114u
#define PACE0 440u
#define RAMP 18u
#define PSLIM 8192.0f
constexpr float CSC = 0.088388347648318447f * 1.4426950408889634f;
constexpr size_t WS_FLAG = 0;
constexpr unsigned FB = 128;
constexpr size_t WS_O = 1 << 20;
constexpr size_t WS_P = WS_O + (8u << 20);
constexpr size_t WS_END = WS_P + (16u << 20);

DI unsigned rfl(unsigned x) { return __builtin_amdgcn_readfirstlane(x); }
DI unsigned cvtpk(float lo, float hi) { f32x2 v = {lo, hi}; bf16x2 b = __builtin_convertvector(v, bf16x2); return __builtin_bit_cast(unsigned, b); }
DI bf16x8 pack8(const f32x16& a, int s) {
  u32x4 w = {cvtpk(a[8 * s], a[8 * s + 1]), cvtpk(a[8 * s + 2], a[8 * s + 3]), cvtpk(a[8 * s + 4], a[8 * s + 5]), cvtpk(a[8 * s + 6], a[8 * s + 7])};
  return __builtin_bit_cast(bf16x8, w);
}
DI bf16x8 lds_b128(unsigned a) { return *(const LAS bf16x8*)a; }
DI f32x4 lds_f4(unsigned a) { return *(const LAS f32x4*)a; }
DI void lds_st4(unsigned a, const f32x16& v, int q) { *(LAS f32x4*)a = (f32x4){v[4 * q], v[4 * q + 1], v[4 * q + 2], v[4 * q + 3]}; }
DI s16x4 lds_tr(unsigned a) { return __builtin_amdgcn_ds_read_tr16_b64_v4i16((LAS s16x4*)a); }
DI bf16x8 lds_tr2(unsigned a0, unsigned a1) {
  s16x4 lo = lds_tr(a0), hi = lds_tr(a1);
  return __builtin_bit_cast(bf16x8, __builtin_shufflevector(lo, hi, 0, 1, 2, 3, 4, 5, 6, 7));
}
DI void dma4_kv(unsigned m0v, unsigned voff, __amdgpu_buffer_rsrc_t srd, unsigned soff) {
  asm volatile("s_mov_b32 m0, %0\n\ts_nop 3\n\t"
               "buffer_load_dwordx4 %1, %2, %3 offen nt lds\n\t"
               "buffer_load_dwordx4 %1, %2, %3 offen offset:1024 nt lds\n\t"
               "buffer_load_dwordx4 %1, %2, %3 offen offset:2048 nt lds\n\t"
               "buffer_load_dwordx4 %1, %2, %3 offen offset:3072 nt lds"
               :: "s"(rfl(m0v)), "v"(voff), "s"(srd), "s"(rfl(soff)) : "memory");
}
DI void dma1(unsigned m0v, unsigned voff, __amdgpu_buffer_rsrc_t srd, unsigned soff) {
  asm volatile("s_mov_b32 m0, %0\n\ts_nop 3\n\t"
               "buffer_load_dwordx4 %1, %2, %3 offen lds"
               :: "s"(rfl(m0v)), "v"(voff), "s"(srd), "s"(rfl(soff)) : "memory");
}
DI void dma1nt(unsigned m0v, unsigned voff, __amdgpu_buffer_rsrc_t srd, unsigned soff) {
  asm volatile("s_mov_b32 m0, %0\n\ts_nop 3\n\t"
               "buffer_load_dwordx4 %1, %2, %3 offen nt lds"
               :: "s"(rfl(m0v)), "v"(voff), "s"(srd), "s"(rfl(soff)) : "memory");
}
#define WAIT_BAR(N) asm volatile("s_waitcnt vmcnt(" #N ") lgkmcnt(0)\n\ts_barrier" ::: "memory")
#define BAR() asm volatile("s_waitcnt lgkmcnt(0)\n\ts_barrier" ::: "memory")
#define WAITVM(N) asm volatile("s_waitcnt vmcnt(" #N ")" ::: "memory")

DI float xhalf_max(float v) { auto r = __builtin_amdgcn_permlane32_swap(__float_as_uint(v), __float_as_uint(v), false, false); return fmaxf(__uint_as_float(r[0]), __uint_as_float(r[1])); }
DI float xhalf_sum(float v) { auto r = __builtin_amdgcn_permlane32_swap(__float_as_uint(v), __float_as_uint(v), false, false); return __uint_as_float(r[0]) + __uint_as_float(r[1]); }

DI unsigned wave_umax(unsigned v) {
#define DPPMAX(ctrl, rmask) v = max(v, (unsigned)__builtin_amdgcn_update_dpp(0, (int)v, ctrl, rmask, 0xF, false))
  DPPMAX(0x111, 0xF); DPPMAX(0x112, 0xF); DPPMAX(0x114, 0xF); DPPMAX(0x118, 0xF);
  DPPMAX(0x142, 0xA); DPPMAX(0x143, 0xC);
#undef DPPMAX
  return (unsigned)__builtin_amdgcn_readlane((int)v, 63);
}
struct Hs { GAS unsigned* base; unsigned mine, peer; bool act; };
DI Hs hs_make(char* ws, unsigned bid, unsigned L) {
  Hs h; h.base = (GAS unsigned*)(ws + WS_FLAG);
  const bool h1 = L < 4u; h.act = h1 || (L >= 8u && L < 16u);
  const unsigned pb = h1 ? ((bid & ~24u) | (L << 3)) : ((bid & ~7u) | (L & 7u));
  h.mine = bid * FB + (L & 15u); h.peer = pb * FB + (h1 ? ((bid >> 3) & 3u) : 8u + (bid & 7u));
  return h;
}
DI void hs_echo(const Hs& h) { if (h.act) __hip_atomic_store(h.base + h.peer + 64u, __hip_atomic_load(h.base + h.mine + 32u, RLX_AGENT), RLX_AGENT); }
DI void hs_wait_armed(const Hs& h) {
#pragma nounroll
  for (unsigned spins = 0; spins < (1u << 20); ++spins) {
    hs_echo(h);
    bool ok = true;
    if (h.act) ok = __hip_atomic_load(h.base + h.mine + 64u, RLX_AGENT) == __hip_atomic_load(h.base + h.peer + 32u, RLX_AGENT);
    if (__all(ok)) break;
    __builtin_amdgcn_s_sleep(4);
  }
}
DI unsigned hs_wait_flags(const Hs& h, bool sel) {
  unsigned v = 1u;
#pragma nounroll
  for (unsigned spins = 0; spins < (1u << 20); ++spins) {
    if (sel) v = __hip_atomic_load(h.base + h.mine, RLX_AGENT);
    if (__all(v != 0u)) break;
    if ((spins & 15u) == 15u) hs_echo(h);
    __builtin_amdgcn_s_sleep(1);
  }
  asm volatile("" ::: "memory");
  return v;
}

#define STAMP(i) do { } while (0)

__global__ __launch_bounds__(512, 2) void mha_fused(const unsigned short* __restrict__ x, const unsigned short* __restrict__ kc, const unsigned short* __restrict__ vc,
                                                    const unsigned short* __restrict__ wq, const unsigned short* __restrict__ wo, unsigned short* __restrict__ out, char* __restrict__ ws) {
  extern __shared__ __attribute__((aligned(16))) char lds[];
  const unsigned tid = threadIdx.x, lane = tid & 63u, r32 = lane & 31u, hi = lane >> 5;
  const unsigned wid = rfl(tid >> 6), g = wid >> 2, w4 = wid & 3u;
  const unsigned bid = blockIdx.x, hg = bid & 7u, b = bid >> 5, j = (bid >> 3) & 3u, h = hg * 4u + j;
  const unsigned lds0 = (unsigned)(uintptr_t)lds;
  STAMP(0);
  const unsigned tref = (unsigned)__builtin_amdgcn_s_memrealtime() + PACE0 + (((bid >> 3) * PACE) >> 5);
  auto due = [&](unsigned t) { unsigned long long t_; asm volatile("s_memrealtime %0\n\ts_waitcnt lgkmcnt(0)" : "=s"(t_) :: "memory"); return (int)((unsigned)t_ - tref - t) >= 0; };
  auto tat = [&](unsigned hx) { const unsigned m = min(hx, 16u); return hx * (PACE / 2u) + ((RAMP * m * (33u - m)) >> 5); };
  auto pace = [&](unsigned t) { while (!due(t)) __builtin_amdgcn_s_sleep(2); };
  asm volatile("" :: "s"(out), "s"(ws), "s"(wo));

  const __amdgpu_buffer_rsrc_t srdK = __builtin_amdgcn_make_buffer_rsrc((void*)(kc + (size_t)(b * NH + h) * SK * DH), (short)0, SK * DH * 2, 0x00020000);
  const __amdgpu_buffer_rsrc_t srdV = __builtin_amdgcn_make_buffer_rsrc((void*)(vc + (size_t)(b * NH + h) * SK * DH), (short)0, SK * DH * 2, 0x00020000);
  const __amdgpu_buffer_rsrc_t srdX = __builtin_amdgcn_make_buffer_rsrc((void*)(x + (size_t)b * SQ * DM), (short)0, SQ * DM * 2, 0x00020000);
  const __amdgpu_buffer_rsrc_t srdWq = __builtin_amdgcn_make_buffer_rsrc((void*)wq, (short)0, DM * NH * DH * 2, 0x00020000);
  const __amdgpu_buffer_rsrc_t srdWo = __builtin_amdgcn_make_buffer_rsrc((void*)(wo + (size_t)hg * 512 * DM), (short)0, 512 * DM * 2, 0x00020000);

  unsigned w8k = w4 * 8192u; asm volatile("" : "+s"(w8k));
  const unsigned l4 = lane >> 4, l15 = lane & 15u;
  const unsigned voffK = w4 * 4096u + l4 * 256u + ((l15 ^ (l4 | (w4 << 2))) << 4);
  const unsigned voffV = w4 * 4096u + l4 * 256u + ((l15 ^ (l4 << 2)) << 4);
  const unsigned voffWq = l4 * 8192u + ((l15 ^ (l4 << 2)) << 4);
  const unsigned voffWo = l4 * 1024u + ((l15 ^ (l4 << 2)) << 4);
  const unsigned voffX = (lane >> 3) * 1024u + (((lane & 7u) ^ (((lane >> 4) & 1u) | (w4 << 1))) << 4);

  const unsigned trot = bid * 13u + (bid >> 5);
  auto issueK = [&](unsigned t, unsigned off) { dma4_kv(lds0 + off + w4 * 4096u, voffK, srdK, ((t + trot) & 31u) * U); };
  auto issueV = [&](unsigned t, unsigned off) { dma4_kv(lds0 + off + w4 * 4096u, voffV, srdV, ((t + trot) & 31u) * U); };
  auto hpos = [&](unsigned cc) { const unsigned r = cc - 1u + b % 3u; return rfl(cc == 0u ? j : (j + 1u + ((0x124u >> (2u * r)) & 3u)) & 3u); };
  auto issueWo = [&](unsigned cc, unsigned slot) {
    const unsigned c = hpos(cc);
#pragma unroll
    for (int q = 0; q < 8; ++q) dma1(lds0 + slot * SL + w8k + q * 1024u, voffWo, srdWo, (c * 128u + w4 * 32u + q * 4u) * 1024u + j * 256u);
  };
  const unsigned rot = (b + 2u * j) & 7u;
  auto qslot = [](int c) { return (c % 3 == 0) ? 3u : (c % 3 == 1) ? 4u : 2u; };
  auto issueQP = [&](int c) {
    const unsigned kcn = ((unsigned)c + rot) & 7u, sb = lds0 + qslot(c) * SL;
#pragma unroll
    for (int q = 0; q < 4; ++q) {
      dma1nt(sb + (w4 * 4u + q) * 1024u, voffWq, srdWq, (kcn * 64u + w4 * 16u + q * 4u) * 8192u + h * 256u);
      dma1(sb + U + (q * 4u + w4) * 1024u, voffX, srdX, (q * 4u + w4) * 8192u + kcn * 128u);
    }
  };
  const unsigned blk = (lane >> 4) & 1u, q4 = l15 >> 2, p4 = lane & 3u, pp = ((p4 & 1u) << 1) | (p4 >> 1);
  const unsigned trV = lds0 + U + (4u * hi + q4) * 256u + ((q4 ^ (2u * g)) << 6) + blk * 32u + pp * 8u;
  const unsigned rdK = lds0 + ((r32 & 7u) + 16u * (r32 >> 3)) * 256u + ((hi ^ ((r32 & 3u) | ((r32 >> 3) << 2))) << 4);
  const unsigned qx = lds0 + 3u * SL + w4 * 4096u + lane * 16u;

  bf16x8 qf[8], kf[16];
  auto rdk = [&](unsigned off, int i) { return lds_b128(((rdK + off) ^ (unsigned)((i & 7) << 5)) + (i >> 3) * 2048); };
  if (g == 0) {
    const unsigned trW = lds0 + (8u * hi + q4) * 256u + (q4 << 6) + blk * 32u + pp * 8u;
    const unsigned rdX = lds0 + U + (w4 * 32u + r32) * 128u + ((hi ^ (((r32 >> 1) & 1u) | ((r32 >> 3) << 1))) << 4);
    f32x16 qa[4] = {};
    bf16x8 xb[2][4], wa_[2][16];
    auto rdq = [&](int c, int s) {
      const unsigned u0 = qslot(c) * SL;
      xb[c & 1][s] = lds_b128((rdX + u0) ^ (unsigned)(s << 5));
#pragma unroll
      for (int d = 0; d < 4; ++d) { const unsigned a = ((trW + u0) ^ (unsigned)(d << 6)) + s * 4096; wa_[c & 1][4 * s + d] = lds_tr2(a, a + 1024); }
    };
    {
      unsigned long long pc; asm volatile("s_getpc_b64 %0" : "=s"(pc));
      const __amdgpu_buffer_rsrc_t srdC = __builtin_amdgcn_make_buffer_rsrc((void*)(pc & ~255ull), (short)0, CODEPF * 1024, 0x00020000);
      if (wid == 1) dma1(lds0, lane * 16u, srdC, ((bid >> 3) % CODEPF) * 1024u);
    }
    unsigned r0;
    { const Hs hs = hs_make(ws, bid, lane);
      if (wid == 0 && hs.act) __hip_atomic_store(hs.base + hs.mine, 0u, RLX_AGENT);
      asm volatile("global_load_dword %0, %1, off offset:256 sc1" : "=v"(r0) : "v"(hs.base + hs.mine) : "memory"); }
    BAR();
#pragma unroll
    for (int s = 0; s < 4; ++s) rdq(0, s);
#pragma unroll
    for (int c = 0; c < 8; ++c) {
      if (c == 0) {
        asm volatile("s_waitcnt vmcnt(0) lgkmcnt(0)\n\ts_barrier" : "+v"(r0) :: "memory");
        const Hs hs = hs_make(ws, bid, lane);
        if (wid == 0 && hs.act) __hip_atomic_store(hs.base + hs.peer + 32u, r0 + 1u, RLX_AGENT);
      } else if (c < 7) BAR();
#pragma unroll
      for (int s = 0; s < 4; ++s)
#pragma unroll
        for (int d = 0; d < 4; ++d) {
          SBAR();
          qa[d] = MFMA(wa_[c & 1][4 * s + d], xb[c & 1][s], qa[d]);
          SBAR();
          if (d & 1) { asm volatile("s_nop 2"); if (d == 3) __builtin_amdgcn_s_sleep(1); SBAR(); }
          if (c < 7) {
            const unsigned u0 = qslot(c + 1) * SL, a = ((trW + u0) ^ (unsigned)(d << 6)) + s * 4096;
            if (d == 0) xb[(c + 1) & 1][s] = lds_b128((rdX + u0) ^ (unsigned)(s << 5));
            wa_[(c + 1) & 1][4 * s + d] = lds_tr2(a, a + 1024);
          }
        }
      SBAR();
    }
#pragma unroll
    for (int d = 0; d < 4; ++d) {
#pragma unroll
      for (int r = 0; r < 16; ++r) qa[d][r] *= CSC;
      qf[2 * d] = pack8(qa[d], 0); qf[2 * d + 1] = pack8(qa[d], 1);
    }
#pragma unroll
    for (int f = 0; f < 8; ++f) *(LAS bf16x8*)(qx + (f >> 2) * U + (f & 3) * 1024) = qf[f];
    BAR();
#pragma unroll
    for (int i = 0; i < 16; ++i) kf[i] = rdk(U, i);
  } else {
    issueQP(0); issueQP(1);
    WAIT_BAR(8); issueQP(2);
#pragma unroll
    for (int c = 0; c < 5; ++c) { WAIT_BAR(8); issueQP(c + 3); }
    issueK(0, U);
    WAIT_BAR(12);
    issueK(1, 0);
    WAIT_BAR(8);
    pace(tat(1)); issueK(2, SL); pace(tat(2)); issueV(0, SL + U);
    WAIT_BAR(8);
    const Hs hs = hs_make(ws, bid, lane);
    unsigned rq;
    asm volatile("global_load_dword %0, %1, off offset:128 sc1" : "=v"(rq) : "v"(hs.base + hs.mine) : "memory");
#pragma unroll
    for (int f = 0; f < 8; ++f) qf[f] = lds_b128(qx + (f >> 2) * U + (f & 3) * 1024);
#pragma unroll
    for (int i = 0; i < 16; ++i) kf[i] = rdk(0, i);
    pace(tat(3)); issueK(3, 2u * SL); pace(tat(4)); issueV(1, 2u * SL + U);
    pace(tat(5));
    issueK(4, 3u * SL);
    pace(tat(6));
    issueV(2, 3u * SL + U);
    asm volatile("s_waitcnt vmcnt(16) lgkmcnt(0)\n\ts_barrier" : "+v"(rq) :: "memory");
    if (wid == 4 && hs.act) __hip_atomic_store(hs.base + hs.peer + 64u, rq, RLX_AGENT);
  }
  STAMP(1);

  f32x16 o[4] = {};
  float m_run = 0.f, l_run = 0.f;
  f32x16 negm = {};
  bf16x8 pf[4], vf[16];
  auto vfrag = [&](unsigned av, int ks, int d) { const unsigned a = (av ^ (unsigned)(d << 6)) + (32 * (ks & 1) + 8 * (ks >> 1)) * 256; return lds_tr2(a, a + 4096); };
  bool vpend = false; unsigned vdst = 0u, vsrc = 0u;
  auto scores_slow = [&](bool first, float& ps) {
    f32x16 s0 = {}, s1 = {};
#pragma unroll
    for (int s = 0; s < 8; ++s) { s0 = MFMA(kf[s], qf[s], s0); s1 = MFMA(kf[8 + s], qf[s], s1); }
    float mx = fmaxf(s0[0], s1[0]);
#pragma unroll
    for (int r = 1; r < 16; ++r) mx = fmaxf(fmaxf(mx, s0[r]), s1[r]);
    mx = xhalf_max(mx);
    const float m_new = first ? mx : fmaxf(mx, m_run);
    if (!first) {
      const float alpha = __builtin_amdgcn_exp2f(m_run - m_new);
      l_run *= alpha;
#pragma unroll
      for (int d = 0; d < 4; ++d)
#pragma unroll
        for (int r = 0; r < 16; ++r) o[d][r] *= alpha;
    }
    m_run = m_new;
    ps = 0.f;
#pragma unroll
    for (int r = 0; r < 16; ++r) { negm[r] = -m_new; s0[r] = __builtin_amdgcn_exp2f(s0[r] - m_new); s1[r] = __builtin_amdgcn_exp2f(s1[r] - m_new); ps += s0[r] + s1[r]; }
    pf[0] = pack8(s0, 0); pf[1] = pack8(s0, 1); pf[2] = pack8(s1, 0); pf[3] = pack8(s1, 1);
  };
  auto scores = [&](bool first, auto inloop) {
    float ps = 0.f;
    PRIO(2);
    if (!first) {
      SBAR();
      f32x16 s0 = MFMA(kf[0], qf[0], negm);
      SBAR();
#pragma unroll
      for (int s = 1; s < 8; ++s) {
        s0 = MFMA(kf[s], qf[s], s0); SBAR();
        if (decltype(inloop)::value && (s & 1)) { PIN(s0); if (vpend) dma1nt(vdst + (s >> 1) * 1024u, voffV, srdV, vsrc + (s >> 1) * 1024u); SBAR(); }
      }
      SBAR();
      f32x16 s1;
      asm volatile("v_mfma_f32_32x32x16_bf16 %0, %1, %2, %3" : "=&v"(s1) : "v"(kf[8]), "v"(qf[0]), "v"(negm));
      SBAR();
      float pa = 0.f, pb = 0.f;
      unsigned w[16];
#define EXP2(S, e) do { S[e] = __builtin_amdgcn_exp2f(S[e]); S[e + 1] = __builtin_amdgcn_exp2f(S[e + 1]); float t0_ = S[e], t1_ = S[e + 1]; PIN(t0_); PIN(t1_); S[e] = t0_; S[e + 1] = t1_; SBAR(); } while (0)
#define FIN2(S, e, wi) do { pa += S[e]; pb += S[e + 1]; w[wi] = cvtpk(S[e], S[e + 1]); PIN(pa); PIN(pb); PIN(w[wi]); SBAR(); } while (0)
#pragma unroll
      for (int s = 1; s < 8; ++s) {
        s1 = MFMA(kf[8 + s], qf[s], s1); SBAR();
        EXP2(s0, 2 * (s - 1));
        if (s > 1) FIN2(s0, 2 * (s - 2), s - 2);
      }
      EXP2(s0, 14); FIN2(s0, 12, 6);
      EXP2(s1, 0); FIN2(s0, 14, 7);
#pragma unroll
      for (int i = 1; i < 8; ++i) { EXP2(s1, 2 * i); FIN2(s1, 2 * (i - 1), 7 + i); }
      FIN2(s1, 14, 15);
#undef EXP2
#undef FIN2
      ps = pa + pb;
#pragma unroll
      for (int i = 0; i < 4; ++i) pf[i] = __builtin_bit_cast(bf16x8, (u32x4){w[4 * i], w[4 * i + 1], w[4 * i + 2], w[4 * i + 3]});
    }
    if (__builtin_expect(first || __any(!(ps <= PSLIM)), 0)) scores_slow(first, ps);
    l_run += ps;
    PIN(pf[0]); PIN(pf[1]); PIN(pf[2]); PIN(pf[3]); PIN(l_run); if (decltype(inloop)::value) PIN(negm);
    PRIO(0);
  };

  __builtin_amdgcn_s_waitcnt(0xC07F);
  unsigned sp = 1u + g;
  const unsigned p0 = 3u + ((((bid >> 3) * 11u) & 31u) * 3u >> 2);
  for (int it = 0; it < 15; ++it) {
    const unsigned p = 2u * (unsigned)it + g + 1u;
    scores(it == 0, std::true_type{});
    unsigned long long ts;
    asm volatile("s_waitcnt vmcnt(0) lgkmcnt(0)\n\ts_barrier\n\ts_memrealtime %0" : "=s"(ts) :: "memory");
    const unsigned av = trV + sp * SL;
#pragma unroll
    for (int i = 0; i < 16; ++i) vf[i] = vfrag(av, i >> 2, i & 3);
    SBAR();
    SBAR();
#pragma unroll
    for (int i = 0; i < 8; ++i) kf[i] = rdk(sp * SL, i);
    SBAR();
    const unsigned sn = sp >= 3u ? sp - 3u : sp + 2u;
    const bool go = p != p0;
    const unsigned ni = p + (p < p0 ? 3u : 2u), ns = (p < p0 ? (sn == 4u ? 0u : sn + 1u) : sn) * SL;
    const unsigned tni = tat(ni + min(ni, 30u));
    bool kgo = go && ni <= 30u;
    SBAR();
#pragma unroll
    for (int i = 0; i < 2; ++i) { o[i & 3] = MFMA(vf[i], pf[i >> 2], o[i & 3]); SBAR(); }
    asm volatile("s_waitcnt lgkmcnt(0)" : "+s"(ts) :: "memory");
    const int late = (int)((unsigned)ts - tref - tni);
    const bool vend = ni >= 31u;
    if (kgo && late + (int)(PACE / 2u) + 15 >= 0) { issueK(ni + 1u, ns); kgo = false; }
    if (vend) issueV(ni - 1u, ns + U);
    SBAR();
#pragma unroll
    for (int i = 2; i < 8; ++i) { o[i & 3] = MFMA(vf[i], pf[i >> 2], o[i & 3]); SBAR(); }
#pragma unroll
    for (int i = 8; i < 16; ++i) { o[i & 3] = MFMA(vf[i], pf[i >> 2], o[i & 3]); if (i < 12) { kf[2 * i - 8] = rdk(sp * SL, 2 * i - 8); kf[2 * i - 7] = rdk(sp * SL, 2 * i - 7); } SBAR(); }
    if (kgo) { pace(tni - PACE / 2u); issueK(ni + 1u, ns); }
    if (go && !vend && late + 35 < 0) pace(tni);
    vpend = go && !vend; vdst = lds0 + ns + U + w4 * 4096u; vsrc = ((ni - 1u + trot) & 31u) * U;
    if (it == 14) issueWo(g, 3u + g);
    sp = sn;
    __builtin_amdgcn_s_waitcnt(0xC07F);
    asm volatile("s_barrier" ::: "memory");
  }
  scores(false, std::false_type{});
  unsigned ln; asm volatile("v_mbcnt_lo_u32_b32 %0, -1, 0\n\tv_mbcnt_hi_u32_b32 %0, -1, %0" : "=v"(ln));
  const unsigned x0 = lds0 + w8k + ln * 16u;
  l_run = xhalf_sum(l_run);
  const unsigned stats = lds0 + 2u * SL + (ln & 31u) * 8u;
  if (ln < 32u) *(LAS f32x2*)(stats + wid * 256u) = (f32x2){m_run, l_run};
  const Hs hs = hs_make(ws, bid, ln);
  unsigned hs_r, hs_q, hs_qi, hs_ro;
  asm volatile("global_load_dword %0, %4, off offset:256 sc1\n\tglobal_load_dword %1, %5, off offset:128 sc1\n\t"
               "global_load_dword %2, %4, off offset:128 sc1\n\tglobal_load_dword %3, %5, off offset:256 sc1"
               : "=&v"(hs_r), "=&v"(hs_q), "=&v"(hs_qi), "=&v"(hs_ro) : "v"(hs.base + hs.mine), "v"(hs.base + hs.peer) : "memory");
  bf16x8 of0[4];
  {
    WAIT_BAR(12);
    STAMP(2);
    const unsigned av = trV + (1u + g) * SL;
#pragma unroll
    for (int ks = 0; ks < 4; ++ks) { vf[4 * ks + 2] = vfrag(av, ks, 2); vf[4 * ks + 3] = vfrag(av, ks, 3); }
    SBAR();
#pragma unroll
    for (int ks = 0; ks < 4; ++ks) { vf[4 * ks] = vfrag(av, ks, 0); vf[4 * ks + 1] = vfrag(av, ks, 1); }
    SBAR();
#pragma unroll
    for (int ks = 0; ks < 4; ++ks) { o[2] = MFMA(vf[4 * ks + 2], pf[ks], o[2]); o[3] = MFMA(vf[4 * ks + 3], pf[ks], o[3]); }
    SBAR();
#pragma unroll
    for (int ks = 0; ks < 2; ++ks) { o[0] = MFMA(vf[4 * ks], pf[ks], o[0]); o[1] = MFMA(vf[4 * ks + 1], pf[ks], o[1]); }
    SBAR();
    const unsigned xo = x0 + g * SL, xi = x0 + (1u - g) * SL;
#pragma unroll
    for (int q = 0; q < 4; ++q) { lds_st4(xo + q * 1024, o[2], q); lds_st4(xo + 4096 + q * 1024, o[3], q); }
    SBAR();
#pragma unroll
    for (int ks = 2; ks < 4; ++ks) { o[0] = MFMA(vf[4 * ks], pf[ks], o[0]); o[1] = MFMA(vf[4 * ks + 1], pf[ks], o[1]); }
    SBAR();
    if (g == 0) BAR();
    const f32x2 st = *(const LAS f32x2*)(stats + (wid ^ 4u) * 256u);
    const float ms = fmaxf(m_run, st[0]);
    const float e_me = __builtin_amdgcn_exp2f(m_run - ms), e_ot = __builtin_amdgcn_exp2f(st[0] - ms);
    const float inv = 1.f / (l_run * e_me + st[1] * e_ot);
    float a_me = e_me * inv, a_ot = e_ot * inv;
    PIN(a_me); PIN(a_ot);
    asm volatile("s_waitcnt vmcnt(0) lgkmcnt(0)\n\ts_barrier" : "+v"(hs_r), "+v"(hs_q), "+v"(hs_qi), "+v"(hs_ro) :: "memory");
    f32x4 w0[4], w1[4];
#pragma unroll
    for (int q = 0; q < 4; ++q) { w0[q] = lds_f4(xi + q * 1024); w1[q] = lds_f4(xi + 4096 + q * 1024); }
#pragma unroll
    for (int q = 0; q < 4; ++q)
#pragma unroll
      for (int e = 0; e < 4; ++e) { o[0][4 * q + e] = fmaf(w0[q][e], a_ot, o[0][4 * q + e] * a_me); o[1][4 * q + e] = fmaf(w1[q][e], a_ot, o[1][4 * q + e] * a_me); }
    of0[0] = pack8(o[0], 0); of0[1] = pack8(o[0], 1); of0[2] = pack8(o[1], 0); of0[3] = pack8(o[1], 1);
  }
  PRIO(0);
  const __amdgpu_buffer_rsrc_t srdO = __builtin_amdgcn_make_buffer_rsrc((void*)(ws + WS_O + (size_t)(hg * 8u + b) * 131072u), (short)0, 4 * 32768, 0x00020000);
#pragma unroll
  for (int s = 0; s < 4; ++s) __builtin_amdgcn_raw_buffer_store_b128(__builtin_bit_cast(u32x4, of0[s]), srdO, j * 32768u + wid * 4096u + (unsigned)s * 1024u + ln * 16u, 0, 16);
  STAMP(3);
  f32x16 c[4] = {};
  const unsigned trW2 = trV - U + (ln >> 5) * 1024u + g * U;
  auto wfrag = [&](unsigned slot, int s, int nb) { const unsigned a = ((trW2 + slot * SL) ^ (unsigned)(nb << 6)) + s * 4096; return lds_tr2(a, a + 1024); };
  bf16x8 w1[16], w2[16];
#pragma unroll
  for (int i = 0; i < 16; ++i) w2[i] = wfrag(3, i >> 2, i & 3);
  SBAR();
#pragma unroll
  for (int i = 0; i < 16; ++i) { c[i & 3] = MFMA(w2[i], of0[i >> 2], c[i & 3]); w1[i] = wfrag(4, i >> 2, i & 3); SBAR(); }
  PIN(c[0]); PIN(c[1]); PIN(c[2]); PIN(c[3]);
  WAIT_BAR(0);
  STAMP(4);
  if (wid == 0) {
    if (hs.act && hs_qi != hs_ro) __hip_atomic_store(hs.base + hs.peer + 64u, hs_qi, RLX_AGENT);
    if (!__all(hs_r == hs_q || !hs.act)) hs_wait_armed(hs);
    if (ln < 4u) __hip_atomic_store(hs.base + hs.peer, 1u, RLX_AGENT);
  }
  issueWo(2u + g, 1u - g);
  if (wid == 0) hs_wait_flags(hs, ln < 4u);
  WAIT_BAR(0);
  STAMP(5);
  const unsigned xch = lds0 + 4u * SL;
  const __amdgpu_buffer_rsrc_t srdP = __builtin_amdgcn_make_buffer_rsrc((void*)(ws + WS_P + (size_t)(b * 4u + j) * 262144u), (short)0, 262144, 0x00020000);
  {
    bf16x8 of[12];
#pragma unroll
    for (int i = 0; i < 12; ++i)
      of[i] = __builtin_bit_cast(bf16x8, __builtin_amdgcn_raw_buffer_load_b128(srdO, wid * 4096u + (unsigned)(i & 3) * 1024u + ln * 16u, hpos(1u + (unsigned)(i >> 2)) * 32768u, 16));
    SBAR();
#pragma unroll
    for (int i = 0; i < 8; ++i) w2[i] = wfrag(1, i >> 2, i & 3);
    SBAR();
#pragma unroll
    for (int i = 0; i < 16; ++i) { c[i & 3] = MFMA(w1[i], of[i >> 2], c[i & 3]); if (i >= 8) w2[i] = wfrag(1, i >> 2, i & 3); SBAR(); }
#pragma unroll
    for (int i = 0; i < 16; ++i) { c[i & 3] = MFMA(w2[i], of[4 + (i >> 2)], c[i & 3]); w1[i] = wfrag(0, (i >> 1) & 3, (i & 1) + (i < 8 ? 2 : 0)); SBAR(); }
#pragma unroll
    for (int i = 0; i < 8; ++i) c[2 + (i & 1)] = MFMA(w1[i], of[8 + (i >> 1)], c[2 + (i & 1)]);
    SBAR();
#pragma unroll
    for (int i = 0; i < 4; ++i) c[0] = MFMA(w1[8 + 2 * i], of[8 + i], c[0]);
    SBAR();
#pragma unroll
    for (int q = 0; q < 4; ++q) { lds_st4(x0 + (2u + g) * SL + q * 1024, c[2], q); lds_st4(x0 + (2u + g) * SL + 4096 + q * 1024, c[3], q); }
    SBAR();
#pragma unroll
    for (int i = 0; i < 4; ++i) c[1] = MFMA(w1[9 + 2 * i], of[8 + i], c[1]);
    PIN(c[0]); PIN(c[1]);
    BAR();
    STAMP(6);
    const unsigned ra = x0 + (3u - g) * SL;
    f32x4 va[8];
#pragma unroll
    for (int q = 0; q < 8; ++q) va[q] = lds_f4(ra + q * 1024);
    SBAR();
    auto put = [&](int nb) {
      float mx = 0.f;
#pragma unroll
      for (int r = 0; r < 16; ++r) { c[nb][r] += va[4 * nb + (r >> 2)][r & 3]; mx = fmaxf(mx, fabsf(c[nb][r])); }
      const unsigned e = max(wave_umax(__float_as_uint(mx)) >> 23, 15u);
      const float sc = __uint_as_float((268u - e) << 23);
#pragma unroll
      for (int s = 0; s < 2; ++s) {
        u32x4 w;
#pragma unroll
        for (int q = 0; q < 4; ++q) { f32x2 v = {c[nb][8 * s + 2 * q] * sc, c[nb][8 * s + 2 * q + 1] * sc}; w[q] = __builtin_bit_cast(unsigned, __builtin_convertvector(v, f16x2)); }
        __builtin_amdgcn_raw_buffer_store_b128(w, srdP, hg * 32768u + wid * 4096u + (unsigned)(2 * nb + s) * 1024u + ln * 16u, 0, 16);
      }
      if (ln == 0) *(LAS unsigned char*)(xch + wid * 4u + (unsigned)nb) = (unsigned char)e;
    };
    put(0);
    put(1);
  }
  STAMP(7);
  WAIT_BAR(0);
  STAMP(8);
  if (wid == 0) {
    const bool sel = hs.act && ln >= 8u;
    if (sel) __hip_atomic_store(hs.base + hs.peer, (*(const LAS unsigned*)(xch + (ln & 7u) * 4u) & 0xFFFFu) | 0x10000u, RLX_AGENT);
    const unsigned fv = hs_wait_flags(hs, sel);
    if (sel) *(LAS unsigned*)(xch + 64u + (ln & 7u) * 4u) = fv;
  }
  BAR();
  STAMP(9);
  if (g == 0) {
    u32x4 pv[8];
#pragma unroll
    for (int u = 0; u < 8; ++u) pv[u] = __builtin_amdgcn_raw_buffer_load_b128(srdP, hg * 4096u + w4 * 1024u + ln * 16u, (unsigned)u * 32768u, 18);
    const u32x4 f0 = *(const LAS u32x4*)(xch + 64u), f1 = *(const LAS u32x4*)(xch + 80u);
    float acc[8] = {};
#pragma unroll
    for (int u = 0; u < 8; ++u) {
      const unsigned e = ((u < 4 ? f0[u & 3] : f1[u & 3]) >> (8u * (w4 >> 1))) & 0xFFu;
      const float isc = __uint_as_float((e - 14u) << 23);
#pragma unroll
      for (int q = 0; q < 4; ++q) { const unsigned wv = pv[u][q]; const f16x2 hv = __builtin_bit_cast(f16x2, wv); acc[2 * q] = fmaf((float)hv[0], isc, acc[2 * q]); acc[2 * q + 1] = fmaf((float)hv[1], isc, acc[2 * q + 1]); }
    }
    const unsigned qrow = (hg & 3u) * 32u + (ln & 31u), n = j * 128u + ((hg >> 2) * 2u + (w4 >> 1)) * 32u + (w4 & 1u) * 16u + (ln >> 5) * 8u;
    *(u32x4*)(out + ((size_t)b * SQ + qrow) * DM + n) = (u32x4){cvtpk(acc[0], acc[1]), cvtpk(acc[2], acc[3]), cvtpk(acc[4], acc[5]), cvtpk(acc[6], acc[7])};
  }
  STAMP(10);
}

extern "C" void kernel_launch(void* const* d_in, const int* in_sizes, int n_in, void* d_out, int out_size, void* d_ws, size_t ws_size, hipStream_t stream) {
  (void)in_sizes; (void)n_in; (void)out_size; (void)ws_size;
  (void)hipFuncSetAttribute((const void*)mha_fused, hipFuncAttributeMaxDynamicSharedMemorySize, (int)LDS_BYTES);
  hipLaunchKernelGGL(mha_fused, dim3(256), dim3(512), LDS_BYTES, stream, (const unsigned short*)d_in[0], (const unsigned short*)d_in[1], (const unsigned short*)d_in[2],
                     (const unsigned short*)d_in[3], (const unsigned short*)d_in[4], (unsigned short*)d_out, (char*)d_ws);
}
```

```cpp
#include <hip/hip_runtime.h>
#include <hip/hip_bf16.h>
#include <stdint.h>
#include <type_traits>

typedef __bf16 bf16x8 __attribute__((ext_vector_type(8)));
typedef __bf16 bf16x2 __attribute__((ext_vector_type(2)));
typedef _Float16 f16x2 __attribute__((ext_vector_type(2)));
typedef short s16x4 __attribute__((ext_vector_type(4)));
typedef float f32x16 __attribute__((ext_vector_type(16)));
typedef float f32x4 __attribute__((ext_vector_type(4)));
typedef float f32x2 __attribute__((ext_vector_type(2)));
typedef unsigned u32x4 __attribute__((ext_vector_type(4)));
typedef unsigned u32x2 __attribute__((ext_vector_type(2)));
#define LAS __attribute__((address_space(3)))
#define GAS __attribute__((address_space(1)))
#define DI __device__ __forceinline__
#define MFMA(a, b, c) __builtin_amdgcn_mfma_f32_32x32x16_bf16((a), (b), (c), 0, 0, 0)
#define RLX_AGENT __ATOMIC_RELAXED, __HIP_MEMORY_SCOPE_AGENT
#define PIN(x) asm volatile("" : "+v"(x))
#define SBAR() __builtin_amdgcn_sched_barrier(0)
#define PRIO(n) __builtin_amdgcn_s_setprio(n)

constexpr int SQ = 128, DM = 512, NH = 32, SK = 2048, DH = 128;
constexpr unsigned U = 16384u, SL = 32768u;
constexpr unsigned LDS_BYTES = 5 * SL;
#define CODEPF 17
#define PACE 112u
#define PACE0 480u
#define PSLIM 8192.0f
constexpr float CSC = 0.088388347648318447f * 1.4426950408889634f;
constexpr size_t WS_FLAG = 0;
constexpr unsigned FB = 128;
constexpr size_t WS_O = 1 << 20;
constexpr size_t WS_P = WS_O + (8u << 20);

DI unsigned rfl(unsigned x) { return __builtin_amdgcn_readfirstlane(x); }
DI unsigned cvtpk(float lo, float hi) { f32x2 v = {lo, hi}; bf16x2 b = __builtin_convertvector(v, bf16x2); return __builtin_bit_cast(unsigned, b); }
DI bf16x8 pack8(const f32x16& a, int s) {
  u32x4 w = {cvtpk(a[8 * s], a[8 * s + 1]), cvtpk(a[8 * s + 2], a[8 * s + 3]), cvtpk(a[8 * s + 4], a[8 * s + 5]), cvtpk(a[8 * s + 6], a[8 * s + 7])};
  return __builtin_bit_cast(bf16x8, w);
}
DI bf16x8 lds_b128(unsigned a) { return *(const LAS bf16x8*)a; }
DI f32x4 lds_f4(unsigned a) { return *(const LAS f32x4*)a; }
DI void lds_st4(unsigned a, const f32x16& v, int q) { *(LAS f32x4*)a = (f32x4){v[4 * q], v[4 * q + 1], v[4 * q + 2], v[4 * q + 3]}; }
DI s16x4 lds_tr(unsigned a) { return __builtin_amdgcn_ds_read_tr16_b64_v4i16((LAS s16x4*)a); }
DI bf16x8 lds_tr2(unsigned a0, unsigned a1) {
  s16x4 lo = lds_tr(a0), hi = lds_tr(a1);
  return __builtin_bit_cast(bf16x8, __builtin_shufflevector(lo, hi, 0, 1, 2, 3, 4, 5, 6, 7));
}
DI void dma4_kv(unsigned m0v, unsigned voff, __amdgpu_buffer_rsrc_t srd, unsigned soff) {
  asm volatile("s_mov_b32 m0, %0\n\ts_nop 3\n\t"
               "buffer_load_dwordx4 %1, %2, %3 offen nt lds\n\t"
               "buffer_load_dwordx4 %1, %2, %3 offen offset:1024 nt lds\n\t"
               "buffer_load_dwordx4 %1, %2, %3 offen offset:2048 nt lds\n\t"
               "buffer_load_dwordx4 %1, %2, %3 offen offset:3072 nt lds"
               :: "s"(rfl(m0v)), "v"(voff), "s"(srd), "s"(rfl(soff)) : "memory");
}
DI void dma1(unsigned m0v, unsigned voff, __amdgpu_buffer_rsrc_t srd, unsigned soff) {
  asm volatile("s_mov_b32 m0, %0\n\ts_nop 3\n\t"
               "buffer_load_dwordx4 %1, %2, %3 offen lds"
               :: "s"(rfl(m0v)), "v"(voff), "s"(srd), "s"(rfl(soff)) : "memory");
}
DI void dma1nt(unsigned m0v, unsigned voff, __amdgpu_buffer_rsrc_t srd, unsigned soff) {
  asm volatile("s_mov_b32 m0, %0\n\ts_nop 3\n\t"
               "buffer_load_dwordx4 %1, %2, %3 offen nt lds"
               :: "s"(rfl(m0v)), "v"(voff), "s"(srd), "s"(rfl(soff)) : "memory");
}
#define WAIT_BAR(N) asm volatile("s_waitcnt vmcnt(" #N ") lgkmcnt(0)\n\ts_barrier" ::: "memory")
#define BAR() asm volatile("s_waitcnt lgkmcnt(0)\n\ts_barrier" ::: "memory")
#define WAITVM(N) asm volatile("s_waitcnt vmcnt(" #N ")" ::: "memory")

DI float xhalf_max(float v) { auto r = __builtin_amdgcn_permlane32_swap(__float_as_uint(v), __float_as_uint(v), false, false); return fmaxf(__uint_as_float(r[0]), __uint_as_float(r[1])); }
DI float xhalf_sum(float v) { auto r = __builtin_amdgcn_permlane32_swap(__float_as_uint(v), __float_as_uint(v), false, false); return __uint_as_float(r[0]) + __uint_as_float(r[1]); }

DI unsigned wave_umax(unsigned v) {
#define DPPMAX(ctrl, rmask) v = max(v, (unsigned)__builtin_amdgcn_update_dpp(0, (int)v, ctrl, rmask, 0xF, false))
  DPPMAX(0x111, 0xF); DPPMAX(0x112, 0xF); DPPMAX(0x114, 0xF); DPPMAX(0x118, 0xF);
  DPPMAX(0x142, 0xA); DPPMAX(0x143, 0xC);
#undef DPPMAX
  return (unsigned)__builtin_amdgcn_readlane((int)v, 63);
}
struct Hs { GAS unsigned* base; unsigned mine, peer; bool act; };
DI Hs hs_make(char* ws, unsigned bid, unsigned L) {
  Hs h; h.base = (GAS unsigned*)(ws + WS_FLAG);
  const bool h1 = L < 4u; h.act = h1 || (L >= 8u && L < 16u);
  const unsigned pb = h1 ? ((bid & ~24u) | (L << 3)) : ((bid & ~7u) | (L & 7u));
  h.mine = bid * FB + (L & 15u); h.peer = pb * FB + (h1 ? ((bid >> 3) & 3u) : 8u + (bid & 7u));
  return h;
}
DI void hs_echo(const Hs& h) { if (h.act) __hip_atomic_store(h.base + h.peer + 64u, __hip_atomic_load(h.base + h.mine + 32u, RLX_AGENT), RLX_AGENT); }
DI void hs_wait_armed(const Hs& h) {
#pragma nounroll
  for (unsigned spins = 0; spins < (1u << 20); ++spins) {
    hs_echo(h);
    bool ok = true;
    if (h.act) ok = __hip_atomic_load(h.base + h.mine + 64u, RLX_AGENT) == __hip_atomic_load(h.base + h.peer + 32u, RLX_AGENT);
    if (__all(ok)) break;
    __builtin_amdgcn_s_sleep(4);
  }
}
DI unsigned hs_wait_flags(const Hs& h, bool sel) {
  unsigned v = 1u;
#pragma nounroll
  for (unsigned spins = 0; spins < (1u << 20); ++spins) {
    if (sel) v = __hip_atomic_load(h.base + h.mine, RLX_AGENT);
    if (__all(v != 0u)) break;
    if ((spins & 15u) == 15u) hs_echo(h);
    __builtin_amdgcn_s_sleep(1);
  }
  asm volatile("" ::: "memory");
  return v;
}

__global__ __launch_bounds__(512, 2) void mha_fused(const unsigned short* __restrict__ x, const unsigned short* __restrict__ kc, const unsigned short* __restrict__ vc,
                                                    const unsigned short* __restrict__ wq, const unsigned short* __restrict__ wo, unsigned short* __restrict__ out, char* __restrict__ ws) {
  extern __shared__ __attribute__((aligned(16))) char lds[];
  const unsigned tid = threadIdx.x, lane = tid & 63u, r32 = lane & 31u, hi = lane >> 5;
  const unsigned wid = rfl(tid >> 6), g = wid >> 2, w4 = wid & 3u;
  const unsigned bid = blockIdx.x, hg = bid & 7u, b = bid >> 5, j = (bid >> 3) & 3u, h = hg * 4u + j;
  const unsigned lds0 = (unsigned)(uintptr_t)lds;
  const unsigned tref = (unsigned)__builtin_amdgcn_s_memrealtime() + PACE0 + (((bid >> 3) * PACE) >> 5);
  auto due = [&](unsigned t) { unsigned long long t_; asm volatile("s_memrealtime %0\n\ts_waitcnt lgkmcnt(0)" : "=s"(t_) :: "memory"); return (int)((unsigned)t_ - tref - t) >= 0; };
  auto pace = [&](unsigned t) { while (!due(t)) __builtin_amdgcn_s_sleep(2); };
  asm volatile("" :: "s"(out), "s"(ws), "s"(wo));

  const __amdgpu_buffer_rsrc_t srdK = __builtin_amdgcn_make_buffer_rsrc((void*)(kc + (size_t)(b * NH + h) * SK * DH), (short)0, SK * DH * 2, 0x00020000);
  const __amdgpu_buffer_rsrc_t srdV = __builtin_amdgcn_make_buffer_rsrc((void*)(vc + (size_t)(b * NH + h) * SK * DH), (short)0, SK * DH * 2, 0x00020000);
  const __amdgpu_buffer_rsrc_t srdX = __builtin_amdgcn_make_buffer_rsrc((void*)(x + (size_t)b * SQ * DM), (short)0, SQ * DM * 2, 0x00020000);
  const __amdgpu_buffer_rsrc_t srdWq = __builtin_amdgcn_make_buffer_rsrc((void*)wq, (short)0, DM * NH * DH * 2, 0x00020000);
  const __amdgpu_buffer_rsrc_t srdWo = __builtin_amdgcn_make_buffer_rsrc((void*)(wo + (size_t)hg * 512 * DM), (short)0, 512 * DM * 2, 0x00020000);

  unsigned w8k = w4 * 8192u; asm volatile("" : "+s"(w8k));
  const unsigned l4 = lane >> 4, l15 = lane & 15u;
  const unsigned voffK = w4 * 4096u + l4 * 256u + ((l15 ^ (l4 | (w4 << 2))) << 4);
  const unsigned voffV = w4 * 4096u + l4 * 256u + ((l15 ^ (l4 << 2)) << 4);
  const unsigned voffWq = l4 * 8192u + ((l15 ^ (l4 << 2)) << 4);
  const unsigned voffWo = l4 * 1024u + ((l15 ^ (l4 << 2)) << 4);
  const unsigned voffX = (lane >> 3) * 1024u + (((lane & 7u) ^ (((lane >> 4) & 1u) | (w4 << 1))) << 4);

  auto issueK = [&](unsigned t, unsigned off) { dma4_kv(lds0 + off + w4 * 4096u, voffK, srdK, t * U); };
  auto issueV = [&](unsigned t, unsigned off) { dma4_kv(lds0 + off + w4 * 4096u, voffV, srdV, t * U); };
  auto hpos = [&](unsigned cc) { const unsigned r = cc - 1u + b % 3u; return rfl(cc == 0u ? j : (j + 1u + ((0x124u >> ((2u * r) & 15u)) & 3u)) & 3u); };
  auto issueWo = [&](unsigned cc, unsigned slot) {
    const unsigned c = hpos(cc);
#pragma unroll
    for (int q = 0; q < 8; ++q) dma1(lds0 + slot * SL + w8k + q * 1024u, voffWo, srdWo, (c * 128u + w4 * 32u + q * 4u) * 1024u + j * 256u);
  };
  const unsigned rot = (b + 2u * j) & 7u;
  auto qslot = [](int c) { return (c % 3 == 0) ? 3u : (c % 3 == 1) ? 4u : 2u; };
  auto issueQP = [&](int c) {
    const unsigned kcn = ((unsigned)c + rot) & 7u, sb = lds0 + qslot(c) * SL;
#pragma unroll
    for (int q = 0; q < 4; ++q) {
      dma1nt(sb + (w4 * 4u + q) * 1024u, voffWq, srdWq, (kcn * 64u + w4 * 16u + q * 4u) * 8192u + h * 256u);
      dma1(sb + U + (q * 4u + w4) * 1024u, voffX, srdX, (q * 4u + w4) * 8192u + kcn * 128u);
    }
  };
  const unsigned blk = (lane >> 4) & 1u, q4 = l15 >> 2, p4 = lane & 3u, pp = ((p4 & 1u) << 1) | (p4 >> 1);
  const unsigned trV = lds0 + U + (4u * hi + q4) * 256u + ((q4 ^ (2u * g)) << 6) + blk * 32u + pp * 8u;
  const unsigned rdK = lds0 + ((r32 & 7u) + 16u * (r32 >> 3)) * 256u + ((hi ^ ((r32 & 3u) | ((r32 >> 3) << 2))) << 4);
  const unsigned qx = lds0 + 3u * SL + w4 * 4096u + lane * 16u;

  bf16x8 qf[8], kf[16];
  auto rdk = [&](unsigned off, int i) { return lds_b128(((rdK + off) ^ (unsigned)((i & 7) << 5)) + (i >> 3) * 2048); };
  if (g == 0) {
    const unsigned trW = lds0 + (8u * hi + q4) * 256u + (q4 << 6) + blk * 32u + pp * 8u;
    const unsigned rdX = lds0 + U + (w4 * 32u + r32) * 128u + ((hi ^ (((r32 >> 1) & 1u) | ((r32 >> 3) << 1))) << 4);
    f32x16 qa[4] = {};
    bf16x8 xb[2][4], wa_[2][16];
    auto rdq = [&](int c, int s) {
      const unsigned u0 = qslot(c) * SL;
      xb[c & 1][s] = lds_b128((rdX + u0) ^ (unsigned)(s << 5));
#pragma unroll
      for (int d = 0; d < 4; ++d) { const unsigned a = ((trW + u0) ^ (unsigned)(d << 6)) + s * 4096; wa_[c & 1][4 * s + d] = lds_tr2(a, a + 1024); }
    };
    {
      unsigned long long pc; asm volatile("s_getpc_b64 %0" : "=s"(pc));
      const __amdgpu_buffer_rsrc_t srdC = __builtin_amdgcn_make_buffer_rsrc((void*)(pc & ~255ull), (short)0, CODEPF * 1024, 0x00020000);
      if (wid == 1) dma1(lds0, lane * 16u, srdC, ((bid >> 3) % CODEPF) * 1024u);
    }
    unsigned r0;
    { const Hs hs = hs_make(ws, bid, lane);
      if (wid == 0 && hs.act) __hip_atomic_store(hs.base + hs.mine, 0u, RLX_AGENT);
      asm volatile("global_load_dword %0, %1, off offset:256 sc1" : "=v"(r0) : "v"(hs.base + hs.mine) : "memory"); }
    BAR();
#pragma unroll
    for (int s = 0; s < 4; ++s) rdq(0, s);
#pragma unroll
    for (int c = 0; c < 8; ++c) {
      if (c == 0) {
        asm volatile("s_waitcnt vmcnt(0) lgkmcnt(0)\n\ts_barrier" : "+v"(r0) :: "memory");
        const Hs hs = hs_make(ws, bid, lane);
        if (wid == 0 && hs.act) __hip_atomic_store(hs.base + hs.peer + 32u, r0 + 1u, RLX_AGENT);
      } else if (c < 7) BAR();
#pragma unroll
      for (int s = 0; s < 4; ++s)
#pragma unroll
        for (int d = 0; d < 4; ++d) {
          SBAR();
          qa[d] = MFMA(wa_[c & 1][4 * s + d], xb[c & 1][s], qa[d]);
          SBAR();
          if (c < 7) {
            const unsigned u0 = qslot(c + 1) * SL, a = ((trW + u0) ^ (unsigned)(d << 6)) + s * 4096;
            if (d == 0) xb[(c + 1) & 1][s] = lds_b128((rdX + u0) ^ (unsigned)(s << 5));
            wa_[(c + 1) & 1][4 * s + d] = lds_tr2(a, a + 1024);
          }
        }
      SBAR();
    }
#pragma unroll
    for (int d = 0; d < 4; ++d) {
#pragma unroll
      for (int r = 0; r < 16; ++r) qa[d][r] *= CSC;
      qf[2 * d] = pack8(qa[d], 0); qf[2 * d + 1] = pack8(qa[d], 1);
    }
#pragma unroll
    for (int f = 0; f < 8; ++f) *(LAS bf16x8*)(qx + (f >> 2) * U + (f & 3) * 1024) = qf[f];
    BAR();
#pragma unroll
    for (int i = 0; i < 16; ++i) kf[i] = rdk(U, i);
  } else {
    issueQP(0); issueQP(1);
    WAIT_BAR(8); issueQP(2);
#pragma unroll
    for (int c = 0; c < 5; ++c) { WAIT_BAR(8); issueQP(c + 3); }
    issueK(0, U);
    WAIT_BAR(12);
    issueK(1, 0);
    WAIT_BAR(8);
    pace(PACE / 2u); issueK(2, SL); pace(PACE); issueV(0, SL + U);
    WAIT_BAR(8);
    const Hs hs = hs_make(ws, bid, lane);
    unsigned rq;
    asm volatile("global_load_dword %0, %1, off offset:128 sc1" : "=v"(rq) : "v"(hs.base + hs.mine) : "memory");
#pragma unroll
    for (int f = 0; f < 8; ++f) qf[f] = lds_b128(qx + (f >> 2) * U + (f & 3) * 1024);
#pragma unroll
    for (int i = 0; i < 16; ++i) kf[i] = rdk(0, i);
    pace(PACE + PACE / 2u); issueK(3, 2u * SL); pace(2u * PACE); issueV(1, 2u * SL + U);
    pace(2u * PACE + PACE / 2u);
    issueK(4, 3u * SL);
    pace(3u * PACE);
    issueV(2, 3u * SL + U);
    asm volatile("s_waitcnt vmcnt(16) lgkmcnt(0)\n\ts_barrier" : "+v"(rq) :: "memory");
    if (wid == 4 && hs.act) __hip_atomic_store(hs.base + hs.peer + 64u, rq, RLX_AGENT);
  }

  f32x16 o[4] = {};
  float m_run = 0.f, l_run = 0.f;
  f32x16 negm = {};
  bf16x8 pf[4], vf[16];
  auto vfrag = [&](unsigned av, int ks, int d) { const unsigned a = (av ^ (unsigned)(d << 6)) + (32 * (ks & 1) + 8 * (ks >> 1)) * 256; return lds_tr2(a, a + 4096); };
  bool vpend = false; unsigned vdst = 0u, vsrc = 0u;
  auto scores_slow = [&](bool first, float& ps) {
    f32x16 s0 = {}, s1 = {};
#pragma unroll
    for (int s = 0; s < 8; ++s) { s0 = MFMA(kf[s], qf[s], s0); s1 = MFMA(kf[8 + s], qf[s], s1); }
    float mx = fmaxf(s0[0], s1[0]);
#pragma unroll
    for (int r = 1; r < 16; ++r) mx = fmaxf(fmaxf(mx, s0[r]), s1[r]);
    mx = xhalf_max(mx);
    const float m_new = first ? mx : fmaxf(mx, m_run);
    if (!first) {
      const float alpha = __builtin_amdgcn_exp2f(m_run - m_new);
      l_run *= alpha;
#pragma unroll
      for (int d = 0; d < 4; ++d)
#pragma unroll
        for (int r = 0; r < 16; ++r) o[d][r] *= alpha;
    }
    m_run = m_new;
    ps = 0.f;
#pragma unroll
    for (int r = 0; r < 16; ++r) { negm[r] = -m_new; s0[r] = __builtin_amdgcn_exp2f(s0[r] - m_new); s1[r] = __builtin_amdgcn_exp2f(s1[r] - m_new); ps += s0[r] + s1[r]; }
    pf[0] = pack8(s0, 0); pf[1] = pack8(s0, 1); pf[2] = pack8(s1, 0); pf[3] = pack8(s1, 1);
  };
  auto scores = [&](bool first, auto inloop) {
    float ps = 0.f;
    PRIO(2);
    if (!first) {
      SBAR();
      f32x16 s0 = MFMA(kf[0], qf[0], negm);
      SBAR();
#pragma unroll
      for (int s = 1; s < 8; ++s) {
        s0 = MFMA(kf[s], qf[s], s0); SBAR();
        if (decltype(inloop)::value && (s & 1)) { PIN(s0); if (vpend) dma1nt(vdst + (s >> 1) * 1024u, voffV, srdV, vsrc + (s >> 1) * 1024u); SBAR(); }
      }
      SBAR();
      f32x16 s1;
      asm volatile("v_mfma_f32_32x32x16_bf16 %0, %1, %2, %3" : "=&v"(s1) : "v"(kf[8]), "v"(qf[0]), "v"(negm));
      SBAR();
      float pa = 0.f, pb = 0.f;
      unsigned w[16];
#define EXP2(S, e) do { S[e] = __builtin_amdgcn_exp2f(S[e]); S[e + 1] = __builtin_amdgcn_exp2f(S[e + 1]); float t0_ = S[e], t1_ = S[e + 1]; PIN(t0_); PIN(t1_); S[e] = t0_; S[e + 1] = t1_; SBAR(); } while (0)
#define FIN2(S, e, wi) do { pa += S[e]; pb += S[e + 1]; w[wi] = cvtpk(S[e], S[e + 1]); PIN(pa); PIN(pb); PIN(w[wi]); SBAR(); } while (0)
#pragma unroll
      for (int s = 1; s < 8; ++s) {
        s1 = MFMA(kf[8 + s], qf[s], s1); SBAR();
        EXP2(s0, 2 * (s - 1));
        if (s > 1) FIN2(s0, 2 * (s - 2), s - 2);
        if (s == 3) asm volatile("" :: "v"(negm));
      }
      EXP2(s0, 14); FIN2(s0, 12, 6);
      EXP2(s1, 0); FIN2(s0, 14, 7);
#pragma unroll
      for (int i = 1; i < 8; ++i) { EXP2(s1, 2 * i); FIN2(s1, 2 * (i - 1), 7 + i); }
      FIN2(s1, 14, 15);
#undef EXP2
#undef FIN2
      ps = pa + pb;
#pragma unroll
      for (int i = 0; i < 4; ++i) pf[i] = __builtin_bit_cast(bf16x8, (u32x4){w[4 * i], w[4 * i + 1], w[4 * i + 2], w[4 * i + 3]});
    }
    if (__builtin_expect(first || __any(!(ps <= PSLIM)), 0)) scores_slow(first, ps);
    l_run += ps;
    PIN(pf[0]); PIN(pf[1]); PIN(pf[2]); PIN(pf[3]); PIN(l_run); if (decltype(inloop)::value) PIN(negm);
    PRIO(0);
  };

  __builtin_amdgcn_s_waitcnt(0xC07F);
  unsigned sp = 1u + g;
  const unsigned p0 = 3u + ((((bid >> 3) * 11u) & 31u) * 3u >> 2);
  for (int it = 0; it < 15; ++it) {
    const unsigned p = 2u * (unsigned)it + g + 1u;
    scores(it == 0, std::true_type{});
    unsigned long long ts;
    asm volatile("s_waitcnt vmcnt(0) lgkmcnt(0)\n\ts_barrier\n\ts_memrealtime %0" : "=s"(ts) :: "memory");
    const unsigned av = trV + sp * SL;
#pragma unroll
    for (int i = 0; i < 16; ++i) vf[i] = vfrag(av, i >> 2, i & 3);
    SBAR();
    SBAR();
#pragma unroll
    for (int i = 0; i < 8; ++i) kf[i] = rdk(sp * SL, i);
    SBAR();
    const unsigned sn = sp >= 3u ? sp - 3u : sp + 2u;
    const bool go = p != p0;
    const unsigned ni = p + (p < p0 ? 3u : 2u), ns = (p < p0 ? (sn == 4u ? 0u : sn + 1u) : sn) * SL;
    const unsigned tni = (ni + min(ni, 30u)) * (PACE / 2u);
    bool kgo = go && ni <= 30u;
    SBAR();
#pragma unroll
    for (int i = 0; i < 2; ++i) { o[i & 3] = MFMA(vf[i], pf[i >> 2], o[i & 3]); SBAR(); }
    asm volatile("s_waitcnt lgkmcnt(0)" : "+s"(ts) :: "memory");
    const int late = (int)((unsigned)ts - tref - tni);
    const bool vend = ni >= 31u;
    if (kgo && late + (int)(PACE / 2u) + 15 >= 0) { issueK(ni + 1u, ns); kgo = false; }
    if (vend) issueV(ni - 1u, ns + U);
    SBAR();
#pragma unroll
    for (int i = 2; i < 8; ++i) { o[i & 3] = MFMA(vf[i], pf[i >> 2], o[i & 3]); SBAR(); }
#pragma unroll
    for (int i = 8; i < 16; ++i) { o[i & 3] = MFMA(vf[i], pf[i >> 2], o[i & 3]); if (i < 12) { kf[2 * i - 8] = rdk(sp * SL, 2 * i - 8); kf[2 * i - 7] = rdk(sp * SL, 2 * i - 7); } SBAR(); }
    if (kgo) { pace(tni - PACE / 2u); issueK(ni + 1u, ns); }
    if (go && !vend && late + 35 < 0) pace(tni);
    vpend = go && !vend; vdst = lds0 + ns + U + w4 * 4096u; vsrc = (ni - 1u) * U;
    if (it == 14) issueWo(g, 3u + g);
    sp = sn;
    __builtin_amdgcn_s_waitcnt(0xC07F);
    asm volatile("s_barrier" ::: "memory");
  }
  scores(false, std::false_type{});
  unsigned ln; asm volatile("v_mbcnt_lo_u32_b32 %0, -1, 0\n\tv_mbcnt_hi_u32_b32 %0, -1, %0" : "=v"(ln));
  const unsigned x0 = lds0 + w8k + ln * 16u;
  l_run = xhalf_sum(l_run);
  const unsigned stats = lds0 + 2u * SL + (ln & 31u) * 8u;
  if (ln < 32u) *(LAS f32x2*)(stats + wid * 256u) = (f32x2){m_run, l_run};
  const Hs hs = hs_make(ws, bid, ln);
  unsigned hs_r, hs_q, hs_qi, hs_ro;
  asm volatile("global_load_dword %0, %4, off offset:256 sc1\n\tglobal_load_dword %1, %5, off offset:128 sc1\n\t"
               "global_load_dword %2, %4, off offset:128 sc1\n\tglobal_load_dword %3, %5, off offset:256 sc1"
               : "=&v"(hs_r), "=&v"(hs_q), "=&v"(hs_qi), "=&v"(hs_ro) : "v"(hs.base + hs.mine), "v"(hs.base + hs.peer) : "memory");
  bf16x8 of0[4];
  {
    WAIT_BAR(12);
    const unsigned av = trV + (1u + g) * SL;
#pragma unroll
    for (int ks = 0; ks < 4; ++ks) { vf[4 * ks + 2] = vfrag(av, ks, 2); vf[4 * ks + 3] = vfrag(av, ks, 3); }
    SBAR();
#pragma unroll
    for (int ks = 0; ks < 4; ++ks) { vf[4 * ks] = vfrag(av, ks, 0); vf[4 * ks + 1] = vfrag(av, ks, 1); }
    SBAR();
#pragma unroll
    for (int ks = 0; ks < 4; ++ks) { o[2] = MFMA(vf[4 * ks + 2], pf[ks], o[2]); o[3] = MFMA(vf[4 * ks + 3], pf[ks], o[3]); }
    SBAR();
#pragma unroll
    for (int ks = 0; ks < 2; ++ks) { o[0] = MFMA(vf[4 * ks], pf[ks], o[0]); o[1] = MFMA(vf[4 * ks + 1], pf[ks], o[1]); }
    SBAR();
    const unsigned xo = x0 + g * SL, xi = x0 + (1u - g) * SL;
#pragma unroll
    for (int q = 0; q < 4; ++q) { lds_st4(xo + q * 1024, o[2], q); lds_st4(xo + 4096 + q * 1024, o[3], q); }
    SBAR();
#pragma unroll
    for (int ks = 2; ks < 4; ++ks) { o[0] = MFMA(vf[4 * ks], pf[ks], o[0]); o[1] = MFMA(vf[4 * ks + 1], pf[ks], o[1]); }
    SBAR();
    if (g == 0) BAR();
    const f32x2 st = *(const LAS f32x2*)(stats + (wid ^ 4u) * 256u);
    const float ms = fmaxf(m_run, st[0]);
    const float e_me = __builtin_amdgcn_exp2f(m_run - ms), e_ot = __builtin_amdgcn_exp2f(st[0] - ms);
    const float inv = 1.f / (l_run * e_me + st[1] * e_ot);
    float a_me = e_me * inv, a_ot = e_ot * inv;
    PIN(a_me); PIN(a_ot);
    asm volatile("s_waitcnt vmcnt(0) lgkmcnt(0)\n\ts_barrier" : "+v"(hs_r), "+v"(hs_q), "+v"(hs_qi), "+v"(hs_ro) :: "memory");
    f32x4 w0[4], w1[4];
#pragma unroll
    for (int q = 0; q < 4; ++q) { w0[q] = lds_f4(xi + q * 1024); w1[q] = lds_f4(xi + 4096 + q * 1024); }
#pragma unroll
    for (int q = 0; q < 4; ++q)
#pragma unroll
      for (int e = 0; e < 4; ++e) { o[0][4 * q + e] = fmaf(w0[q][e], a_ot, o[0][4 * q + e] * a_me); o[1][4 * q + e] = fmaf(w1[q][e], a_ot, o[1][4 * q + e] * a_me); }
    of0[0] = pack8(o[0], 0); of0[1] = pack8(o[0], 1); of0[2] = pack8(o[1], 0); of0[3] = pack8(o[1], 1);
  }
  PRIO(0);
  const __amdgpu_buffer_rsrc_t srdO = __builtin_amdgcn_make_buffer_rsrc((void*)(ws + WS_O + (size_t)(hg * 8u + b) * 131072u), (short)0, 4 * 32768, 0x00020000);
#pragma unroll
  for (int s = 0; s < 4; ++s) __builtin_amdgcn_raw_buffer_store_b128(__builtin_bit_cast(u32x4, of0[s]), srdO, j * 32768u + wid * 4096u + (unsigned)s * 1024u + ln * 16u, 0, 16);
  f32x16 c[4] = {};
  const unsigned trW2 = trV - U + (ln >> 5) * 1024u + g * U;
  auto wfrag = [&](unsigned slot, int s, int nb) { const unsigned a = ((trW2 + slot * SL) ^ (unsigned)(nb << 6)) + s * 4096; return lds_tr2(a, a + 1024); };
  bf16x8 w1[16], w2[16];
#pragma unroll
  for (int i = 0; i < 16; ++i) w2[i] = wfrag(3, i >> 2, i & 3);
  SBAR();
#pragma unroll
  for (int i = 0; i < 16; ++i) { c[i & 3] = MFMA(w2[i], of0[i >> 2], c[i & 3]); w1[i] = wfrag(4, i >> 2, i & 3); SBAR(); }
  PIN(c[0]); PIN(c[1]); PIN(c[2]); PIN(c[3]);
  WAIT_BAR(0);
  if (wid == 0) {
    if (hs.act && hs_qi != hs_ro) __hip_atomic_store(hs.base + hs.peer + 64u, hs_qi, RLX_AGENT);
    if (!__all(hs_r == hs_q || !hs.act)) hs_wait_armed(hs);
    if (ln < 4u) __hip_atomic_store(hs.base + hs.peer, 1u, RLX_AGENT);
  }
  issueWo(2u + g, 1u - g);
  if (wid == 0) hs_wait_flags(hs, ln < 4u);
  WAIT_BAR(0);
  const unsigned xch = lds0 + 4u * SL;
  const __amdgpu_buffer_rsrc_t srdP = __builtin_amdgcn_make_buffer_rsrc((void*)(ws + WS_P + (size_t)(b * 4u + j) * 262144u), (short)0, 262144, 0x00020000);
  {
    bf16x8 of[12];
#pragma unroll
    for (int i = 0; i < 12; ++i)
      of[i] = __builtin_bit_cast(bf16x8, __builtin_amdgcn_raw_buffer_load_b128(srdO, wid * 4096u + (unsigned)(i & 3) * 1024u + ln * 16u, hpos(1u + (unsigned)(i >> 2)) * 32768u, 16));
    SBAR();
#pragma unroll
    for (int i = 0; i < 8; ++i) w2[i] = wfrag(1, i >> 2, i & 3);
    SBAR();
#pragma unroll
    for (int i = 0; i < 16; ++i) { c[i & 3] = MFMA(w1[i], of[i >> 2], c[i & 3]); if (i >= 8) w2[i] = wfrag(1, i >> 2, i & 3); SBAR(); }
#pragma unroll
    for (int i = 0; i < 16; ++i) { c[i & 3] = MFMA(w2[i], of[4 + (i >> 2)], c[i & 3]); w1[i] = wfrag(0, (i >> 1) & 3, (i & 1) + (i < 8 ? 2 : 0)); SBAR(); }
#pragma unroll
    for (int i = 0; i < 8; ++i) c[2 + (i & 1)] = MFMA(w1[i], of[8 + (i >> 1)], c[2 + (i & 1)]);
    SBAR();
#pragma unroll
    for (int i = 0; i < 4; ++i) c[0] = MFMA(w1[8 + 2 * i], of[8 + i], c[0]);
    SBAR();
#pragma unroll
    for (int q = 0; q < 4; ++q) { lds_st4(x0 + (2u + g) * SL + q * 1024, c[2], q); lds_st4(x0 + (2u + g) * SL + 4096 + q * 1024, c[3], q); }
    SBAR();
#pragma unroll
    for (int i = 0; i < 4; ++i) c[1] = MFMA(w1[9 + 2 * i], of[8 + i], c[1]);
    PIN(c[0]); PIN(c[1]);
    BAR();
    const unsigned ra = x0 + (3u - g) * SL;
    f32x4 va[8];
#pragma unroll
    for (int q = 0; q < 8; ++q) va[q] = lds_f4(ra + q * 1024);
    SBAR();
    auto put = [&](int nb) {
      float mx = 0.f;
#pragma unroll
      for (int r = 0; r < 16; ++r) { c[nb][r] += va[4 * nb + (r >> 2)][r & 3]; mx = fmaxf(mx, fabsf(c[nb][r])); }
      const unsigned e = max(wave_umax(__float_as_uint(mx)) >> 23, 15u);
      const float sc = __uint_as_float((268u - e) << 23);
#pragma unroll
      for (int s = 0; s < 2; ++s) {
        u32x4 w;
#pragma unroll
        for (int q = 0; q < 4; ++q) { f32x2 v = {c[nb][8 * s + 2 * q] * sc, c[nb][8 * s + 2 * q + 1] * sc}; w[q] = __builtin_bit_cast(unsigned, __builtin_convertvector(v, f16x2)); }
        __builtin_amdgcn_raw_buffer_store_b128(w, srdP, hg * 32768u + wid * 4096u + (unsigned)(2 * nb + s) * 1024u + ln * 16u, 0, 16);
      }
      if (ln == 0) *(LAS unsigned char*)(xch + wid * 4u + (unsigned)nb) = (unsigned char)e;
    };
    put(0);
    put(1);
  }
  WAIT_BAR(0);
  if (wid == 0) {
    const bool sel = hs.act && ln >= 8u;
    if (sel) __hip_atomic_store(hs.base + hs.peer, (*(const LAS unsigned*)(xch + (ln & 7u) * 4u) & 0xFFFFu) | 0x10000u, RLX_AGENT);
    const unsigned fv = hs_wait_flags(hs, sel);
    if (sel) *(LAS unsigned*)(xch + 64u + (ln & 7u) * 4u) = fv;
  }
  BAR();
  if (g == 0) {
    u32x4 pv[8];
#pragma unroll
    for (int u = 0; u < 8; ++u) pv[u] = __builtin_amdgcn_raw_buffer_load_b128(srdP, hg * 4096u + w4 * 1024u + ln * 16u, (unsigned)u * 32768u, 18);
    const u32x4 f0 = *(const LAS u32x4*)(xch + 64u), f1 = *(const LAS u32x4*)(xch + 80u);
    float acc[8] = {};
#pragma unroll
    for (int u = 0; u < 8; ++u) {
      const unsigned e = ((u < 4 ? f0[u & 3] : f1[u & 3]) >> (8u * (w4 >> 1))) & 0xFFu;
      const float isc = __uint_as_float((e - 14u) << 23);
#pragma unroll
      for (int q = 0; q < 4; ++q) { const unsigned wv = pv[u][q]; const f16x2 hv = __builtin_bit_cast(f16x2, wv); acc[2 * q] = fmaf((float)hv[0], isc, acc[2 * q]); acc[2 * q + 1] = fmaf((float)hv[1], isc, acc[2 * q + 1]); }
    }
    const unsigned qrow = (hg & 3u) * 32u + (ln & 31u), n = j * 128u + ((hg >> 2) * 2u + (w4 >> 1)) * 32u + (w4 & 1u) * 16u + (ln >> 5) * 8u;
    *(u32x4*)(out + ((size_t)b * SQ + qrow) * DM + n) = (u32x4){cvtpk(acc[0], acc[1]), cvtpk(acc[2], acc[3]), cvtpk(acc[4], acc[5]), cvtpk(acc[6], acc[7])};
  }
}

extern "C" void kernel_launch(void* const* d_in, const int* in_sizes, int n_in, void* d_out, int out_size, void* d_ws, size_t ws_size, hipStream_t stream) {
  (void)in_sizes; (void)n_in; (void)out_size; (void)ws_size;
  (void)hipFuncSetAttribute((const void*)mha_fused, hipFuncAttributeMaxDynamicSharedMemorySize, (int)LDS_BYTES);
  hipLaunchKernelGGL(mha_fused, dim3(256), dim3(512), LDS_BYTES, stream, (const unsigned short*)d_in[0], (const unsigned short*)d_in[1], (const unsigned short*)d_in[2],
                     (const unsigned short*)d_in[3], (const unsigned short*)d_in[4], (unsigned short*)d_out, (char*)d_ws);
}
```

```cpp
#include <hip/hip_runtime.h>
#include <hip/hip_bf16.h>
#include <stdint.h>
#include <type_traits>

typedef __bf16 bf16x8 __attribute__((ext_vector_type(8)));
typedef __bf16 bf16x2 __attribute__((ext_vector_type(2)));
typedef _Float16 f16x2 __attribute__((ext_vector_type(2)));
typedef short s16x4 __attribute__((ext_vector_type(4)));
typedef float f32x16 __attribute__((ext_vector_type(16)));
typedef float f32x4 __attribute__((ext_vector_type(4)));
typedef float f32x2 __attribute__((ext_vector_type(2)));
typedef unsigned u32x4 __attribute__((ext_vector_type(4)));
typedef unsigned u32x2 __attribute__((ext_vector_type(2)));
#define LAS __attribute__((address_space(3)))
#define GAS __attribute__((address_space(1)))
#define DI __device__ __forceinline__
#define MFMA(a, b, c) __builtin_amdgcn_mfma_f32_32x32x16_bf16((a), (b), (c), 0, 0, 0)
#define RLX_AGENT __ATOMIC_RELAXED, __HIP_MEMORY_SCOPE_AGENT
#define PIN(x) asm volatile("" : "+v"(x))
#define SBAR() __builtin_amdgcn_sched_barrier(0)
#define PRIO(n) __builtin_amdgcn_s_setprio(n)

constexpr int SQ = 128, DM = 512, NH = 32, SK = 2048, DH = 128;
constexpr unsigned U = 16384u, SL = 32768u;
constexpr unsigned LDS_BYTES = 5 * SL;
#define CODEPF 17
#define PACE 112u
#define PACE0 500u
#define CASC 122u
#define CDEC 7u
#define CFLOOR 94u
#define PSLIM 8192.0f
constexpr float CSC = 0.088388347648318447f * 1.4426950408889634f;
constexpr size_t WS_FLAG = 0;
constexpr unsigned FB = 128;
constexpr size_t WS_O = 1 << 20;
constexpr size_t WS_P = WS_O + (8u << 20);

DI unsigned rfl(unsigned x) { return __builtin_amdgcn_readfirstlane(x); }
DI unsigned cvtpk(float lo, float hi) { f32x2 v = {lo, hi}; bf16x2 b = __builtin_convertvector(v, bf16x2); return __builtin_bit_cast(unsigned, b); }
DI bf16x8 pack8(const f32x16& a, int s) {
  u32x4 w = {cvtpk(a[8 * s], a[8 * s + 1]), cvtpk(a[8 * s + 2], a[8 * s + 3]), cvtpk(a[8 * s + 4], a[8 * s + 5]), cvtpk(a[8 * s + 6], a[8 * s + 7])};
  return __builtin_bit_cast(bf16x8, w);
}
DI bf16x8 lds_b128(unsigned a) { return *(const LAS bf16x8*)a; }
DI f32x4 lds_f4(unsigned a) { return *(const LAS f32x4*)a; }
DI void lds_st4(unsigned a, const f32x16& v, int q) { *(LAS f32x4*)a = (f32x4){v[4 * q], v[4 * q + 1], v[4 * q + 2], v[4 * q + 3]}; }
DI s16x4 lds_tr(unsigned a) { return __builtin_amdgcn_ds_read_tr16_b64_v4i16((LAS s16x4*)a); }
DI bf16x8 lds_tr2(unsigned a0, unsigned a1) {
  s16x4 lo = lds_tr(a0), hi = lds_tr(a1);
  return __builtin_bit_cast(bf16x8, __builtin_shufflevector(lo, hi, 0, 1, 2, 3, 4, 5, 6, 7));
}
DI void dma4_kv(unsigned m0v, unsigned voff, __amdgpu_buffer_rsrc_t srd, unsigned soff) {
  asm volatile("s_mov_b32 m0, %0\n\ts_nop 3\n\t"
               "buffer_load_dwordx4 %1, %2, %3 offen nt lds\n\t"
               "buffer_load_dwordx4 %1, %2, %3 offen offset:1024 nt lds\n\t"
               "buffer_load_dwordx4 %1, %2, %3 offen offset:2048 nt lds\n\t"
               "buffer_load_dwordx4 %1, %2, %3 offen offset:3072 nt lds"
               :: "s"(rfl(m0v)), "v"(voff), "s"(srd), "s"(rfl(soff)) : "memory");
}
DI void dma1(unsigned m0v, unsigned voff, __amdgpu_buffer_rsrc_t srd, unsigned soff) {
  asm volatile("s_mov_b32 m0, %0\n\ts_nop 3\n\t"
               "buffer_load_dwordx4 %1, %2, %3 offen lds"
               :: "s"(rfl(m0v)), "v"(voff), "s"(srd), "s"(rfl(soff)) : "memory");
}
DI void dma1nt(unsigned m0v, unsigned voff, __amdgpu_buffer_rsrc_t srd, unsigned soff) {
  asm volatile("s_mov_b32 m0, %0\n\ts_nop 3\n\t"
               "buffer_load_dwordx4 %1, %2, %3 offen nt lds"
               :: "s"(rfl(m0v)), "v"(voff), "s"(srd), "s"(rfl(soff)) : "memory");
}
#define WAIT_BAR(N) asm volatile("s_waitcnt vmcnt(" #N ") lgkmcnt(0)\n\ts_barrier" ::: "memory")
#define BAR() asm volatile("s_waitcnt lgkmcnt(0)\n\ts_barrier" ::: "memory")
#define WAITVM(N) asm volatile("s_waitcnt vmcnt(" #N ")" ::: "memory")

DI float xhalf_max(float v) { auto r = __builtin_amdgcn_permlane32_swap(__float_as_uint(v), __float_as_uint(v), false, false); return fmaxf(__uint_as_float(r[0]), __uint_as_float(r[1])); }
DI float xhalf_sum(float v) { auto r = __builtin_amdgcn_permlane32_swap(__float_as_uint(v), __float_as_uint(v), false, false); return __uint_as_float(r[0]) + __uint_as_float(r[1]); }

DI unsigned wave_umax(unsigned v) {
#define DPPMAX(ctrl, rmask) v = max(v, (unsigned)__builtin_amdgcn_update_dpp(0, (int)v, ctrl, rmask, 0xF, false))
  DPPMAX(0x111, 0xF); DPPMAX(0x112, 0xF); DPPMAX(0x114, 0xF); DPPMAX(0x118, 0xF);
  DPPMAX(0x142, 0xA); DPPMAX(0x143, 0xC);
#undef DPPMAX
  return (unsigned)__builtin_amdgcn_readlane((int)v, 63);
}
struct Hs { GAS unsigned* base; unsigned mine, peer; bool act; };
DI Hs hs_make(char* ws, unsigned bid, unsigned L) {
  Hs h; h.base = (GAS unsigned*)(ws + WS_FLAG);
  const bool h1 = L < 4u; h.act = h1 || (L >= 8u && L < 16u);
  const unsigned pb = h1 ? ((bid & ~24u) | (L << 3)) : ((bid & ~7u) | (L & 7u));
  h.mine = bid * FB + (L & 15u); h.peer = pb * FB + (h1 ? ((bid >> 3) & 3u) : 8u + (bid & 7u));
  return h;
}
DI void hs_echo(const Hs& h) { if (h.act) __hip_atomic_store(h.base + h.peer + 64u, __hip_atomic_load(h.base + h.mine + 32u, RLX_AGENT), RLX_AGENT); }
DI void hs_wait_armed(const Hs& h) {
#pragma nounroll
  for (unsigned spins = 0; spins < (1u << 20); ++spins) {
    hs_echo(h);
    bool ok = true;
    if (h.act) ok = __hip_atomic_load(h.base + h.mine + 64u, RLX_AGENT) == __hip_atomic_load(h.base + h.peer + 32u, RLX_AGENT);
    if (__all(ok)) break;
    __builtin_amdgcn_s_sleep(4);
  }
}
DI unsigned hs_wait_flags(const Hs& h, bool sel) {
  unsigned v = 1u;
#pragma nounroll
  for (unsigned spins = 0; spins < (1u << 20); ++spins) {
    if (sel) v = __hip_atomic_load(h.base + h.mine, RLX_AGENT);
    if (__all(v != 0u)) break;
    if ((spins & 15u) == 15u) hs_echo(h);
    __builtin_amdgcn_s_sleep(1);
  }
  asm volatile("" ::: "memory");
  return v;
}

__global__ __launch_bounds__(512, 2) void mha_fused(const unsigned short* __restrict__ x, const unsigned short* __restrict__ kc, const unsigned short* __restrict__ vc,
                                                    const unsigned short* __restrict__ wq, const unsigned short* __restrict__ wo, unsigned short* __restrict__ out, char* __restrict__ ws) {
  extern __shared__ __attribute__((aligned(16))) char lds[];
  const unsigned tid = threadIdx.x, lane = tid & 63u, r32 = lane & 31u, hi = lane >> 5;
  const unsigned wid = rfl(tid >> 6), g = wid >> 2, w4 = wid & 3u;
  const unsigned bid = blockIdx.x, hg = bid & 7u, b = bid >> 5, j = (bid >> 3) & 3u, h = hg * 4u + j;
  const unsigned lds0 = (unsigned)(uintptr_t)lds;
  const unsigned tref = (unsigned)__builtin_amdgcn_s_memrealtime() + PACE0 + (((bid >> 3) * PACE) >> 5);
  auto due = [&](unsigned t) { unsigned long long t_; asm volatile("s_memrealtime %0\n\ts_waitcnt lgkmcnt(0)" : "=s"(t_) :: "memory"); return (int)((unsigned)t_ - tref - t) >= 0; };
  auto tat = [](unsigned hx) { return hx * (PACE / 2u); };
  auto pace = [&](unsigned t) { while (!due(t)) __builtin_amdgcn_s_sleep(2); };
  asm volatile("" :: "s"(out), "s"(ws), "s"(wo));

  const __amdgpu_buffer_rsrc_t srdK = __builtin_amdgcn_make_buffer_rsrc((void*)(kc + (size_t)(b * NH + h) * SK * DH), (short)0, SK * DH * 2, 0x00020000);
  const __amdgpu_buffer_rsrc_t srdV = __builtin_amdgcn_make_buffer_rsrc((void*)(vc + (size_t)(b * NH + h) * SK * DH), (short)0, SK * DH * 2, 0x00020000);
  const __amdgpu_buffer_rsrc_t srdX = __builtin_amdgcn_make_buffer_rsrc((void*)(x + (size_t)b * SQ * DM), (short)0, SQ * DM * 2, 0x00020000);
  const __amdgpu_buffer_rsrc_t srdWq = __builtin_amdgcn_make_buffer_rsrc((void*)wq, (short)0, DM * NH * DH * 2, 0x00020000);
  const __amdgpu_buffer_rsrc_t srdWo = __builtin_amdgcn_make_buffer_rsrc((void*)(wo + (size_t)hg * 512 * DM), (short)0, 512 * DM * 2, 0x00020000);

  unsigned w8k = w4 * 8192u; asm volatile("" : "+s"(w8k));
  const unsigned l4 = lane >> 4, l15 = lane & 15u;
  const unsigned voffK = w4 * 4096u + l4 * 256u + ((l15 ^ (l4 | (w4 << 2))) << 4);
  const unsigned voffV = w4 * 4096u + l4 * 256u + ((l15 ^ (l4 << 2)) << 4);
  const unsigned voffWq = l4 * 8192u + ((l15 ^ (l4 << 2)) << 4);
  const unsigned voffWo = l4 * 1024u + ((l15 ^ (l4 << 2)) << 4);
  const unsigned voffX = (lane >> 3) * 1024u + (((lane & 7u) ^ (((lane >> 4) & 1u) | (w4 << 1))) << 4);

  auto issueK = [&](unsigned t, unsigned off) { dma4_kv(lds0 + off + w4 * 4096u, voffK, srdK, t * U); };
  auto issueV = [&](unsigned t, unsigned off) { dma4_kv(lds0 + off + w4 * 4096u, voffV, srdV, t * U); };
  auto hpos = [&](unsigned cc) { const unsigned r = cc - 1u + b % 3u; return rfl(cc == 0u ? j : (j + 1u + ((0x124u >> ((2u * r) & 15u)) & 3u)) & 3u); };
  auto issueWo = [&](unsigned cc, unsigned slot) {
    const unsigned c = hpos(cc);
#pragma unroll
    for (int q = 0; q < 8; ++q) dma1(lds0 + slot * SL + w8k + q * 1024u, voffWo, srdWo, (c * 128u + w4 * 32u + q * 4u) * 1024u + j * 256u);
  };
  const unsigned rot = (b + 2u * j) & 7u;
  auto qslot = [](int c) { return (c % 3 == 0) ? 3u : (c % 3 == 1) ? 4u : 2u; };
  auto issueQP = [&](int c) {
    const unsigned kcn = ((unsigned)c + rot) & 7u, sb = lds0 + qslot(c) * SL;
#pragma unroll
    for (int q = 0; q < 4; ++q) {
      dma1nt(sb + (w4 * 4u + q) * 1024u, voffWq, srdWq, (kcn * 64u + w4 * 16u + q * 4u) * 8192u + h * 256u);
      dma1(sb + U + (q * 4u + w4) * 1024u, voffX, srdX, (q * 4u + w4) * 8192u + kcn * 128u);
    }
  };
  const unsigned blk = (lane >> 4) & 1u, q4 = l15 >> 2, p4 = lane & 3u, pp = ((p4 & 1u) << 1) | (p4 >> 1);
  const unsigned trV = lds0 + U + (4u * hi + q4) * 256u + ((q4 ^ (2u * g)) << 6) + blk * 32u + pp * 8u;
  const unsigned rdK = lds0 + ((r32 & 7u) + 16u * (r32 >> 3)) * 256u + ((hi ^ ((r32 & 3u) | ((r32 >> 3) << 2))) << 4);
  const unsigned qx = lds0 + 3u * SL + w4 * 4096u + lane * 16u;

  bf16x8 qf[8], kf[16];
  auto rdk = [&](unsigned off, int i) { return lds_b128(((rdK + off) ^ (unsigned)((i & 7) << 5)) + (i >> 3) * 2048); };
  if (g == 0) {
    const unsigned trW = lds0 + (8u * hi + q4) * 256u + (q4 << 6) + blk * 32u + pp * 8u;
    const unsigned rdX = lds0 + U + (w4 * 32u + r32) * 128u + ((hi ^ (((r32 >> 1) & 1u) | ((r32 >> 3) << 1))) << 4);
    f32x16 qa[4] = {};
    bf16x8 xb[2][4], wa_[2][16];
    auto rdq = [&](int c, int s) {
      const unsigned u0 = qslot(c) * SL;
      xb[c & 1][s] = lds_b128((rdX + u0) ^ (unsigned)(s << 5));
#pragma unroll
      for (int d = 0; d < 4; ++d) { const unsigned a = ((trW + u0) ^ (unsigned)(d << 6)) + s * 4096; wa_[c & 1][4 * s + d] = lds_tr2(a, a + 1024); }
    };
    {
      unsigned long long pc; asm volatile("s_getpc_b64 %0" : "=s"(pc));
      const __amdgpu_buffer_rsrc_t srdC = __builtin_amdgcn_make_buffer_rsrc((void*)(pc & ~255ull), (short)0, CODEPF * 1024, 0x00020000);
      if (wid == 1) dma1(lds0, lane * 16u, srdC, ((bid >> 3) % CODEPF) * 1024u);
    }
    unsigned r0;
    { const Hs hs = hs_make(ws, bid, lane);
      if (wid == 0 && hs.act) __hip_atomic_store(hs.base + hs.mine, 0u, RLX_AGENT);
      asm volatile("global_load_dword %0, %1, off offset:256 sc1" : "=v"(r0) : "v"(hs.base + hs.mine) : "memory"); }
    BAR();
#pragma unroll
    for (int s = 0; s < 4; ++s) rdq(0, s);
#pragma unroll
    for (int c = 0; c < 8; ++c) {
      if (c == 0) {
        asm volatile("s_waitcnt vmcnt(0) lgkmcnt(0)\n\ts_barrier" : "+v"(r0) :: "memory");
        const Hs hs = hs_make(ws, bid, lane);
        if (wid == 0 && hs.act) __hip_atomic_store(hs.base + hs.peer + 32u, r0 + 1u, RLX_AGENT);
      } else if (c < 7) BAR();
#pragma unroll
      for (int s = 0; s < 4; ++s)
#pragma unroll
        for (int d = 0; d < 4; ++d) {
          SBAR();
          qa[d] = MFMA(wa_[c & 1][4 * s + d], xb[c & 1][s], qa[d]);
          SBAR();
          if (d & 1) { asm volatile("s_nop 2"); if (d == 3) __builtin_amdgcn_s_sleep(1); SBAR(); }
          if (c < 7) {
            const unsigned u0 = qslot(c + 1) * SL, a = ((trW + u0) ^ (unsigned)(d << 6)) + s * 4096;
            if (d == 0) xb[(c + 1) & 1][s] = lds_b128((rdX + u0) ^ (unsigned)(s << 5));
            wa_[(c + 1) & 1][4 * s + d] = lds_tr2(a, a + 1024);
          }
        }
      SBAR();
    }
#pragma unroll
    for (int d = 0; d < 4; ++d) {
#pragma unroll
      for (int r = 0; r < 16; ++r) qa[d][r] *= CSC;
      qf[2 * d] = pack8(qa[d], 0); qf[2 * d + 1] = pack8(qa[d], 1);
    }
#pragma unroll
    for (int f = 0; f < 8; ++f) *(LAS bf16x8*)(qx + (f >> 2) * U + (f & 3) * 1024) = qf[f];
    BAR();
#pragma unroll
    for (int i = 0; i < 16; ++i) kf[i] = rdk(U, i);
  } else {
    issueQP(0); issueQP(1);
    WAIT_BAR(8); issueQP(2);
#pragma unroll
    for (int c = 0; c < 5; ++c) { WAIT_BAR(8); issueQP(c + 3); }
    issueK(0, U);
    WAIT_BAR(12);
    issueK(1, 0);
    WAIT_BAR(8);
    pace(tat(1)); issueK(2, SL); pace(tat(2)); issueV(0, SL + U);
    WAIT_BAR(8);
    const Hs hs = hs_make(ws, bid, lane);
    unsigned rq;
    asm volatile("global_load_dword %0, %1, off offset:128 sc1" : "=v"(rq) : "v"(hs.base + hs.mine) : "memory");
#pragma unroll
    for (int f = 0; f < 8; ++f) qf[f] = lds_b128(qx + (f >> 2) * U + (f & 3) * 1024);
#pragma unroll
    for (int i = 0; i < 16; ++i) kf[i] = rdk(0, i);
    pace(tat(3)); issueK(3, 2u * SL); pace(tat(4)); issueV(1, 2u * SL + U);
    pace(tat(5));
    issueK(4, 3u * SL);
    pace(tat(6));
    issueV(2, 3u * SL + U);
    asm volatile("s_waitcnt vmcnt(16) lgkmcnt(0)\n\ts_barrier" : "+v"(rq) :: "memory");
    if (wid == 4 && hs.act) __hip_atomic_store(hs.base + hs.peer + 64u, rq, RLX_AGENT);
  }

  f32x16 o[4] = {};
  float m_run = 0.f, l_run = 0.f;
  f32x16 negm = {};
  bf16x8 pf[4], vf[16];
  auto vfrag = [&](unsigned av, int ks, int d) { const unsigned a = (av ^ (unsigned)(d << 6)) + (32 * (ks & 1) + 8 * (ks >> 1)) * 256; return lds_tr2(a, a + 4096); };
  bool vpend = false; unsigned vdst = 0u, vsrc = 0u;
  auto scores_slow = [&](bool first, float& ps) {
    f32x16 s0 = {}, s1 = {};
#pragma unroll
    for (int s = 0; s < 8; ++s) { s0 = MFMA(kf[s], qf[s], s0); s1 = MFMA(kf[8 + s], qf[s], s1); }
    float mx = fmaxf(s0[0], s1[0]);
#pragma unroll
    for (int r = 1; r < 16; ++r) mx = fmaxf(fmaxf(mx, s0[r]), s1[r]);
    mx = xhalf_max(mx);
    const float m_new = first ? mx : fmaxf(mx, m_run);
    if (!first) {
      const float alpha = __builtin_amdgcn_exp2f(m_run - m_new);
      l_run *= alpha;
#pragma unroll
      for (int d = 0; d < 4; ++d)
#pragma unroll
        for (int r = 0; r < 16; ++r) o[d][r] *= alpha;
    }
    m_run = m_new;
    ps = 0.f;
#pragma unroll
    for (int r = 0; r < 16; ++r) { negm[r] = -m_new; s0[r] = __builtin_amdgcn_exp2f(s0[r] - m_new); s1[r] = __builtin_amdgcn_exp2f(s1[r] - m_new); ps += s0[r] + s1[r]; }
    pf[0] = pack8(s0, 0); pf[1] = pack8(s0, 1); pf[2] = pack8(s1, 0); pf[3] = pack8(s1, 1);
  };
  auto scores = [&](bool first, auto inloop) {
    float ps = 0.f;
    PRIO(2);
    if (!first) {
      SBAR();
      f32x16 s0 = MFMA(kf[0], qf[0], negm);
      SBAR();
#pragma unroll
      for (int s = 1; s < 8; ++s) {
        s0 = MFMA(kf[s], qf[s], s0); SBAR();
        if (decltype(inloop)::value && (s & 1)) { PIN(s0); if (vpend) dma1nt(vdst + (s >> 1) * 1024u, voffV, srdV, vsrc + (s >> 1) * 1024u); SBAR(); }
      }
      SBAR();
      f32x16 s1;
      asm volatile("v_mfma_f32_32x32x16_bf16 %0, %1, %2, %3" : "=&v"(s1) : "v"(kf[8]), "v"(qf[0]), "v"(negm));
      SBAR();
      float pa = 0.f, pb = 0.f;
      unsigned w[16];
#define EXP2(S, e) do { S[e] = __builtin_amdgcn_exp2f(S[e]); S[e + 1] = __builtin_amdgcn_exp2f(S[e + 1]); float t0_ = S[e], t1_ = S[e + 1]; PIN(t0_); PIN(t1_); S[e] = t0_; S[e + 1] = t1_; SBAR(); } while (0)
#define FIN2(S, e, wi) do { pa += S[e]; pb += S[e + 1]; w[wi] = cvtpk(S[e], S[e + 1]); PIN(pa); PIN(pb); PIN(w[wi]); SBAR(); } while (0)
#pragma unroll
      for (int s = 1; s < 8; ++s) {
        s1 = MFMA(kf[8 + s], qf[s], s1); SBAR();
        EXP2(s0, 2 * (s - 1));
        if (s > 1) FIN2(s0, 2 * (s - 2), s - 2);
        if (s == 3) asm volatile("" :: "v"(negm));
      }
      EXP2(s0, 14); FIN2(s0, 12, 6);
      EXP2(s1, 0); FIN2(s0, 14, 7);
#pragma unroll
      for (int i = 1; i < 8; ++i) { EXP2(s1, 2 * i); FIN2(s1, 2 * (i - 1), 7 + i); }
      FIN2(s1, 14, 15);
#undef EXP2
#undef FIN2
      ps = pa + pb;
#pragma unroll
      for (int i = 0; i < 4; ++i) pf[i] = __builtin_bit_cast(bf16x8, (u32x4){w[4 * i], w[4 * i + 1], w[4 * i + 2], w[4 * i + 3]});
    }
    if (__builtin_expect(first || __any(!(ps <= PSLIM)), 0)) scores_slow(first, ps);
    l_run += ps;
    PIN(pf[0]); PIN(pf[1]); PIN(pf[2]); PIN(pf[3]); PIN(l_run); if (decltype(inloop)::value) PIN(negm);
    PRIO(0);
  };

  __builtin_amdgcn_s_waitcnt(0xC07F);
  unsigned tprev = tref, imin = 0u;
  unsigned sp = 1u + g;
  const unsigned p0 = 3u + ((((bid >> 3) * 11u) & 31u) * 3u >> 2);
  for (int it = 0; it < 15; ++it) {
    const unsigned p = 2u * (unsigned)it + g + 1u;
    scores(it == 0, std::true_type{});
    unsigned long long ts;
    asm volatile("s_waitcnt vmcnt(0) lgkmcnt(0)\n\ts_barrier\n\ts_memrealtime %0" : "=s"(ts) :: "memory");
    const unsigned av = trV + sp * SL;
#pragma unroll
    for (int i = 0; i < 16; ++i) vf[i] = vfrag(av, i >> 2, i & 3);
    SBAR();
    SBAR();
#pragma unroll
    for (int i = 0; i < 8; ++i) kf[i] = rdk(sp * SL, i);
    SBAR();
    const unsigned sn = sp >= 3u ? sp - 3u : sp + 2u;
    const bool go = p != p0;
    const unsigned ni = p + (p < p0 ? 3u : 2u), ns = (p < p0 ? (sn == 4u ? 0u : sn + 1u) : sn) * SL;
    const unsigned tni = tat(ni + min(ni, 30u));
    bool kgo = go && ni <= 30u;
    SBAR();
#pragma unroll
    for (int i = 0; i < 2; ++i) { o[i & 3] = MFMA(vf[i], pf[i >> 2], o[i & 3]); SBAR(); }
    asm volatile("s_waitcnt lgkmcnt(0)" : "+s"(ts) :: "memory");
    const int late = (int)((unsigned)ts - tref - tni);
    const bool vend = ni >= 31u;
    if (kgo && late + (int)(PACE / 2u) + 15 >= 0) { issueK(ni + 1u, ns); kgo = false; }
    if (vend) issueV(ni - 1u, ns + U);
    SBAR();
#pragma unroll
    for (int i = 2; i < 8; ++i) { o[i & 3] = MFMA(vf[i], pf[i >> 2], o[i & 3]); SBAR(); }
#pragma unroll
    for (int i = 8; i < 16; ++i) { o[i & 3] = MFMA(vf[i], pf[i >> 2], o[i & 3]); if (i < 12) { kf[2 * i - 8] = rdk(sp * SL, 2 * i - 8); kf[2 * i - 7] = rdk(sp * SL, 2 * i - 7); } SBAR(); }
    if (kgo) { pace(tni - PACE / 2u); issueK(ni + 1u, ns); }
    if (go && !vend && late + 35 < 0) pace(tni);
    if (late > 150) {
      imin = ((unsigned)ts - tprev > 335u && it > 0) ? CASC : imin > CFLOOR + CDEC ? imin - CDEC : min(imin, CFLOOR);
      if (!vend && imin != 0u) pace((unsigned)ts - tref + imin);
    }
    tprev = (unsigned)ts;
    vpend = go && !vend; vdst = lds0 + ns + U + w4 * 4096u; vsrc = (ni - 1u) * U;
    if (it == 14) issueWo(g, 3u + g);
    sp = sn;
    __builtin_amdgcn_s_waitcnt(0xC07F);
    asm volatile("s_barrier" ::: "memory");
  }
  scores(false, std::false_type{});
  unsigned ln; asm volatile("v_mbcnt_lo_u32_b32 %0, -1, 0\n\tv_mbcnt_hi_u32_b32 %0, -1, %0" : "=v"(ln));
  const unsigned x0 = lds0 + w8k + ln * 16u;
  l_run = xhalf_sum(l_run);
  const unsigned stats = lds0 + 2u * SL + (ln & 31u) * 8u;
  if (ln < 32u) *(LAS f32x2*)(stats + wid * 256u) = (f32x2){m_run, l_run};
  const Hs hs = hs_make(ws, bid, ln);
  unsigned hs_r, hs_q, hs_qi, hs_ro;
  asm volatile("global_load_dword %0, %4, off offset:256 sc1\n\tglobal_load_dword %1, %5, off offset:128 sc1\n\t"
               "global_load_dword %2, %4, off offset:128 sc1\n\tglobal_load_dword %3, %5, off offset:256 sc1"
               : "=&v"(hs_r), "=&v"(hs_q), "=&v"(hs_qi), "=&v"(hs_ro) : "v"(hs.base + hs.mine), "v"(hs.base + hs.peer) : "memory");
  bf16x8 of0[4];
  {
    WAIT_BAR(12);
    const unsigned av = trV + (1u + g) * SL;
#pragma unroll
    for (int ks = 0; ks < 4; ++ks) { vf[4 * ks + 2] = vfrag(av, ks, 2); vf[4 * ks + 3] = vfrag(av, ks, 3); }
    SBAR();
#pragma unroll
    for (int ks = 0; ks < 4; ++ks) { vf[4 * ks] = vfrag(av, ks, 0); vf[4 * ks + 1] = vfrag(av, ks, 1); }
    SBAR();
#pragma unroll
    for (int ks = 0; ks < 4; ++ks) { o[2] = MFMA(vf[4 * ks + 2], pf[ks], o[2]); o[3] = MFMA(vf[4 * ks + 3], pf[ks], o[3]); }
    SBAR();
#pragma unroll
    for (int ks = 0; ks < 2; ++ks) { o[0] = MFMA(vf[4 * ks], pf[ks], o[0]); o[1] = MFMA(vf[4 * ks + 1], pf[ks], o[1]); }
    SBAR();
    const unsigned xo = x0 + g * SL, xi = x0 + (1u - g) * SL;
#pragma unroll
    for (int q = 0; q < 4; ++q) { lds_st4(xo + q * 1024, o[2], q); lds_st4(xo + 4096 + q * 1024, o[3], q); }
    SBAR();
#pragma unroll
    for (int ks = 2; ks < 4; ++ks) { o[0] = MFMA(vf[4 * ks], pf[ks], o[0]); o[1] = MFMA(vf[4 * ks + 1], pf[ks], o[1]); }
    SBAR();
    if (g == 0) BAR();
    const f32x2 st = *(const LAS f32x2*)(stats + (wid ^ 4u) * 256u);
    const float ms = fmaxf(m_run, st[0]);
    const float e_me = __builtin_amdgcn_exp2f(m_run - ms), e_ot = __builtin_amdgcn_exp2f(st[0] - ms);
    const float inv = 1.f / (l_run * e_me + st[1] * e_ot);
    float a_me = e_me * inv, a_ot = e_ot * inv;
    PIN(a_me); PIN(a_ot);
    asm volatile("s_waitcnt vmcnt(0) lgkmcnt(0)\n\ts_barrier" : "+v"(hs_r), "+v"(hs_q), "+v"(hs_qi), "+v"(hs_ro) :: "memory");
    f32x4 w0[4], w1[4];
#pragma unroll
    for (int q = 0; q < 4; ++q) { w0[q] = lds_f4(xi + q * 1024); w1[q] = lds_f4(xi + 4096 + q * 1024); }
#pragma unroll
    for (int q = 0; q < 4; ++q)
#pragma unroll
      for (int e = 0; e < 4; ++e) { o[0][4 * q + e] = fmaf(w0[q][e], a_ot, o[0][4 * q + e] * a_me); o[1][4 * q + e] = fmaf(w1[q][e], a_ot, o[1][4 * q + e] * a_me); }
    of0[0] = pack8(o[0], 0); of0[1] = pack8(o[0], 1); of0[2] = pack8(o[1], 0); of0[3] = pack8(o[1], 1);
  }
  PRIO(0);
  const __amdgpu_buffer_rsrc_t srdO = __builtin_amdgcn_make_buffer_rsrc((void*)(ws + WS_O + (size_t)(hg * 8u + b) * 131072u), (short)0, 4 * 32768, 0x00020000);
#pragma unroll
  for (int s = 0; s < 4; ++s) __builtin_amdgcn_raw_buffer_store_b128(__builtin_bit_cast(u32x4, of0[s]), srdO, j * 32768u + wid * 4096u + (unsigned)s * 1024u + ln * 16u, 0, 16);
  f32x16 c[4] = {};
  const unsigned trW2 = trV - U + (ln >> 5) * 1024u + g * U;
  auto wfrag = [&](unsigned slot, int s, int nb) { const unsigned a = ((trW2 + slot * SL) ^ (unsigned)(nb << 6)) + s * 4096; return lds_tr2(a, a + 1024); };
  bf16x8 w1[16], w2[16];
#pragma unroll
  for (int i = 0; i < 16; ++i) w2[i] = wfrag(3, i >> 2, i & 3);
  SBAR();
#pragma unroll
  for (int i = 0; i < 16; ++i) { c[i & 3] = MFMA(w2[i], of0[i >> 2], c[i & 3]); w1[i] = wfrag(4, i >> 2, i & 3); SBAR(); }
  PIN(c[0]); PIN(c[1]); PIN(c[2]); PIN(c[3]);
  WAIT_BAR(0);
  if (wid == 0) {
    if (hs.act && hs_qi != hs_ro) __hip_atomic_store(hs.base + hs.peer + 64u, hs_qi, RLX_AGENT);
    if (!__all(hs_r == hs_q || !hs.act)) hs_wait_armed(hs);
    if (ln < 4u) __hip_atomic_store(hs.base + hs.peer, 1u, RLX_AGENT);
  }
  issueWo(2u + g, 1u - g);
  if (wid == 0) hs_wait_flags(hs, ln < 4u);
  WAIT_BAR(0);
  const unsigned xch = lds0 + 4u * SL;
  const __amdgpu_buffer_rsrc_t srdP = __builtin_amdgcn_make_buffer_rsrc((void*)(ws + WS_P + (size_t)(b * 4u + j) * 262144u), (short)0, 262144, 0x00020000);
  {
    bf16x8 of[12];
#pragma unroll
    for (int i = 0; i < 12; ++i)
      of[i] = __builtin_bit_cast(bf16x8, __builtin_amdgcn_raw_buffer_load_b128(srdO, wid * 4096u + (unsigned)(i & 3) * 1024u + ln * 16u, hpos(1u + (unsigned)(i >> 2)) * 32768u, 16));
    SBAR();
#pragma unroll
    for (int i = 0; i < 8; ++i) w2[i] = wfrag(1, i >> 2, i & 3);
    SBAR();
#pragma unroll
    for (int i = 0; i < 16; ++i) { c[i & 3] = MFMA(w1[i], of[i >> 2], c[i & 3]); if (i >= 8) w2[i] = wfrag(1, i >> 2, i & 3); SBAR(); }
#pragma unroll
    for (int i = 0; i < 16; ++i) { c[i & 3] = MFMA(w2[i], of[4 + (i >> 2)], c[i & 3]); w1[i] = wfrag(0, (i >> 1) & 3, (i & 1) + (i < 8 ? 2 : 0)); SBAR(); }
#pragma unroll
    for (int i = 0; i < 8; ++i) c[2 + (i & 1)] = MFMA(w1[i], of[8 + (i >> 1)], c[2 + (i & 1)]);
    SBAR();
#pragma unroll
    for (int i = 0; i < 4; ++i) c[0] = MFMA(w1[8 + 2 * i], of[8 + i], c[0]);
    SBAR();
#pragma unroll
    for (int q = 0; q < 4; ++q) { lds_st4(x0 + (2u + g) * SL + q * 1024, c[2], q); lds_st4(x0 + (2u + g) * SL + 4096 + q * 1024, c[3], q); }
    SBAR();
#pragma unroll
    for (int i = 0; i < 4; ++i) c[1] = MFMA(w1[9 + 2 * i], of[8 + i], c[1]);
    PIN(c[0]); PIN(c[1]);
    BAR();
    const unsigned ra = x0 + (3u - g) * SL;
    f32x4 va[8];
#pragma unroll
    for (int q = 0; q < 8; ++q) va[q] = lds_f4(ra + q * 1024);
    SBAR();
    auto put = [&](int nb) {
      float mx = 0.f;
#pragma unroll
      for (int r = 0; r < 16; ++r) { c[nb][r] += va[4 * nb + (r >> 2)][r & 3]; mx = fmaxf(mx, fabsf(c[nb][r])); }
      const unsigned e = max(wave_umax(__float_as_uint(mx)) >> 23, 15u);
      const float sc = __uint_as_float((268u - e) << 23);
#pragma unroll
      for (int s = 0; s < 2; ++s) {
        u32x4 w;
#pragma unroll
        for (int q = 0; q < 4; ++q) { f32x2 v = {c[nb][8 * s + 2 * q] * sc, c[nb][8 * s + 2 * q + 1] * sc}; w[q] = __builtin_bit_cast(unsigned, __builtin_convertvector(v, f16x2)); }
        __builtin_amdgcn_raw_buffer_store_b128(w, srdP, hg * 32768u + wid * 4096u + (unsigned)(2 * nb + s) * 1024u + ln * 16u, 0, 16);
      }
      if (ln == 0) *(LAS unsigned char*)(xch + wid * 4u + (unsigned)nb) = (unsigned char)e;
    };
    put(0);
    put(1);
  }
  WAIT_BAR(0);
  if (wid == 0) {
    const bool sel = hs.act && ln >= 8u;
    if (sel) __hip_atomic_store(hs.base + hs.peer, (*(const LAS unsigned*)(xch + (ln & 7u) * 4u) & 0xFFFFu) | 0x10000u, RLX_AGENT);
    const unsigned fv = hs_wait_flags(hs, sel);
    if (sel) *(LAS unsigned*)(xch + 64u + (ln & 7u) * 4u) = fv;
  }
  BAR();
  if (g == 0) {
    u32x4 pv[8];
#pragma unroll
    for (int u = 0; u < 8; ++u) pv[u] = __builtin_amdgcn_raw_buffer_load_b128(srdP, hg * 4096u + w4 * 1024u + ln * 16u, (unsigned)u * 32768u, 18);
    const u32x4 f0 = *(const LAS u32x4*)(xch + 64u), f1 = *(const LAS u32x4*)(xch + 80u);
    float acc[8] = {};
#pragma unroll
    for (int u = 0; u < 8; ++u) {
      const unsigned e = ((u < 4 ? f0[u & 3] : f1[u & 3]) >> (8u * (w4 >> 1))) & 0xFFu;
      const float isc = __uint_as_float((e - 14u) << 23);
#pragma unroll
      for (int q = 0; q < 4; ++q) { const unsigned wv = pv[u][q]; const f16x2 hv = __builtin_bit_cast(f16x2, wv); acc[2 * q] = fmaf((float)hv[0], isc, acc[2 * q]); acc[2 * q + 1] = fmaf((float)hv[1], isc, acc[2 * q + 1]); }
    }
    const unsigned qrow = (hg & 3u) * 32u + (ln & 31u), n = j * 128u + ((hg >> 2) * 2u + (w4 >> 1)) * 32u + (w4 & 1u) * 16u + (ln >> 5) * 8u;
    *(u32x4*)(out + ((size_t)b * SQ + qrow) * DM + n) = (u32x4){cvtpk(acc[0], acc[1]), cvtpk(acc[2], acc[3]), cvtpk(acc[4], acc[5]), cvtpk(acc[6], acc[7])};
  }
}

extern "C" void kernel_launch(void* const* d_in, const int* in_sizes, int n_in, void* d_out, int out_size, void* d_ws, size_t ws_size, hipStream_t stream) {
  (void)in_sizes; (void)n_in; (void)out_size; (void)ws_size;
  (void)hipFuncSetAttribute((const void*)mha_fused, hipFuncAttributeMaxDynamicSharedMemorySize, (int)LDS_BYTES);
  hipLaunchKernelGGL(mha_fused, dim3(256), dim3(512), LDS_BYTES, stream, (const unsigned short*)d_in[0], (const unsigned short*)d_in[1], (const unsigned short*)d_in[2],
                     (const unsigned short*)d_in[3], (const unsigned short*)d_in[4], (unsigned short*)d_out, (char*)d_ws);
}
```

```cpp
#include <hip/hip_runtime.h>
#include <hip/hip_bf16.h>
#include <stdint.h>
#include <type_traits>

typedef __bf16 bf16x8 __attribute__((ext_vector_type(8)));
typedef __bf16 bf16x2 __attribute__((ext_vector_type(2)));
typedef _Float16 f16x2 __attribute__((ext_vector_type(2)));
typedef short s16x4 __attribute__((ext_vector_type(4)));
typedef float f32x16 __attribute__((ext_vector_type(16)));
typedef float f32x4 __attribute__((ext_vector_type(4)));
typedef float f32x2 __attribute__((ext_vector_type(2)));
typedef unsigned u32x4 __attribute__((ext_vector_type(4)));
typedef unsigned u32x2 __attribute__((ext_vector_type(2)));
#define LAS __attribute__((address_space(3)))
#define GAS __attribute__((address_space(1)))
#define DI __device__ __forceinline__
#define MFMA(a, b, c) __builtin_amdgcn_mfma_f32_32x32x16_bf16((a), (b), (c), 0, 0, 0)
#define RLX_AGENT __ATOMIC_RELAXED, __HIP_MEMORY_SCOPE_AGENT
#define PIN(x) asm volatile("" : "+v"(x))
#define SBAR() __builtin_amdgcn_sched_barrier(0)
#define PRIO(n) __builtin_amdgcn_s_setprio(n)

constexpr int SQ = 128, DM = 512, NH = 32, SK = 2048, DH = 128;
constexpr unsigned U = 16384u, SL = 32768u;
constexpr unsigned LDS_BYTES = 5 * SL;
#define CODEPF 17
#define PACE 114u
#define PACE0 470u
#define CASC 122u
#define CDEC 7u
#define CFLOOR 94u
#define PSLIM 8192.0f
constexpr float CSC = 0.088388347648318447f * 1.4426950408889634f;
constexpr size_t WS_FLAG = 0;
constexpr unsigned FB = 128;
constexpr size_t WS_O = 1 << 20;
constexpr size_t WS_P = WS_O + (8u << 20);

DI unsigned rfl(unsigned x) { return __builtin_amdgcn_readfirstlane(x); }
DI unsigned cvtpk(float lo, float hi) { f32x2 v = {lo, hi}; bf16x2 b = __builtin_convertvector(v, bf16x2); return __builtin_bit_cast(unsigned, b); }
DI bf16x8 pack8(const f32x16& a, int s) {
  u32x4 w = {cvtpk(a[8 * s], a[8 * s + 1]), cvtpk(a[8 * s + 2], a[8 * s + 3]), cvtpk(a[8 * s + 4], a[8 * s + 5]), cvtpk(a[8 * s + 6], a[8 * s + 7])};
  return __builtin_bit_cast(bf16x8, w);
}
DI bf16x8 lds_b128(unsigned a) { return *(const LAS bf16x8*)a; }
DI f32x4 lds_f4(unsigned a) { return *(const LAS f32x4*)a; }
DI void lds_st4(unsigned a, const f32x16& v, int q) { *(LAS f32x4*)a = (f32x4){v[4 * q], v[4 * q + 1], v[4 * q + 2], v[4 * q + 3]}; }
DI s16x4 lds_tr(unsigned a) { return __builtin_amdgcn_ds_read_tr16_b64_v4i16((LAS s16x4*)a); }
DI bf16x8 lds_tr2(unsigned a0, unsigned a1) {
  s16x4 lo = lds_tr(a0), hi = lds_tr(a1);
  return __builtin_bit_cast(bf16x8, __builtin_shufflevector(lo, hi, 0, 1, 2, 3, 4, 5, 6, 7));
}
DI void dma4_kv(unsigned m0v, unsigned voff, __amdgpu_buffer_rsrc_t srd, unsigned soff) {
  asm volatile("s_mov_b32 m0, %0\n\ts_nop 3\n\t"
               "buffer_load_dwordx4 %1, %2, %3 offen nt lds\n\t"
               "buffer_load_dwordx4 %1, %2, %3 offen offset:1024 nt lds\n\t"
               "buffer_load_dwordx4 %1, %2, %3 offen offset:2048 nt lds\n\t"
               "buffer_load_dwordx4 %1, %2, %3 offen offset:3072 nt lds"
               :: "s"(rfl(m0v)), "v"(voff), "s"(srd), "s"(rfl(soff)) : "memory");
}
DI void dma1(unsigned m0v, unsigned voff, __amdgpu_buffer_rsrc_t srd, unsigned soff) {
  asm volatile("s_mov_b32 m0, %0\n\ts_nop 3\n\t"
               "buffer_load_dwordx4 %1, %2, %3 offen lds"
               :: "s"(rfl(m0v)), "v"(voff), "s"(srd), "s"(rfl(soff)) : "memory");
}
DI void dma1nt(unsigned m0v, unsigned voff, __amdgpu_buffer_rsrc_t srd, unsigned soff) {
  asm volatile("s_mov_b32 m0, %0\n\ts_nop 3\n\t"
               "buffer_load_dwordx4 %1, %2, %3 offen nt lds"
               :: "s"(rfl(m0v)), "v"(voff), "s"(srd), "s"(rfl(soff)) : "memory");
}
#define WAIT_BAR(N) asm volatile("s_waitcnt vmcnt(" #N ") lgkmcnt(0)\n\ts_barrier" ::: "memory")
#define BAR() asm volatile("s_waitcnt lgkmcnt(0)\n\ts_barrier" ::: "memory")
#define WAITVM(N) asm volatile("s_waitcnt vmcnt(" #N ")" ::: "memory")

DI float xhalf_max(float v) { auto r = __builtin_amdgcn_permlane32_swap(__float_as_uint(v), __float_as_uint(v), false, false); return fmaxf(__uint_as_float(r[0]), __uint_as_float(r[1])); }
DI float xhalf_sum(float v) { auto r = __builtin_amdgcn_permlane32_swap(__float_as_uint(v), __float_as_uint(v), false, false); return __uint_as_float(r[0]) + __uint_as_float(r[1]); }

DI unsigned wave_umax(unsigned v) {
#define DPPMAX(ctrl, rmask) v = max(v, (unsigned)__builtin_amdgcn_update_dpp(0, (int)v, ctrl, rmask, 0xF, false))
  DPPMAX(0x111, 0xF); DPPMAX(0x112, 0xF); DPPMAX(0x114, 0xF); DPPMAX(0x118, 0xF);
  DPPMAX(0x142, 0xA); DPPMAX(0x143, 0xC);
#undef DPPMAX
  return (unsigned)__builtin_amdgcn_readlane((int)v, 63);
}
struct Hs { GAS unsigned* base; unsigned mine, peer; bool act; };
DI Hs hs_make(char* ws, unsigned bid, unsigned L) {
  Hs h; h.base = (GAS unsigned*)(ws + WS_FLAG);
  const bool h1 = L < 4u; h.act = h1 || (L >= 8u && L < 16u);
  const unsigned pb = h1 ? ((bid & ~24u) | (L << 3)) : ((bid & ~7u) | (L & 7u));
  h.mine = bid * FB + (L & 15u); h.peer = pb * FB + (h1 ? ((bid >> 3) & 3u) : 8u + (bid & 7u));
  return h;
}
DI void hs_echo(const Hs& h) { if (h.act) __hip_atomic_store(h.base + h.peer + 64u, __hip_atomic_load(h.base + h.mine + 32u, RLX_AGENT), RLX_AGENT); }
DI void hs_wait_armed(const Hs& h) {
#pragma nounroll
  for (unsigned spins = 0; spins < (1u << 20); ++spins) {
    hs_echo(h);
    bool ok = true;
    if (h.act) ok = __hip_atomic_load(h.base + h.mine + 64u, RLX_AGENT) == __hip_atomic_load(h.base + h.peer + 32u, RLX_AGENT);
    if (__all(ok)) break;
    __builtin_amdgcn_s_sleep(4);
  }
}
DI unsigned hs_wait_flags(const Hs& h, bool sel) {
  unsigned v = 1u;
#pragma nounroll
  for (unsigned spins = 0; spins < (1u << 20); ++spins) {
    if (sel) v = __hip_atomic_load(h.base + h.mine, RLX_AGENT);
    if (__all(v != 0u)) break;
    if ((spins & 15u) == 15u) hs_echo(h);
    __builtin_amdgcn_s_sleep(1);
  }
  asm volatile("" ::: "memory");
  return v;
}

__global__ __launch_bounds__(512, 2) void mha_fused(const unsigned short* __restrict__ x, const unsigned short* __restrict__ kc, const unsigned short* __restrict__ vc,
                                                    const unsigned short* __restrict__ wq, const unsigned short* __restrict__ wo, unsigned short* __restrict__ out, char* __restrict__ ws) {
  extern __shared__ __attribute__((aligned(16))) char lds[];
  const unsigned tid = threadIdx.x, lane = tid & 63u, r32 = lane & 31u, hi = lane >> 5;
  const unsigned wid = rfl(tid >> 6), g = wid >> 2, w4 = wid & 3u;
  const unsigned bid = blockIdx.x, hg = bid & 7u, b = bid >> 5, j = (bid >> 3) & 3u, h = hg * 4u + j;
  const unsigned lds0 = (unsigned)(uintptr_t)lds;
  const unsigned tref = (unsigned)__builtin_amdgcn_s_memrealtime() + PACE0 + (((bid >> 3) * PACE) >> 5);
  auto due = [&](unsigned t) { unsigned long long t_; asm volatile("s_memrealtime %0\n\ts_waitcnt lgkmcnt(0)" : "=s"(t_) :: "memory"); return (int)((unsigned)t_ - tref - t) >= 0; };
  auto tat = [](unsigned hx) { return hx * (PACE / 2u); };
  auto pace = [&](unsigned t) { while (!due(t)) __builtin_amdgcn_s_sleep(2); };
  asm volatile("" :: "s"(out), "s"(ws), "s"(wo));

  const __amdgpu_buffer_rsrc_t srdK = __builtin_amdgcn_make_buffer_rsrc((void*)(kc + (size_t)(b * NH + h) * SK * DH), (short)0, SK * DH * 2, 0x00020000);
  const __amdgpu_buffer_rsrc_t srdV = __builtin_amdgcn_make_buffer_rsrc((void*)(vc + (size_t)(b * NH + h) * SK * DH), (short)0, SK * DH * 2, 0x00020000);
  const __amdgpu_buffer_rsrc_t srdX = __builtin_amdgcn_make_buffer_rsrc((void*)(x + (size_t)b * SQ * DM), (short)0, SQ * DM * 2, 0x00020000);
  const __amdgpu_buffer_rsrc_t srdWq = __builtin_amdgcn_make_buffer_rsrc((void*)wq, (short)0, DM * NH * DH * 2, 0x00020000);
  const __amdgpu_buffer_rsrc_t srdWo = __builtin_amdgcn_make_buffer_rsrc((void*)(wo + (size_t)hg * 512 * DM), (short)0, 512 * DM * 2, 0x00020000);

  unsigned w8k = w4 * 8192u; asm volatile("" : "+s"(w8k));
  const unsigned l4 = lane >> 4, l15 = lane & 15u;
  const unsigned voffK = w4 * 4096u + l4 * 256u + ((l15 ^ (l4 | (w4 << 2))) << 4);
  const unsigned voffV = w4 * 4096u + l4 * 256u + ((l15 ^ (l4 << 2)) << 4);
  const unsigned voffWq = l4 * 8192u + ((l15 ^ (l4 << 2)) << 4);
  const unsigned voffWo = l4 * 1024u + ((l15 ^ (l4 << 2)) << 4);
  const unsigned voffX = (lane >> 3) * 1024u + (((lane & 7u) ^ (((lane >> 4) & 1u) | (w4 << 1))) << 4);

  auto issueK = [&](unsigned t, unsigned off) { dma4_kv(lds0 + off + w4 * 4096u, voffK, srdK, t * U); };
  auto issueV = [&](unsigned t, unsigned off) { dma4_kv(lds0 + off + w4 * 4096u, voffV, srdV, t * U); };
  auto hpos = [&](unsigned cc) { const unsigned r = cc - 1u + b % 3u; return rfl(cc == 0u ? j : (j + 1u + ((0x124u >> ((2u * r) & 15u)) & 3u)) & 3u); };
  auto issueWo = [&](unsigned cc, unsigned slot) {
    const unsigned c = hpos(cc);
#pragma unroll
    for (int q = 0; q < 8; ++q) dma1(lds0 + slot * SL + w8k + q * 1024u, voffWo, srdWo, (c * 128u + w4 * 32u + q * 4u) * 1024u + j * 256u);
  };
  const unsigned rot = (b + 2u * j) & 7u;
  auto qslot = [](int c) { return (c % 3 == 0) ? 3u : (c % 3 == 1) ? 4u : 2u; };
  auto issueQP = [&](int c) {
    const unsigned kcn = ((unsigned)c + rot) & 7u, sb = lds0 + qslot(c) * SL;
#pragma unroll
    for (int q = 0; q < 4; ++q) {
      dma1nt(sb + (w4 * 4u + q) * 1024u, voffWq, srdWq, (kcn * 64u + w4 * 16u + q * 4u) * 8192u + h * 256u);
      dma1(sb + U + (q * 4u + w4) * 1024u, voffX, srdX, (q * 4u + w4) * 8192u + kcn * 128u);
    }
  };
  const unsigned blk = (lane >> 4) & 1u, q4 = l15 >> 2, p4 = lane & 3u, pp = ((p4 & 1u) << 1) | (p4 >> 1);
  const unsigned trV = lds0 + U + (4u * hi + q4) * 256u + ((q4 ^ (2u * g)) << 6) + blk * 32u + pp * 8u;
  const unsigned rdK = lds0 + ((r32 & 7u) + 16u * (r32 >> 3)) * 256u + ((hi ^ ((r32 & 3u) | ((r32 >> 3) << 2))) << 4);
  const unsigned qx = lds0 + 3u * SL + w4 * 4096u + lane * 16u;

  bf16x8 qf[8], kf[16];
  auto rdk = [&](unsigned off, int i) { return lds_b128(((rdK + off) ^ (unsigned)((i & 7) << 5)) + (i >> 3) * 2048); };
  if (g == 0) {
    const unsigned trW = lds0 + (8u * hi + q4) * 256u + (q4 << 6) + blk * 32u + pp * 8u;
    const unsigned rdX = lds0 + U + (w4 * 32u + r32) * 128u + ((hi ^ (((r32 >> 1) & 1u) | ((r32 >> 3) << 1))) << 4);
    f32x16 qa[4] = {};
    bf16x8 xb[2][4], wa_[2][16];
    auto rdq = [&](int c, int s) {
      const unsigned u0 = qslot(c) * SL;
      xb[c & 1][s] = lds_b128((rdX + u0) ^ (unsigned)(s << 5));
#pragma unroll
      for (int d = 0; d < 4; ++d) { const unsigned a = ((trW + u0) ^ (unsigned)(d << 6)) + s * 4096; wa_[c & 1][4 * s + d] = lds_tr2(a, a + 1024); }
    };
    {
      unsigned long long pc; asm volatile("s_getpc_b64 %0" : "=s"(pc));
      const __amdgpu_buffer_rsrc_t srdC = __builtin_amdgcn_make_buffer_rsrc((void*)(pc & ~255ull), (short)0, CODEPF * 1024, 0x00020000);
      if (wid == 1) dma1(lds0, lane * 16u, srdC, ((bid >> 3) % CODEPF) * 1024u);
    }
    unsigned r0;
    { const Hs hs = hs_make(ws, bid, lane);
      if (wid == 0 && hs.act) __hip_atomic_store(hs.base + hs.mine, 0u, RLX_AGENT);
      asm volatile("global_load_dword %0, %1, off offset:256 sc1" : "=v"(r0) : "v"(hs.base + hs.mine) : "memory"); }
    BAR();
#pragma unroll
    for (int s = 0; s < 4; ++s) rdq(0, s);
#pragma unroll
    for (int c = 0; c < 8; ++c) {
      if (c == 0) {
        asm volatile("s_waitcnt vmcnt(0) lgkmcnt(0)\n\ts_barrier" : "+v"(r0) :: "memory");
        const Hs hs = hs_make(ws, bid, lane);
        if (wid == 0 && hs.act) __hip_atomic_store(hs.base + hs.peer + 32u, r0 + 1u, RLX_AGENT);
      } else if (c < 7) BAR();
#pragma unroll
      for (int s = 0; s < 4; ++s)
#pragma unroll
        for (int d = 0; d < 4; ++d) {
          SBAR();
          qa[d] = MFMA(wa_[c & 1][4 * s + d], xb[c & 1][s], qa[d]);
          SBAR();
          if (d & 1) { asm volatile("s_nop 2"); if (d == 3) __builtin_amdgcn_s_sleep(1); SBAR(); }
          if (c < 7) {
            const unsigned u0 = qslot(c + 1) * SL, a = ((trW + u0) ^ (unsigned)(d << 6)) + s * 4096;
            if (d == 0) xb[(c + 1) & 1][s] = lds_b128((rdX + u0) ^ (unsigned)(s << 5));
            wa_[(c + 1) & 1][4 * s + d] = lds_tr2(a, a + 1024);
          }
        }
      SBAR();
    }
#pragma unroll
    for (int d = 0; d < 4; ++d) {
#pragma unroll
      for (int r = 0; r < 16; ++r) qa[d][r] *= CSC;
      qf[2 * d] = pack8(qa[d], 0); qf[2 * d + 1] = pack8(qa[d], 1);
    }
#pragma unroll
    for (int f = 0; f < 8; ++f) *(LAS bf16x8*)(qx + (f >> 2) * U + (f & 3) * 1024) = qf[f];
    BAR();
#pragma unroll
    for (int i = 0; i < 16; ++i) kf[i] = rdk(U, i);
  } else {
    issueQP(0); issueQP(1);
    WAIT_BAR(8); issueQP(2);
#pragma unroll
    for (int c = 0; c < 5; ++c) { WAIT_BAR(8); issueQP(c + 3); }
    issueK(0, U);
    WAIT_BAR(12);
    issueK(1, 0);
    WAIT_BAR(8);
    pace(tat(1)); issueK(2, SL); pace(tat(2)); issueV(0, SL + U);
    WAIT_BAR(8);
    const Hs hs = hs_make(ws, bid, lane);
    unsigned rq;
    asm volatile("global_load_dword %0, %1, off offset:128 sc1" : "=v"(rq) : "v"(hs.base + hs.mine) : "memory");
#pragma unroll
    for (int f = 0; f < 8; ++f) qf[f] = lds_b128(qx + (f >> 2) * U + (f & 3) * 1024);
#pragma unroll
    for (int i = 0; i < 16; ++i) kf[i] = rdk(0, i);
    pace(tat(3)); issueK(3, 2u * SL); pace(tat(4)); issueV(1, 2u * SL + U);
    pace(tat(5));
    issueK(4, 3u * SL);
    pace(tat(6));
    issueV(2, 3u * SL + U);
    asm volatile("s_waitcnt vmcnt(16) lgkmcnt(0)\n\ts_barrier" : "+v"(rq) :: "memory");
    if (wid == 4 && hs.act) __hip_atomic_store(hs.base + hs.peer + 64u, rq, RLX_AGENT);
  }

  f32x16 o[4] = {};
  float m_run = 0.f, l_run = 0.f;
  f32x16 negm = {};
  bf16x8 pf[4], vf[16];
  auto vfrag = [&](unsigned av, int ks, int d) { const unsigned a = (av ^ (unsigned)(d << 6)) + (32 * (ks & 1) + 8 * (ks >> 1)) * 256; return lds_tr2(a, a + 4096); };
  bool vpend = false; unsigned vdst = 0u, vsrc = 0u;
  auto scores_slow = [&](bool first, float& ps) {
    f32x16 s0 = {}, s1 = {};
#pragma unroll
    for (int s = 0; s < 8; ++s) { s0 = MFMA(kf[s], qf[s], s0); s1 = MFMA(kf[8 + s], qf[s], s1); }
    float mx = fmaxf(s0[0], s1[0]);
#pragma unroll
    for (int r = 1; r < 16; ++r) mx = fmaxf(fmaxf(mx, s0[r]), s1[r]);
    mx = xhalf_max(mx);
    const float m_new = first ? mx : fmaxf(mx, m_run);
    if (!first) {
      const float alpha = __builtin_amdgcn_exp2f(m_run - m_new);
      l_run *= alpha;
#pragma unroll
      for (int d = 0; d < 4; ++d)
#pragma unroll
        for (int r = 0; r < 16; ++r) o[d][r] *= alpha;
    }
    m_run = m_new;
    ps = 0.f;
#pragma unroll
    for (int r = 0; r < 16; ++r) { negm[r] = -m_new; s0[r] = __builtin_amdgcn_exp2f(s0[r] - m_new); s1[r] = __builtin_amdgcn_exp2f(s1[r] - m_new); ps += s0[r] + s1[r]; }
    pf[0] = pack8(s0, 0); pf[1] = pack8(s0, 1); pf[2] = pack8(s1, 0); pf[3] = pack8(s1, 1);
  };
  auto scores = [&](bool first, auto inloop) {
    float ps = 0.f;
    PRIO(2);
    if (!first) {
      SBAR();
      f32x16 s0 = MFMA(kf[0], qf[0], negm);
      SBAR();
#pragma unroll
      for (int s = 1; s < 8; ++s) {
        s0 = MFMA(kf[s], qf[s], s0); SBAR();
        if (decltype(inloop)::value && (s & 1)) { PIN(s0); if (vpend) dma1nt(vdst + (s >> 1) * 1024u, voffV, srdV, vsrc + (s >> 1) * 1024u); SBAR(); }
      }
      SBAR();
      f32x16 s1;
      asm volatile("v_mfma_f32_32x32x16_bf16 %0, %1, %2, %3" : "=&v"(s1) : "v"(kf[8]), "v"(qf[0]), "v"(negm));
      SBAR();
      float pa = 0.f, pb = 0.f;
      unsigned w[16];
#define EXP2(S, e) do { S[e] = __builtin_amdgcn_exp2f(S[e]); S[e + 1] = __builtin_amdgcn_exp2f(S[e + 1]); float t0_ = S[e], t1_ = S[e + 1]; PIN(t0_); PIN(t1_); S[e] = t0_; S[e + 1] = t1_; SBAR(); } while (0)
#define FIN2(S, e, wi) do { pa += S[e]; pb += S[e + 1]; w[wi] = cvtpk(S[e], S[e + 1]); PIN(pa); PIN(pb); PIN(w[wi]); SBAR(); } while (0)
#pragma unroll
      for (int s = 1; s < 8; ++s) {
        s1 = MFMA(kf[8 + s], qf[s], s1); SBAR();
        EXP2(s0, 2 * (s - 1));
        if (s > 1) FIN2(s0, 2 * (s - 2), s - 2);
        if (s == 3) asm volatile("" :: "v"(negm));
      }
      EXP2(s0, 14); FIN2(s0, 12, 6);
      EXP2(s1, 0); FIN2(s0, 14, 7);
#pragma unroll
      for (int i = 1; i < 8; ++i) { EXP2(s1, 2 * i); FIN2(s1, 2 * (i - 1), 7 + i); }
      FIN2(s1, 14, 15);
#undef EXP2
#undef FIN2
      ps = pa + pb;
#pragma unroll
      for (int i = 0; i < 4; ++i) pf[i] = __builtin_bit_cast(bf16x8, (u32x4){w[4 * i], w[4 * i + 1], w[4 * i + 2], w[4 * i + 3]});
    }
    if (__builtin_expect(first || __any(!(ps <= PSLIM)), 0)) scores_slow(first, ps);
    l_run += ps;
    PIN(pf[0]); PIN(pf[1]); PIN(pf[2]); PIN(pf[3]); PIN(l_run); if (decltype(inloop)::value) PIN(negm);
    PRIO(0);
  };

  __builtin_amdgcn_s_waitcnt(0xC07F);
  unsigned tprev = tref, imin = 0u;
  unsigned sp = 1u + g;
  const unsigned p0 = 3u + ((((bid >> 3) * 11u) & 31u) * 3u >> 2);
  for (int it = 0; it < 15; ++it) {
    const unsigned p = 2u * (unsigned)it + g + 1u;
    scores(it == 0, std::true_type{});
    unsigned long long ts;
    asm volatile("s_waitcnt vmcnt(0) lgkmcnt(0)\n\ts_barrier\n\ts_memrealtime %0" : "=s"(ts) :: "memory");
    const unsigned av = trV + sp * SL;
#pragma unroll
    for (int i = 0; i < 16; ++i) vf[i] = vfrag(av, i >> 2, i & 3);
    SBAR();
    SBAR();
#pragma unroll
    for (int i = 0; i < 8; ++i) kf[i] = rdk(sp * SL, i);
    SBAR();
    const unsigned sn = sp >= 3u ? sp - 3u : sp + 2u;
    const bool go = p != p0;
    const unsigned ni = p + (p < p0 ? 3u : 2u), ns = (p < p0 ? (sn == 4u ? 0u : sn + 1u) : sn) * SL;
    const unsigned tni = tat(ni + min(ni, 30u));
    bool kgo = go && ni <= 30u;
    SBAR();
#pragma unroll
    for (int i = 0; i < 2; ++i) { o[i & 3] = MFMA(vf[i], pf[i >> 2], o[i & 3]); SBAR(); }
    asm volatile("s_waitcnt lgkmcnt(0)" : "+s"(ts) :: "memory");
    const int late = (int)((unsigned)ts - tref - tni);
    const bool vend = ni >= 31u;
    if (kgo && late + (int)(PACE / 2u) + 15 >= 0) { issueK(ni + 1u, ns); kgo = false; }
    if (vend) issueV(ni - 1u, ns + U);
    SBAR();
#pragma unroll
    for (int i = 2; i < 8; ++i) { o[i & 3] = MFMA(vf[i], pf[i >> 2], o[i & 3]); SBAR(); }
#pragma unroll
    for (int i = 8; i < 16; ++i) { o[i & 3] = MFMA(vf[i], pf[i >> 2], o[i & 3]); if (i < 12) { kf[2 * i - 8] = rdk(sp * SL, 2 * i - 8); kf[2 * i - 7] = rdk(sp * SL, 2 * i - 7); } SBAR(); }
    if (kgo) { pace(tni - PACE / 2u); issueK(ni + 1u, ns); }
    if (go && !vend && late + 35 < 0) pace(tni);
    if (late > 150) {
      imin = ((unsigned)ts - tprev > 335u && it > 0) ? CASC : imin > CFLOOR + CDEC ? imin - CDEC : min(imin, CFLOOR);
      if (!vend && imin != 0u) pace((unsigned)ts - tref + imin);
    }
    tprev = (unsigned)ts;
    vpend = go && !vend; vdst = lds0 + ns + U + w4 * 4096u; vsrc = (ni - 1u) * U;
    if (it == 14) issueWo(g, 3u + g);
    sp = sn;
    __builtin_amdgcn_s_waitcnt(0xC07F);
    asm volatile("s_barrier" ::: "memory");
  }
  scores(false, std::false_type{});
  unsigned ln; asm volatile("v_mbcnt_lo_u32_b32 %0, -1, 0\n\tv_mbcnt_hi_u32_b32 %0, -1, %0" : "=v"(ln));
  const unsigned x0 = lds0 + w8k + ln * 16u;
  l_run = xhalf_sum(l_run);
  const unsigned stats = lds0 + 2u * SL + (ln & 31u) * 8u;
  if (ln < 32u) *(LAS f32x2*)(stats + wid * 256u) = (f32x2){m_run, l_run};
  const Hs hs = hs_make(ws, bid, ln);
  unsigned hs_r, hs_q, hs_qi, hs_ro;
  asm volatile("global_load_dword %0, %4, off offset:256 sc1\n\tglobal_load_dword %1, %5, off offset:128 sc1\n\t"
               "global_load_dword %2, %4, off offset:128 sc1\n\tglobal_load_dword %3, %5, off offset:256 sc1"
               : "=&v"(hs_r), "=&v"(hs_q), "=&v"(hs_qi), "=&v"(hs_ro) : "v"(hs.base + hs.mine), "v"(hs.base + hs.peer) : "memory");
  bf16x8 of0[4];
  {
    WAIT_BAR(12);
    const unsigned av = trV + (1u + g) * SL;
#pragma unroll
    for (int ks = 0; ks < 4; ++ks) { vf[4 * ks + 2] = vfrag(av, ks, 2); vf[4 * ks + 3] = vfrag(av, ks, 3); }
    SBAR();
#pragma unroll
    for (int ks = 0; ks < 4; ++ks) { vf[4 * ks] = vfrag(av, ks, 0); vf[4 * ks + 1] = vfrag(av, ks, 1); }
    SBAR();
#pragma unroll
    for (int ks = 0; ks < 4; ++ks) { o[2] = MFMA(vf[4 * ks + 2], pf[ks], o[2]); o[3] = MFMA(vf[4 * ks + 3], pf[ks], o[3]); }
    SBAR();
#pragma unroll
    for (int ks = 0; ks < 2; ++ks) { o[0] = MFMA(vf[4 * ks], pf[ks], o[0]); o[1] = MFMA(vf[4 * ks + 1], pf[ks], o[1]); }
    SBAR();
    const unsigned xo = x0 + g * SL, xi = x0 + (1u - g) * SL;
#pragma unroll
    for (int q = 0; q < 4; ++q) { lds_st4(xo + q * 1024, o[2], q); lds_st4(xo + 4096 + q * 1024, o[3], q); }
    SBAR();
#pragma unroll
    for (int ks = 2; ks < 4; ++ks) { o[0] = MFMA(vf[4 * ks], pf[ks], o[0]); o[1] = MFMA(vf[4 * ks + 1], pf[ks], o[1]); }
    SBAR();
    if (g == 0) BAR();
    const f32x2 st = *(const LAS f32x2*)(stats + (wid ^ 4u) * 256u);
    const float ms = fmaxf(m_run, st[0]);
    const float e_me = __builtin_amdgcn_exp2f(m_run - ms), e_ot = __builtin_amdgcn_exp2f(st[0] - ms);
    const float inv = 1.f / (l_run * e_me + st[1] * e_ot);
    float a_me = e_me * inv, a_ot = e_ot * inv;
    PIN(a_me); PIN(a_ot);
    asm volatile("s_waitcnt vmcnt(0) lgkmcnt(0)\n\ts_barrier" : "+v"(hs_r), "+v"(hs_q), "+v"(hs_qi), "+v"(hs_ro) :: "memory");
    f32x4 w0[4], w1[4];
#pragma unroll
    for (int q = 0; q < 4; ++q) { w0[q] = lds_f4(xi + q * 1024); w1[q] = lds_f4(xi + 4096 + q * 1024); }
#pragma unroll
    for (int q = 0; q < 4; ++q)
#pragma unroll
      for (int e = 0; e < 4; ++e) { o[0][4 * q + e] = fmaf(w0[q][e], a_ot, o[0][4 * q + e] * a_me); o[1][4 * q + e] = fmaf(w1[q][e], a_ot, o[1][4 * q + e] * a_me); }
    of0[0] = pack8(o[0], 0); of0[1] = pack8(o[0], 1); of0[2] = pack8(o[1], 0); of0[3] = pack8(o[1], 1);
  }
  PRIO(0);
  const __amdgpu_buffer_rsrc_t srdO = __builtin_amdgcn_make_buffer_rsrc((void*)(ws + WS_O + (size_t)(hg * 8u + b) * 131072u), (short)0, 4 * 32768, 0x00020000);
#pragma unroll
  for (int s = 0; s < 4; ++s) __builtin_amdgcn_raw_buffer_store_b128(__builtin_bit_cast(u32x4, of0[s]), srdO, j * 32768u + wid * 4096u + (unsigned)s * 1024u + ln * 16u, 0, 16);
  f32x16 c[4] = {};
  const unsigned trW2 = trV - U + (ln >> 5) * 1024u + g * U;
  auto wfrag = [&](unsigned slot, int s, int nb) { const unsigned a = ((trW2 + slot * SL) ^ (unsigned)(nb << 6)) + s * 4096; return lds_tr2(a, a + 1024); };
  bf16x8 w1[16], w2[16];
#pragma unroll
  for (int i = 0; i < 16; ++i) w2[i] = wfrag(3, i >> 2, i & 3);
  SBAR();
#pragma unroll
  for (int i = 0; i < 8; ++i) { c[i & 3] = MFMA(w2[i], of0[i >> 2], c[i & 3]); w1[i] = wfrag(4, i >> 2, i & 3); SBAR(); }
  PIN(c[0]); PIN(c[1]); PIN(c[2]); PIN(c[3]);
  WAIT_BAR(0);
  if (wid == 0) {
    if (hs.act && hs_qi != hs_ro) __hip_atomic_store(hs.base + hs.peer + 64u, hs_qi, RLX_AGENT);
    if (!__all(hs_r == hs_q || !hs.act)) hs_wait_armed(hs);
    if (ln < 4u) __hip_atomic_store(hs.base + hs.peer, 1u, RLX_AGENT);
  }
  issueWo(2u + g, 1u - g);
  SBAR();
#pragma unroll
  for (int i = 8; i < 16; ++i) { c[i & 3] = MFMA(w2[i], of0[i >> 2], c[i & 3]); w1[i] = wfrag(4, i >> 2, i & 3); SBAR(); }
  PIN(c[0]); PIN(c[1]); PIN(c[2]); PIN(c[3]);
  if (wid == 0) hs_wait_flags(hs, ln < 4u);
  WAIT_BAR(0);
  const unsigned xch = lds0 + 4u * SL;
  const __amdgpu_buffer_rsrc_t srdP = __builtin_amdgcn_make_buffer_rsrc((void*)(ws + WS_P + (size_t)(b * 4u + j) * 262144u), (short)0, 262144, 0x00020000);
  {
    bf16x8 of[12];
#pragma unroll
    for (int i = 0; i < 12; ++i)
      of[i] = __builtin_bit_cast(bf16x8, __builtin_amdgcn_raw_buffer_load_b128(srdO, wid * 4096u + (unsigned)(i & 3) * 1024u + ln * 16u, hpos(1u + (unsigned)(i >> 2)) * 32768u, 16));
    SBAR();
#pragma unroll
    for (int i = 0; i < 8; ++i) w2[i] = wfrag(1, i >> 2, i & 3);
    SBAR();
#pragma unroll
    for (int i = 0; i < 16; ++i) { c[i & 3] = MFMA(w1[i], of[i >> 2], c[i & 3]); if (i >= 8) w2[i] = wfrag(1, i >> 2, i & 3); SBAR(); }
#pragma unroll
    for (int i = 0; i < 16; ++i) { c[i & 3] = MFMA(w2[i], of[4 + (i >> 2)], c[i & 3]); w1[i] = wfrag(0, (i >> 1) & 3, (i & 1) + (i < 8 ? 2 : 0)); SBAR(); }
#pragma unroll
    for (int i = 0; i < 8; ++i) c[2 + (i & 1)] = MFMA(w1[i], of[8 + (i >> 1)], c[2 + (i & 1)]);
    SBAR();
#pragma unroll
    for (int i = 0; i < 4; ++i) c[0] = MFMA(w1[8 + 2 * i], of[8 + i], c[0]);
    SBAR();
#pragma unroll
    for (int q = 0; q < 4; ++q) { lds_st4(x0 + (2u + g) * SL + q * 1024, c[2], q); lds_st4(x0 + (2u + g) * SL + 4096 + q * 1024, c[3], q); }
    SBAR();
#pragma unroll
    for (int i = 0; i < 4; ++i) c[1] = MFMA(w1[9 + 2 * i], of[8 + i], c[1]);
    PIN(c[0]); PIN(c[1]);
    BAR();
    const unsigned ra = x0 + (3u - g) * SL;
    f32x4 va[8];
#pragma unroll
    for (int q = 0; q < 8; ++q) va[q] = lds_f4(ra + q * 1024);
    SBAR();
    float mx = 0.f;
#pragma unroll
    for (int nb = 0; nb < 2; ++nb)
#pragma unroll
      for (int r = 0; r < 16; ++r) { c[nb][r] += va[4 * nb + (r >> 2)][r & 3]; mx = fmaxf(mx, fabsf(c[nb][r])); }
    const unsigned e = max(wave_umax(__float_as_uint(mx)) >> 23, 15u);
    const float sc = __uint_as_float((268u - e) << 23);
#pragma unroll
    for (int s = 0; s < 4; ++s) {
      u32x4 w;
#pragma unroll
      for (int q = 0; q < 4; ++q) { f32x2 v = {c[s >> 1][8 * (s & 1) + 2 * q] * sc, c[s >> 1][8 * (s & 1) + 2 * q + 1] * sc}; w[q] = __builtin_bit_cast(unsigned, __builtin_convertvector(v, f16x2)); }
      __builtin_amdgcn_raw_buffer_store_b128(w, srdP, hg * 32768u + wid * 4096u + (unsigned)s * 1024u + ln * 16u, 0, 16);
    }
    if (ln == 0) *(LAS unsigned*)(xch + wid * 4u) = e;
  }
  WAIT_BAR(0);
  if (wid == 0) {
    const bool sel = hs.act && ln >= 8u;
    if (sel) __hip_atomic_store(hs.base + hs.peer, *(const LAS unsigned*)(xch + (ln & 7u) * 4u), RLX_AGENT);
    const unsigned fv = hs_wait_flags(hs, sel);
    if (sel) *(LAS unsigned*)(xch + 64u + (ln & 7u) * 4u) = fv;
  }
  BAR();
  if (g == 0) {
    u32x4 pv[8];
#pragma unroll
    for (int u = 0; u < 8; ++u) pv[u] = __builtin_amdgcn_raw_buffer_load_b128(srdP, hg * 4096u + w4 * 1024u + ln * 16u, (unsigned)u * 32768u, 18);
    const u32x4 f0 = *(const LAS u32x4*)(xch + 64u), f1 = *(const LAS u32x4*)(xch + 80u);
    float acc[8] = {};
#pragma unroll
    for (int u = 0; u < 8; ++u) {
      const unsigned e = u < 4 ? f0[u & 3] : f1[u & 3];
      const float isc = __uint_as_float((e - 14u) << 23);
#pragma unroll
      for (int q = 0; q < 4; ++q) { const unsigned wv = pv[u][q]; const f16x2 hv = __builtin_bit_cast(f16x2, wv); acc[2 * q] = fmaf((float)hv[0], isc, acc[2 * q]); acc[2 * q + 1] = fmaf((float)hv[1], isc, acc[2 * q + 1]); }
    }
    const unsigned qrow = (hg & 3u) * 32u + (ln & 31u), n = j * 128u + ((hg >> 2) * 2u + (w4 >> 1)) * 32u + (w4 & 1u) * 16u + (ln >> 5) * 8u;
    *(u32x4*)(out + ((size_t)b * SQ + qrow) * DM + n) = (u32x4){cvtpk(acc[0], acc[1]), cvtpk(acc[2], acc[3]), cvtpk(acc[4], acc[5]), cvtpk(acc[6], acc[7])};
  }
}

extern "C" void kernel_launch(void* const* d_in, const int* in_sizes, int n_in, void* d_out, int out_size, void* d_ws, size_t ws_size, hipStream_t stream) {
  (void)in_sizes; (void)n_in; (void)out_size; (void)ws_size;
  (void)hipFuncSetAttribute((const void*)mha_fused, hipFuncAttributeMaxDynamicSharedMemorySize, (int)LDS_BYTES);
  hipLaunchKernelGGL(mha_fused, dim3(256), dim3(512), LDS_BYTES, stream, (const unsigned short*)d_in[0], (const unsigned short*)d_in[1], (const unsigned short*)d_in[2],
                     (const unsigned short*)d_in[3], (const unsigned short*)d_in[4], (unsigned short*)d_out, (char*)d_ws);
}
```
